```python
import torch

def _softmax(x, axis=-1):
    return torch.softmax(x, dim=axis)
def _gelu(x):
    return torch.nn.functional.gelu(x, approximate="tanh")
def _silu(x):
    return torch.nn.functional.silu(x)
def _sigmoid(x):
    return torch.sigmoid(x)
def _layernorm(x, axis=-1, eps=1e-5):
    m = x.mean(dim=axis, keepdim=True)
    v = x.var(dim=axis, keepdim=True, unbiased=False)
    return (x - m) / torch.sqrt(v + eps)
def _scatter_add(tgt, idx, src, axis=0):
    return tgt.clone().index_add_(axis, idx, src)
def _segment_sum(src, seg, n):
    out = torch.zeros((n,) + src.shape[1:], dtype=src.dtype)
    return out.index_add_(0, seg, src)
def _pad(x, pw):
    flat = tuple(v for pair in reversed(pw) for v in pair)
    return torch.nn.functional.pad(x, flat)

kernel_name = 'parallel_sum__linear_attn_block__glu_gated_swiglu__prompt'

def setup_inputs():

    _w = (lambda a: (lambda t: t.to(torch.bfloat16) if t.is_floating_point() else t)(torch.as_tensor(a)))
    def _A_setup():
        B, S, D = 1, 2048, 576
        return {
            "x": _w((lambda *s: torch.randn(*s, dtype=torch.bfloat16) * 0.05)(B, S, D)),
            "wqkv": _w((lambda *s: torch.randn(*s, dtype=torch.bfloat16) * 0.05)(D, 3 * D)),
            "wo": _w((lambda *s: torch.randn(*s, dtype=torch.bfloat16) * 0.05)(D, D)),
        }
    _A_in = _A_setup()

    def _B_setup():
        B, S, D, F = 1, 2048, 576, 4608
        return {
            "x": _w((lambda *s: torch.randn(*s, dtype=torch.bfloat16) * 0.05)(B, S, D)),
            "w1": _w((lambda *s: torch.randn(*s, dtype=torch.bfloat16) * 0.2)(D, F) * (2.0 / D) ** 0.5),
            "w3": _w((lambda *s: torch.randn(*s, dtype=torch.bfloat16) * 0.2)(D, F) * (2.0 / D) ** 0.5),
            "w2": _w((lambda *s: torch.randn(*s, dtype=torch.bfloat16) * 0.2)(F, D) * (2.0 / F) ** 0.5),
        }
    _B_in = _B_setup()

    return {
        "A_x": _A_in["x"],
        "A_wqkv": _A_in["wqkv"],
        "A_wo": _A_in["wo"],
        "B_x": _B_in["x"],
        "B_w1": _B_in["w1"],
        "B_w3": _B_in["w3"],
        "B_w2": _B_in["w2"],
    }

def reference(A_x, A_wqkv, A_wo, B_x, B_w1, B_w3, B_w2):
    def _A_ref(x, wqkv, wo):
        B, S, D = x.shape
        H = 32; Dh = D // H
        qkv = (lambda x, *d: x.permute(*d))((x @ wqkv).reshape(B, S, 3, H, Dh), 2, 0, 3, 1, 4)
        q, k, v = qkv[0], qkv[1], qkv[2]
        phi = lambda t: torch.where(t > 0, t + 1, torch.exp(t)) + 1e-6
        pq, pk = phi(q), phi(k)
        kv = torch.einsum("bhsd,bhse->bhde", pk, v)
        z = torch.einsum("bhsd->bhd", pk)
        num = torch.einsum("bhqd,bhde->bhqe", pq, kv)
        den = torch.einsum("bhqd,bhd->bhq", pq, z)[..., None] + 1e-6
        o = num / den
        return (lambda x, *d: x.permute(*d))(o, 0, 2, 1, 3).reshape(B, S, D) @ wo

    def _B_ref(x, w1, w3, w2):
        gate = torch.einsum("bsd,df->bsf", x, w1)
        up = torch.einsum("bsd,df->bsf", x, w3)
        h = _silu(gate) * up
        out = torch.einsum("bsf,fd->bsd", h, w2)
        return out

    x = A_x
    xn = _layernorm(x, axis=-1)
    return x + _A_ref(xn, A_wqkv, A_wo) + _B_ref(xn, B_w1, B_w3, B_w2)
```

```cpp
#include <hip/hip_runtime.h>
#include <hip/hip_bf16.h>
typedef unsigned short u16;
__device__ __forceinline__ float bf2f(u16 v){ return __uint_as_float(((unsigned)v)<<16); }
__device__ __forceinline__ u16 f2bf(float x){ unsigned u=__float_as_uint(x); u += 0x7fffu + ((u>>16)&1u); return (u16)(u>>16); }
constexpr int S=2048, D=576, F=4608, H=32, DH=18;

__global__ void k_ln(const u16* x, float* xn){
  int s=blockIdx.x; __shared__ float red[2][64];
  float a=0,b=0; for(int i=threadIdx.x;i<D;i+=64){float v=bf2f(x[s*D+i]); a+=v;}
  red[0][threadIdx.x]=a; __syncthreads(); float m=0; for(int i=0;i<64;i++) m+=red[0][i]; m/=D;
  for(int i=threadIdx.x;i<D;i+=64){float v=bf2f(x[s*D+i])-m; b+=v*v;}
  red[1][threadIdx.x]=b; __syncthreads(); float var=0; for(int i=0;i<64;i++) var+=red[1][i]; var/=D;
  float r=rsqrtf(var+1e-5f);
  for(int i=threadIdx.x;i<D;i+=64) xn[s*D+i]=bf2f(f2bf((bf2f(x[s*D+i])-m)*r));
}
__global__ void k_mm(const float* A, const u16* B, float* C, int N, int K){
  int n=blockIdx.x*256+threadIdx.x; int m0=blockIdx.y*8; if(n>=N) return;
  float acc[8]={0,0,0,0,0,0,0,0};
  for(int k=0;k<K;k++){ float b=bf2f(B[(size_t)k*N+n]); for(int j=0;j<8;j++) acc[j]+=A[(size_t)(m0+j)*K+k]*b; }
  for(int j=0;j<8;j++) C[(size_t)(m0+j)*N+n]=acc[j];
}
__device__ float phi(float t){ return (t>0? t+1.f: __expf(t))+1e-6f; }
__global__ void k_kv(const float* qkv, float* kv){
  int h=blockIdx.x; int d=threadIdx.x/19, e=threadIdx.x%19; if(d>=DH) return;
  float a=0; for(int s=0;s<S;s++){ float pk=bf2f(f2bf(phi(qkv[(size_t)s*3*D+D+h*DH+d]))); float v= e<DH? bf2f(f2bf(qkv[(size_t)s*3*D+2*D+h*DH+e])):1.f; a+=pk*v; }
  kv[(h*DH+d)*19+e]=a;
}
__global__ void k_o(const float* qkv, const float* kv, float* o){
  int i=blockIdx.x*256+threadIdx.x; int s=i/D, c=i%D, h=c/DH, e=c%DH;
  float num=0, den=0; for(int d=0;d<DH;d++){ float pq=bf2f(f2bf(phi(qkv[(size_t)s*3*D+h*DH+d]))); num+=pq*kv[(h*DH+d)*19+e]; den+=pq*kv[(h*DH+d)*19+18]; }
  o[i]=bf2f(f2bf(num/(den+1e-6f)));
}
__global__ void k_glu(float* g, const float* u){ size_t i=(size_t)blockIdx.x*256+threadIdx.x; float a=g[i]; g[i]=bf2f(f2bf(a/(1.f+__expf(-a))*u[i])); }
__global__ void k_fin(const u16* x, const float* a, const float* b, u16* out){ int i=blockIdx.x*256+threadIdx.x; out[i]=f2bf(bf2f(x[i])+a[i]+b[i]); }

extern "C" void kernel_launch(void* const* d_in, const int* in_sizes, int n_in, void* d_out, int out_size, void* d_ws, size_t ws_size, hipStream_t stream){
  const u16* x=(const u16*)d_in[0]; const u16* wqkv=(const u16*)d_in[1]; const u16* wo=(const u16*)d_in[2];
  const u16* w1=(const u16*)d_in[4]; const u16* w3=(const u16*)d_in[5]; const u16* w2=(const u16*)d_in[6];
  float* ws=(float*)d_ws; float* xn=ws; float* qkv=xn+S*D; float* kv=qkv+S*3*D; float* o=kv+32768; float* ya=o+S*D; float* yb=ya+S*D; float* g=yb+S*D; float* u=g+(size_t)S*F;
  k_ln<<<S,64,0,stream>>>(x,xn);
  k_mm<<<dim3((3*D+255)/256,S/8),256,0,stream>>>(xn,wqkv,qkv,3*D,D);
  k_kv<<<H,384,0,stream>>>(qkv,kv);
  k_o<<<S*D/256,256,0,stream>>>(qkv,kv,o);
  k_mm<<<dim3((D+255)/256,S/8),256,0,stream>>>(o,wo,ya,D,D);
  k_mm<<<dim3(F/256,S/8),256,0,stream>>>(xn,w1,g,F,D);
  k_mm<<<dim3(F/256,S/8),256,0,stream>>>(xn,w3,u,F,D);
  k_glu<<<S*F/256,256,0,stream>>>(g,u);
  k_mm<<<dim3((D+255)/256,S/8),256,0,stream>>>(g,w2,yb,D,F);
  k_fin<<<S*D/256,256,0,stream>>>(x,ya,yb,(u16*)d_out);
}
```

```cpp
#include <hip/hip_runtime.h>
#include <hip/hip_bf16.h>
typedef unsigned short u16;
__device__ __forceinline__ float bf2f(u16 v){ return __uint_as_float(((unsigned)v)<<16); }
__device__ __forceinline__ u16 f2bf(float x){ unsigned u=__float_as_uint(x); u += 0x7fffu + ((u>>16)&1u); return (u16)(u>>16); }
constexpr int S=2048, D=576, F=4608, H=32, DH=18;

__global__ void k_ln(const u16* x, float* xn){
  int s=blockIdx.x; __shared__ float red[2][64];
  float a=0,b=0; for(int i=threadIdx.x;i<D;i+=64){float v=bf2f(x[s*D+i]); a+=v;}
  red[0][threadIdx.x]=a; __syncthreads(); float m=0; for(int i=0;i<64;i++) m+=red[0][i]; m/=D;
  for(int i=threadIdx.x;i<D;i+=64){float v=bf2f(x[s*D+i])-m; b+=v*v;}
  red[1][threadIdx.x]=b; __syncthreads(); float var=0; for(int i=0;i<64;i++) var+=red[1][i]; var/=D;
  float r=rsqrtf(var+1e-5f);
  for(int i=threadIdx.x;i<D;i+=64) xn[s*D+i]=bf2f(f2bf((bf2f(x[s*D+i])-m)*r));
}
__global__ void k_mm(const float* A, const u16* B, float* C, int N, int K){
  int n=blockIdx.x*256+threadIdx.x; int m0=blockIdx.y*8; if(n>=N) return;
  float acc[8]={0,0,0,0,0,0,0,0};
  for(int k=0;k<K;k++){ float b=bf2f(B[(size_t)k*N+n]); for(int j=0;j<8;j++) acc[j]+=A[(size_t)(m0+j)*K+k]*b; }
  for(int j=0;j<8;j++) C[(size_t)(m0+j)*N+n]=acc[j];
}
__device__ float phi(float t){ return (t>0? t+1.f: __expf(t))+1e-6f; }
__global__ void k_kv(const float* qkv, float* kv){
  int h=blockIdx.x; int d=threadIdx.x/19, e=threadIdx.x%19; if(d>=DH) return;
  float a=0; for(int s=0;s<S;s++){ float pk=bf2f(f2bf(phi(qkv[(size_t)s*3*D+D+h*DH+d]))); float v= e<DH? bf2f(f2bf(qkv[(size_t)s*3*D+2*D+h*DH+e])):1.f; a+=pk*v; }
  kv[(h*DH+d)*19+e]=a;
}
__global__ void k_o(const float* qkv, const float* kv, float* o){
  int i=blockIdx.x*256+threadIdx.x; int s=i/D, c=i%D, h=c/DH, e=c%DH;
  float num=0, den=0; for(int d=0;d<DH;d++){ float pq=bf2f(f2bf(phi(qkv[(size_t)s*3*D+h*DH+d]))); num+=pq*kv[(h*DH+d)*19+e]; den+=pq*kv[(h*DH+d)*19+18]; }
  o[i]=bf2f(f2bf(num/(den+1e-6f)));
}
__global__ void k_glu(float* g, const float* u){ size_t i=(size_t)blockIdx.x*256+threadIdx.x; float a=g[i]; g[i]=bf2f(f2bf(a/(1.f+__expf(-a))*u[i])); }
__global__ void k_fin(const u16* x, const float* a, const float* b, u16* out){ int i=blockIdx.x*256+threadIdx.x; out[i]=f2bf(bf2f(x[i])+a[i]+b[i]); }


typedef unsigned u32x4 __attribute__((ext_vector_type(4)));
__global__ __launch_bounds__(256) void k_probe(char* ws, const char* w1, const char* w3, const char* w2, float* sink){
  unsigned long long t0 = __builtin_amdgcn_s_memrealtime();
  int MODE = 2;
  if (MODE == 0 || MODE == 1) {
    u32x4 v = {threadIdx.x, blockIdx.x, 3u, 4u};
    char* p = ws + (size_t)blockIdx.x * 147456 + threadIdx.x * 16;
    #pragma unroll 4
    for (int i = 0; i < 36; ++i) {
      if (MODE == 0) *(u32x4*)(p + i * 4096) = v;
      else asm volatile("global_store_dwordx4 %0, %1, off sc1\n s_nop 1" :: "v"(p + i * 4096), "v"(v) : "memory");
    }
    asm volatile("s_waitcnt vmcnt(0)" ::: "memory");
  } else {
    u32x4 a = {0,0,0,0};
    for (int m = 0; m < 3; ++m) {
      const char* p = (m == 0 ? w1 : m == 1 ? w3 : w2) + (size_t)blockIdx.x * 20736;
      for (int i = threadIdx.x * 16; i < 20736; i += 4096) a += *(const u32x4*)(p + i);
    }
    if (a[0] + a[1] + a[2] + a[3] == 0x12345678u) sink[0] = 1.f;
    asm volatile("s_waitcnt vmcnt(0)" ::: "memory");
  }
  __syncthreads();
  unsigned long long t1 = __builtin_amdgcn_s_memrealtime();
  unsigned long long d = (t1 - t0) * 100;
  while (__builtin_amdgcn_s_memrealtime() - t1 < d) __builtin_amdgcn_s_sleep(8);
}

extern "C" void kernel_launch(void* const* d_in, const int* in_sizes, int n_in, void* d_out, int out_size, void* d_ws, size_t ws_size, hipStream_t stream){
  const u16* x=(const u16*)d_in[0]; const u16* wqkv=(const u16*)d_in[1]; const u16* wo=(const u16*)d_in[2];
  const u16* w1=(const u16*)d_in[4]; const u16* w3=(const u16*)d_in[5]; const u16* w2=(const u16*)d_in[6];
  float* ws=(float*)d_ws; float* xn=ws; float* qkv=xn+S*D; float* kv=qkv+S*3*D; float* o=kv+32768; float* ya=o+S*D; float* yb=ya+S*D; float* g=yb+S*D; float* u=g+(size_t)S*F;
  k_probe<<<256,256,0,stream>>>((char*)d_ws + (size_t)(0), (const char*)w1, (const char*)w3, (const char*)w2, ws + (60<<20));
  k_ln<<<S,64,0,stream>>>(x,xn);
  k_mm<<<dim3((3*D+255)/256,S/8),256,0,stream>>>(xn,wqkv,qkv,3*D,D);
  k_kv<<<H,384,0,stream>>>(qkv,kv);
  k_o<<<S*D/256,256,0,stream>>>(qkv,kv,o);
  k_mm<<<dim3((D+255)/256,S/8),256,0,stream>>>(o,wo,ya,D,D);
  k_mm<<<dim3(F/256,S/8),256,0,stream>>>(xn,w1,g,F,D);
  k_mm<<<dim3(F/256,S/8),256,0,stream>>>(xn,w3,u,F,D);
  k_glu<<<S*F/256,256,0,stream>>>(g,u);
  k_mm<<<dim3((D+255)/256,S/8),256,0,stream>>>(g,w2,yb,D,F);
  k_fin<<<S*D/256,256,0,stream>>>(x,ya,yb,(u16*)d_out);
}
```

```cpp
#include <hip/hip_runtime.h>
#include <hip/hip_bf16.h>

extern "C" __global__ __launch_bounds__(256, 1) void fused_main(const void* x, const void* wqkv, const void* wo, const void* w1,
                                                                const void* w3, const void* w2, void* ws, void* out) {
  __shared__ __attribute__((aligned(16))) char lds[163840];
  asm volatile(
    "s_load_dwordx16 s[4:19], %0, 0x0\n"
    "s_mov_b32 s20, %1\n"
    "v_lshrrev_b32 v1, 6, %2\n"
    "v_and_b32 v0, 63, %2\n"
    "v_readfirstlane_b32 s21, v1\n"
    "s_getpc_b64 s[92:93]\n"
    "L_pcbase:\n"
    "s_and_b32 s93, s93, 0xffff\n"
    "s_mov_b32 s94, L_end-L_pcbase\n"
    "s_mov_b32 s95, 0x20000\n"
    "s_lshr_b32 s86, s20, 3\n"
    "s_lshl_b32 s86, s86, 2\n"
    "s_add_u32 s86, s86, s21\n"
    "s_lshl_b32 s86, s86, 7\n"
    "v_lshlrev_b32 v20, 14, v0\n"
    "buffer_load_dword a22, v20, s[92:95], s86 offen offset:0\n"
    "s_lshr_b32 s86, s20, 3\n"
    "s_and_b32 s87, s20, 7\n"
    "s_and_b32 s22, s86, 1\n"
    "s_lshr_b32 s23, s86, 1\n"
    "s_lshl_b32 s87, s87, 1\n"
    "s_add_u32 s22, s22, s87\n"
    "s_waitcnt lgkmcnt(0)\n"
    "s_mov_b64 s[28:29], s[4:5]\n"
    "s_and_b32 s29, s29, 0xffff\n"
    "s_mov_b32 s30, 0x240000\n"
    "s_mov_b32 s31, 0x20000\n"
    "v_lshrrev_b32 v20, 3, v0\n"
    "v_and_b32 v21, 7, v0\n"
    "v_mul_u32_u24 v47, 1152, v20\n"
    "v_lshl_add_u32 v47, v21, 4, v47\n"
    "s_lshl_b32 s86, s23, 7\n"
    "s_lshl_b32 s87, s21, 5\n"
    "s_add_u32 s86, s86, s87\n"
    "s_mul_i32 s70, s86, 1152\n"
    "s_add_u32 s71, s70, 9216\n"
    "s_add_u32 s69, s70, 18432\n"
    "s_add_u32 s68, s70, 27648\n"
    "buffer_load_dwordx4 v[184:187], v47, s[28:31], s70 offen offset:0\n"
    "buffer_load_dwordx4 v[188:191], v47, s[28:31], s71 offen offset:0\n"
    "buffer_load_dwordx4 v[112:115], v47, s[28:31], s69 offen offset:0\n"
    "buffer_load_dwordx4 v[116:119], v47, s[28:31], s68 offen offset:0\n"
    "buffer_load_dwordx4 v[192:195], v47, s[28:31], s70 offen offset:128\n"
    "buffer_load_dwordx4 v[196:199], v47, s[28:31], s71 offen offset:128\n"
    "buffer_load_dwordx4 v[120:123], v47, s[28:31], s69 offen offset:128\n"
    "buffer_load_dwordx4 v[124:127], v47, s[28:31], s68 offen offset:128\n"
    "s_mov_b64 s[48:49], s[16:17]\n"
    "s_and_b32 s49, s49, 0xffff\n"
    "s_mov_b32 s50, 0x2c41000\n"
    "s_mov_b32 s51, 0x20000\n"
    "s_lshl_b32 s89, s23, 4\n"
    "s_add_u32 s89, s89, s22\n"
    "s_lshl_b32 s27, s89, 10\n"
    "v_mov_b32 v1, 0\n"
    "buffer_load_dword v43, v1, s[48:51], s27 offen offset:0 sc1\n"
    "buffer_load_dwordx4 v[200:203], v47, s[28:31], s70 offen offset:256\n"
    "buffer_load_dwordx4 v[204:207], v47, s[28:31], s71 offen offset:256\n"
    "buffer_load_dwordx4 v[128:131], v47, s[28:31], s69 offen offset:256\n"
    "buffer_load_dwordx4 v[132:135], v47, s[28:31], s68 offen offset:256\n"
    "buffer_load_dwordx4 v[208:211], v47, s[28:31], s70 offen offset:384\n"
    "v_xor_b32 v21, v20, v21\n"
    "v_lshlrev_b32 v21, 4, v21\n"
    "v_lshl_add_u32 v36, v20, 7, v21\n"
    "s_lshl_b32 s86, s21, 12\n"
    "v_add_u32 v36, s86, v36\n"
    "s_add_u32 s56, s86, 73728\n"
    "s_lshr_b32 s24, s21, 1\n"
    "s_and_b32 s25, s21, 1\n"
    "s_and_b32 s26, s22, 1\n"
    "s_cmp_ge_u32 s21, 2\n"
    "s_cselect_b64 s[36:37], s[12:13], s[10:11]\n"
    "s_and_b32 s37, s37, 0xffff\n"
    "s_mov_b32 s38, 0x510000\n"
    "s_mov_b32 s39, 0x20000\n"
    "s_mul_i32 s86, s22, 576\n"
    "s_mul_i32 s87, s25, 294912\n"
    "s_lshl_b32 s88, s26, 6\n"
    "s_add_u32 s61, s86, s87\n"
    "s_add_u32 s61, s61, s88\n"
    "s_xor_b32 s88, s26, 1\n"
    "s_lshl_b32 s88, s88, 9\n"
    "s_add_u32 s86, s86, s88\n"
    "buffer_load_dwordx4 v[212:215], v47, s[28:31], s71 offen offset:384\n"
    "s_add_u32 s63, s86, s87\n"
    "s_add_u32 s62, s63, s87\n"
    "s_add_u32 s63, s63, 4718592\n"
    "s_mul_i32 s88, s26, -144\n"
    "s_add_u32 s88, s88, 72\n"
    "v_lshrrev_b32 v1, 3, v0\n"
    "v_mul_u32_u24 v5, 9216, v1\n"
    "v_bfe_u32 v1, v0, 4, 1\n"
    "v_lshlrev_b32 v1, 1, v1\n"
    "v_and_b32 v4, 7, v0\n"
    "v_xor_b32 v1, v1, v4\n"
    "v_lshl_add_u32 v2, v1, 4, v5\n"
    "v_xor_b32 v1, 4, v1\n"
    "v_lshl_add_u32 v3, v1, 4, v5\n"
    "v_add_u32 v4, 145408, v2\n"
    "v_add_u32 v5, 218112, v3\n"
    "v_add_u32 v3, 72704, v3\n"
    "v_accvgpr_write_b32 a32, v2\n"
    "v_accvgpr_write_b32 a33, v3\n"
    "v_accvgpr_write_b32 a34, v4\n"
    "v_accvgpr_write_b32 a35, v5\n"
    "buffer_load_dwordx4 v[136:139], v47, s[28:31], s69 offen offset:384\n"
    "v_lshrrev_b32 v3, 4, v0\n"
    "v_lshlrev_b32 v1, 1, v3\n"
    "v_sub_u32 v1, v0, v1\n"
    "v_add_u32 v1, 0, v1\n"
    "v_and_b32 v1, 15, v1\n"
    "v_mul_u32_u24 v22, 52, v1\n"
    "v_lshrrev_b32 v22, 8, v22\n"
    "v_mul_u32_u24 v23, 5, v22\n"
    "v_sub_u32 v23, v1, v23\n"
    "v_mul_u32_u24 v23, 3, v23\n"
    "v_lshrrev_b32 v24, 1, v22\n"
    "v_bfi_b32 v24, v22, 0, v24\n"
    "v_and_b32 v24, 1, v24\n"
    "v_mul_lo_u32 v25, v24, s88\n"
    "v_mul_u32_u24 v24, 64, v24\n"
    "v_sub_u32 v24, 31504, v24\n"
    "v_bfe_u32 v1, v24, v23, 3\n"
    "v_lshlrev_b32 v22, 1, v22\n"
    "v_mov_b32 v24, 73\n"
    "v_bfe_u32 v22, v24, v22, 2\n"
    "v_mul_u32_u24 v22, 1152, v22\n"
    "buffer_load_dwordx4 v[140:143], v47, s[28:31], s68 offen offset:384\n"
    "v_lshl_add_u32 v1, v1, 3, v22\n"
    "v_mul_u32_u24 v22, 3456, v3\n"
    "v_add3_u32 v1, v1, v22, v25\n"
    "v_add_u32 v2, 72, v1\n"
    "v_add_u32 v3, 12872, v1\n"
    "v_lshrrev_b32 v5, 4, v0\n"
    "v_lshlrev_b32 v1, 1, v5\n"
    "v_sub_u32 v1, v0, v1\n"
    "v_add_u32 v1, 8, v1\n"
    "v_and_b32 v1, 15, v1\n"
    "v_mul_u32_u24 v22, 52, v1\n"
    "v_lshrrev_b32 v22, 8, v22\n"
    "v_mul_u32_u24 v23, 5, v22\n"
    "v_sub_u32 v23, v1, v23\n"
    "v_mul_u32_u24 v23, 3, v23\n"
    "v_lshrrev_b32 v24, 1, v22\n"
    "v_bfi_b32 v24, v22, 0, v24\n"
    "v_and_b32 v24, 1, v24\n"
    "v_mul_lo_u32 v25, v24, s88\n"
    "v_mul_u32_u24 v24, 64, v24\n"
    "v_sub_u32 v24, 31504, v24\n"
    "buffer_load_dwordx4 v[216:219], v47, s[28:31], s70 offen offset:512\n"
    "v_bfe_u32 v1, v24, v23, 3\n"
    "v_lshlrev_b32 v22, 1, v22\n"
    "v_mov_b32 v24, 73\n"
    "v_bfe_u32 v22, v24, v22, 2\n"
    "v_mul_u32_u24 v22, 1152, v22\n"
    "v_lshl_add_u32 v1, v1, 3, v22\n"
    "v_mul_u32_u24 v22, 3456, v5\n"
    "v_add3_u32 v1, v1, v22, v25\n"
    "v_add_u32 v4, 25672, v1\n"
    "v_add_u32 v5, 38472, v1\n"
    "v_and_b32 v20, 15, v0\n"
    "v_bfe_u32 v21, v0, 4, 1\n"
    "v_cmp_eq_u32_e64 vcc, 0, v21\n"
    "s_xor_b32 s86, s22, 1\n"
    "v_lshl_add_u32 v22, v20, 4, s86\n"
    "s_lshl_b32 s86, s23, 4\n"
    "v_add_u32 v23, s86, v20\n"
    "v_cndmask_b32_e64 v22, v23, v22, vcc\n"
    "v_lshlrev_b32 v18, 10, v22\n"
    "s_lshr_b32 s86, s22, 2\n"
    "v_mov_b32 v23, s86\n"
    "buffer_load_dwordx4 v[220:223], v47, s[28:31], s71 offen offset:512\n"
    "v_mov_b32 v24, s25\n"
    "v_cndmask_b32_e64 v23, v23, v24, vcc\n"
    "v_lshl_add_u32 v17, v23, 7, v18\n"
    "s_add_u32 s86, s22, 16\n"
    "v_mov_b32 v23, s86\n"
    "v_mov_b32 v24, s23\n"
    "v_cndmask_b32_e64 v23, v23, v24, vcc\n"
    "v_lshlrev_b32 v23, 4, v23\n"
    "v_lshl_add_u32 v41, v22, 9, v23\n"
    "s_min_u32 s87, s21, 1\n"
    "s_lshl_b32 s87, s87, 31\n"
    "s_add_u32 s86, s87, 262144\n"
    "v_add_u32 v41, s86, v41\n"
    "v_and_b32 v20, 31, v0\n"
    "s_lshr_b32 s86, s27, 1\n"
    "s_add_u32 s86, s86, 262144\n"
    "v_lshl_add_u32 v46, v20, 4, s86\n"
    "s_or_b32 s79, s27, s87\n"
    "s_lshl_b32 s88, s21, 7\n"
    "s_add_u32 s89, s27, s88\n"
    "s_add_u32 s89, s89, 256\n"
    "buffer_load_dwordx4 v[144:147], v47, s[28:31], s69 offen offset:512\n"
    "s_add_u32 s86, s79, 128\n"
    "v_mov_b32 v26, 0x80000000\n"
    "v_writelane_b32 v26, s89, 0\n"
    "v_writelane_b32 v26, s86, 1\n"
    "s_mov_b64 s[32:33], s[6:7]\n"
    "s_sub_u32 s32, s32, 72\n"
    "s_subb_u32 s33, s33, 0\n"
    "s_and_b32 s33, s33, 0xffff\n"
    "s_mov_b32 s34, 0x1e6048\n"
    "s_mov_b32 s35, 0x20000\n"
    "s_mov_b64 s[40:41], s[14:15]\n"
    "s_sub_u32 s40, s40, 3072\n"
    "s_subb_u32 s41, s41, 0\n"
    "s_and_b32 s41, s41, 0xffff\n"
    "s_mov_b32 s42, 0x510c00\n"
    "s_mov_b32 s43, 0x20000\n"
    "s_mov_b64 s[44:45], s[8:9]\n"
    "s_sub_u32 s44, s44, 3072\n"
    "s_subb_u32 s45, s45, 0\n"
    "s_and_b32 s45, s45, 0xffff\n"
    "s_mov_b32 s46, 0xa2c00\n"
    "s_mov_b32 s47, 0x20000\n"
    "s_mov_b64 s[52:53], s[18:19]\n"
    "s_and_b32 s53, s53, 0xffff\n"
    "buffer_load_dwordx4 v[148:151], v47, s[28:31], s68 offen offset:512\n"
    "s_mov_b32 s54, 0x240000\n"
    "s_mov_b32 s55, 0x20000\n"
    "s_mov_b32 s72, 0x3ef5fdf0\n"
    "s_mov_b32 s73, 0xbf317218\n"
    "s_mov_b32 s74, 0x358637bd\n"
    "s_mov_b32 s75, 0xbfb8aa3b\n"
    "s_mov_b32 s76, 0xb53a10b6\n"
    "s_mov_b32 s77, 0xffff0000\n"
    "s_mov_b32 s78, 0x3f803f80\n"
    "s_mul_i32 s60, s21, 55296\n"
    "s_mul_i32 s87, s22, 72\n"
    "s_add_u32 s60, s60, s87\n"
    "s_lshl_b32 s57, s24, 13\n"
    "s_lshl_b32 s87, s25, 11\n"
    "s_add_u32 s57, s57, s87\n"
    "s_add_u32 s57, s57, 73728\n"
    "s_mul_i32 s86, s26, 640\n"
    "s_mul_i32 s87, s21, 9216\n"
    "s_add_u32 s86, s86, s87\n"
    "s_add_u32 s86, s86, 3072\n"
    "s_mul_i32 s88, s26, 640\n"
    "buffer_load_dwordx4 v[224:227], v47, s[28:31], s70 offen offset:640\n"
    "s_sub_u32 s95, 512, s88\n"
    "s_mul_i32 s87, s22, 331776\n"
    "s_add_u32 s87, s87, s86\n"
    "s_mul_i32 s88, s26, 36864\n"
    "s_add_u32 s64, s87, s88\n"
    "s_lshl_b32 s88, s88, 3\n"
    "s_sub_u32 s65, s87, s88\n"
    "s_add_u32 s65, s65, 294912\n"
    "s_xor_b32 s87, s22, 1\n"
    "s_mul_i32 s87, s87, 331776\n"
    "s_add_u32 s87, s87, s86\n"
    "s_add_u32 s67, s87, s88\n"
    "s_lshr_b32 s88, s88, 3\n"
    "s_sub_u32 s66, s87, s88\n"
    "s_add_u32 s66, s66, 36864\n"
    "s_lshr_b32 s87, s22, 1\n"
    "s_mul_i32 s87, s87, 82944\n"
    "s_add_u32 s91, s87, s86\n"
    "s_add_u32 s91, s91, 18432\n"
    "s_mul_i32 s87, s24, 4608\n"
    "s_add_u32 s86, s86, s87\n"
    "buffer_load_dwordx4 v[228:231], v47, s[28:31], s71 offen offset:640\n"
    "s_xor_b32 s87, s22, 1\n"
    "s_mul_i32 s87, s87, 41472\n"
    "s_add_u32 s90, s87, s86\n"
    "s_mov_b32 s26, s89\n"
    "s_lshl_b32 s89, s23, 4\n"
    "s_add_u32 s89, s89, s22\n"
    "s_mul_i32 s92, s89, 81920\n"
    "s_add_u32 s92, s92, 1835008\n"
    "s_xor_b32 s87, s89, 1\n"
    "s_mul_i32 s93, s87, 81920\n"
    "s_add_u32 s93, s93, 1835008\n"
    "s_lshr_b32 s87, s89, 1\n"
    "s_lshl_b32 s94, s87, 11\n"
    "s_add_u32 s94, s94, 1572864\n"
    "s_mov_b32 s59, s86\n"
    "s_mov_b32 s80, 0x00030003\n"
    "s_mov_b32 s81, 0x00030003\n"
    "s_lshl_b32 s86, s25, 1\n"
    "s_lshl_b64 s[80:81], s[80:81], s86\n"
    "s_cmp_eq_u32 s25, 1\n"
    "s_cselect_b64 s[82:83], -1, 0\n"
    "s_mov_b32 s84, 0xffff\n"
    "buffer_load_dwordx4 v[152:155], v47, s[28:31], s69 offen offset:640\n"
    "s_mov_b32 s85, 0\n"
    "v_lshrrev_b32 v20, 4, v0\n"
    "v_bfe_u32 v21, v0, 2, 2\n"
    "v_and_b32 v22, 3, v0\n"
    "v_lshl_add_u32 v23, v20, 3, v21\n"
    "v_lshlrev_b32 v23, 7, v23\n"
    "v_lshl_add_u32 v23, v22, 3, v23\n"
    "v_add_u32 v23, 32768, v23\n"
    "v_lshrrev_b32 v24, 1, v21\n"
    "v_and_b32 v25, 1, v20\n"
    "v_lshl_add_u32 v24, v25, 1, v24\n"
    "s_lshl_b32 s86, s25, 1\n"
    "s_add_u32 s87, s86, 0\n"
    "v_xor_b32 v25, s87, v24\n"
    "v_lshl_add_u32 v6, v25, 5, v23\n"
    "v_add_u32 v8, 57344, v6\n"
    "s_add_u32 s87, s86, 1\n"
    "v_xor_b32 v25, s87, v24\n"
    "v_lshl_add_u32 v7, v25, 5, v23\n"
    "v_add_u32 v9, 57344, v7\n"
    "s_lshl_b32 s86, s24, 12\n"
    "buffer_load_dwordx4 v[156:159], v47, s[28:31], s68 offen offset:640\n"
    "s_lshl_b32 s87, s25, 11\n"
    "s_add_u32 s88, s86, s87\n"
    "s_xor_b32 s89, s88, 2048\n"
    "v_and_b32 v23, 15, v0\n"
    "v_and_b32 v24, 7, v0\n"
    "v_xor_b32 v24, v20, v24\n"
    "v_lshlrev_b32 v24, 4, v24\n"
    "v_lshl_add_u32 v23, v23, 7, v24\n"
    "s_lshl_b32 s86, s88, 1\n"
    "v_add_u32 v37, s86, v23\n"
    "v_xor_b32 v38, 64, v37\n"
    "v_xor_b32 v39, 4096, v37\n"
    "v_xor_b32 v40, 4096, v38\n"
    "s_lshl_b32 s87, s25, 13\n"
    "s_add_u32 s88, s88, s87\n"
    "s_add_u32 s89, s89, s87\n"
    "v_lshl_add_u32 v10, v0, 4, s88\n"
    "v_lshl_add_u32 v11, v0, 4, s89\n"
    "v_lshl_add_u32 v31, v20, 3, v21\n"
    "v_lshlrev_b32 v31, 8, v31\n"
    "v_add_u32 v31, 32768, v31\n"
    "buffer_load_dwordx4 v[232:235], v47, s[28:31], s70 offen offset:768\n"
    "v_and_b32 v28, 1, v20\n"
    "v_lshl_add_u32 v28, v28, 2, v21\n"
    "v_lshlrev_b32 v28, 5, v28\n"
    "s_mul_i32 s88, s25, 6\n"
    "v_add_u32 v29, s88, v22\n"
    "v_lshl_add_u32 v29, v29, 3, v28\n"
    "v_add_u32 v32, 0, v29\n"
    "v_and_b32 v32, 255, v32\n"
    "v_add_u32 v32, v31, v32\n"
    "v_accvgpr_write_b32 a28, v32\n"
    "v_add_u32 v32, 80, v29\n"
    "v_and_b32 v32, 255, v32\n"
    "v_add_u32 v32, v31, v32\n"
    "v_accvgpr_write_b32 a29, v32\n"
    "v_add_u32 v32, 160, v29\n"
    "v_and_b32 v32, 255, v32\n"
    "v_add_u32 v32, v31, v32\n"
    "v_accvgpr_write_b32 a31, v32\n"
    "v_mul_u32_u24 v30, 5, v22\n"
    "v_mov_b32 v32, 156814\n"
    "v_bfe_u32 v30, v32, v30, 5\n"
    "buffer_load_dwordx4 v[236:239], v47, s[28:31], s71 offen offset:768\n"
    "s_mov_b64 exec, 0\n"
    "s_cmp_eq_u32 s21, 0\n"
    "s_cbranch_scc1 L_hop0\n"
    "s_cmp_eq_u32 s21, 1\n"
    "s_cbranch_scc1 L_hop1\n"
    "s_cmp_eq_u32 s21, 2\n"
    "s_cbranch_scc1 L_hop2\n"
    "s_branch L_hop3\n"
    "L_back0:\n"
    "s_mov_b64 exec, -1\n"
    "v_lshl_add_u32 v30, v30, 3, v28\n"
    "v_and_b32 v30, 255, v30\n"
    "v_add_u32 v32, v31, v30\n"
    "v_accvgpr_write_b32 a30, v32\n"
    "v_and_b32 v28, 1, v0\n"
    "v_lshlrev_b32 v28, 4, v28\n"
    "v_mov_b32 v29, 0x3f80\n"
    "v_lshlrev_b32 v28, v28, v29\n"
    "v_cmp_eq_u32_e64 vcc, v20, v21\n"
    "s_nop 1\n"
    "v_cndmask_b32_e64 v28, 0, v28, vcc\n"
    "v_cmp_gt_u32_e64 vcc, 2, v22\n"
    "s_nop 1\n"
    "v_cndmask_b32_e64 v29, 0, v28, vcc\n"
    "v_cndmask_b32_e64 v30, v28, 0, vcc\n"
    "v_accvgpr_write_b32 a16, 0\n"
    "v_accvgpr_write_b32 a17, 0\n"
    "v_accvgpr_write_b32 a18, v29\n"
    "v_accvgpr_write_b32 a19, v30\n"
    "v_accvgpr_write_b32 a20, 0\n"
    "v_accvgpr_write_b32 a21, 0\n"
    "buffer_load_dwordx4 v[160:163], v47, s[28:31], s69 offen offset:768\n"
    "s_waitcnt vmcnt(27)\n"
    "v_dot2_f32_bf16 v32, v184, s78, 0\n"
    "v_dot2_f32_bf16 v28, v184, v184, 0\n"
    "s_waitcnt vmcnt(26)\n"
    "v_dot2_f32_bf16 v33, v188, s78, 0\n"
    "v_dot2_f32_bf16 v29, v188, v188, 0\n"
    "v_dot2_f32_bf16 v32, v185, s78, v32\n"
    "v_dot2_f32_bf16 v28, v185, v185, v28\n"
    "v_dot2_f32_bf16 v33, v189, s78, v33\n"
    "v_dot2_f32_bf16 v29, v189, v189, v29\n"
    "v_dot2_f32_bf16 v32, v186, s78, v32\n"
    "v_dot2_f32_bf16 v28, v186, v186, v28\n"
    "v_dot2_f32_bf16 v33, v190, s78, v33\n"
    "v_dot2_f32_bf16 v29, v190, v190, v29\n"
    "v_dot2_f32_bf16 v32, v187, s78, v32\n"
    "v_dot2_f32_bf16 v28, v187, v187, v28\n"
    "v_dot2_f32_bf16 v33, v191, s78, v33\n"
    "v_dot2_f32_bf16 v29, v191, v191, v29\n"
    "buffer_load_dwordx4 v[164:167], v47, s[28:31], s68 offen offset:768\n"
    "s_waitcnt vmcnt(26)\n"
    "v_dot2_f32_bf16 v34, v112, s78, 0\n"
    "v_dot2_f32_bf16 v30, v112, v112, 0\n"
    "s_waitcnt vmcnt(25)\n"
    "v_dot2_f32_bf16 v35, v116, s78, 0\n"
    "v_dot2_f32_bf16 v31, v116, v116, 0\n"
    "v_dot2_f32_bf16 v34, v113, s78, v34\n"
    "v_dot2_f32_bf16 v30, v113, v113, v30\n"
    "v_dot2_f32_bf16 v35, v117, s78, v35\n"
    "v_dot2_f32_bf16 v31, v117, v117, v31\n"
    "v_dot2_f32_bf16 v34, v114, s78, v34\n"
    "v_dot2_f32_bf16 v30, v114, v114, v30\n"
    "v_dot2_f32_bf16 v35, v118, s78, v35\n"
    "v_dot2_f32_bf16 v31, v118, v118, v31\n"
    "v_dot2_f32_bf16 v34, v115, s78, v34\n"
    "v_dot2_f32_bf16 v30, v115, v115, v30\n"
    "v_dot2_f32_bf16 v35, v119, s78, v35\n"
    "v_dot2_f32_bf16 v31, v119, v119, v31\n"
    "buffer_load_dwordx4 v[240:243], v47, s[28:31], s70 offen offset:896\n"
    "s_waitcnt vmcnt(25)\n"
    "v_dot2_f32_bf16 v32, v192, s78, v32\n"
    "v_dot2_f32_bf16 v28, v192, v192, v28\n"
    "s_waitcnt vmcnt(24)\n"
    "v_dot2_f32_bf16 v33, v196, s78, v33\n"
    "v_dot2_f32_bf16 v29, v196, v196, v29\n"
    "v_dot2_f32_bf16 v32, v193, s78, v32\n"
    "v_dot2_f32_bf16 v28, v193, v193, v28\n"
    "v_dot2_f32_bf16 v33, v197, s78, v33\n"
    "v_dot2_f32_bf16 v29, v197, v197, v29\n"
    "v_dot2_f32_bf16 v32, v194, s78, v32\n"
    "v_dot2_f32_bf16 v28, v194, v194, v28\n"
    "v_dot2_f32_bf16 v33, v198, s78, v33\n"
    "v_dot2_f32_bf16 v29, v198, v198, v29\n"
    "v_dot2_f32_bf16 v32, v195, s78, v32\n"
    "v_dot2_f32_bf16 v28, v195, v195, v28\n"
    "v_dot2_f32_bf16 v33, v199, s78, v33\n"
    "v_dot2_f32_bf16 v29, v199, v199, v29\n"
    "buffer_load_dwordx4 v[244:247], v47, s[28:31], s71 offen offset:896\n"
    "s_waitcnt vmcnt(24)\n"
    "v_dot2_f32_bf16 v34, v120, s78, v34\n"
    "v_dot2_f32_bf16 v30, v120, v120, v30\n"
    "s_waitcnt vmcnt(23)\n"
    "v_dot2_f32_bf16 v35, v124, s78, v35\n"
    "v_dot2_f32_bf16 v31, v124, v124, v31\n"
    "v_dot2_f32_bf16 v34, v121, s78, v34\n"
    "v_dot2_f32_bf16 v30, v121, v121, v30\n"
    "v_dot2_f32_bf16 v35, v125, s78, v35\n"
    "v_dot2_f32_bf16 v31, v125, v125, v31\n"
    "v_dot2_f32_bf16 v34, v122, s78, v34\n"
    "v_dot2_f32_bf16 v30, v122, v122, v30\n"
    "v_dot2_f32_bf16 v35, v126, s78, v35\n"
    "v_dot2_f32_bf16 v31, v126, v126, v31\n"
    "v_dot2_f32_bf16 v34, v123, s78, v34\n"
    "v_dot2_f32_bf16 v30, v123, v123, v30\n"
    "v_dot2_f32_bf16 v35, v127, s78, v35\n"
    "v_dot2_f32_bf16 v31, v127, v127, v31\n"
    "buffer_load_dwordx4 v[168:171], v47, s[28:31], s69 offen offset:896\n"
    "s_waitcnt vmcnt(22)\n"
    "v_dot2_f32_bf16 v32, v200, s78, v32\n"
    "v_dot2_f32_bf16 v28, v200, v200, v28\n"
    "s_waitcnt vmcnt(21)\n"
    "v_dot2_f32_bf16 v33, v204, s78, v33\n"
    "v_dot2_f32_bf16 v29, v204, v204, v29\n"
    "v_dot2_f32_bf16 v32, v201, s78, v32\n"
    "v_dot2_f32_bf16 v28, v201, v201, v28\n"
    "v_dot2_f32_bf16 v33, v205, s78, v33\n"
    "v_dot2_f32_bf16 v29, v205, v205, v29\n"
    "v_dot2_f32_bf16 v32, v202, s78, v32\n"
    "v_dot2_f32_bf16 v28, v202, v202, v28\n"
    "v_dot2_f32_bf16 v33, v206, s78, v33\n"
    "v_dot2_f32_bf16 v29, v206, v206, v29\n"
    "v_dot2_f32_bf16 v32, v203, s78, v32\n"
    "v_dot2_f32_bf16 v28, v203, v203, v28\n"
    "v_dot2_f32_bf16 v33, v207, s78, v33\n"
    "v_dot2_f32_bf16 v29, v207, v207, v29\n"
    "buffer_load_dwordx4 v[172:175], v47, s[28:31], s68 offen offset:896\n"
    "s_waitcnt vmcnt(21)\n"
    "v_dot2_f32_bf16 v34, v128, s78, v34\n"
    "v_dot2_f32_bf16 v30, v128, v128, v30\n"
    "s_waitcnt vmcnt(20)\n"
    "v_dot2_f32_bf16 v35, v132, s78, v35\n"
    "v_dot2_f32_bf16 v31, v132, v132, v31\n"
    "v_dot2_f32_bf16 v34, v129, s78, v34\n"
    "v_dot2_f32_bf16 v30, v129, v129, v30\n"
    "v_dot2_f32_bf16 v35, v133, s78, v35\n"
    "v_dot2_f32_bf16 v31, v133, v133, v31\n"
    "v_dot2_f32_bf16 v34, v130, s78, v34\n"
    "v_dot2_f32_bf16 v30, v130, v130, v30\n"
    "v_dot2_f32_bf16 v35, v134, s78, v35\n"
    "v_dot2_f32_bf16 v31, v134, v134, v31\n"
    "v_dot2_f32_bf16 v34, v131, s78, v34\n"
    "v_dot2_f32_bf16 v30, v131, v131, v30\n"
    "v_dot2_f32_bf16 v35, v135, s78, v35\n"
    "v_dot2_f32_bf16 v31, v135, v135, v31\n"
    "buffer_load_dwordx4 v[248:251], v47, s[28:31], s70 offen offset:1024\n"
    "s_waitcnt vmcnt(20)\n"
    "v_dot2_f32_bf16 v32, v208, s78, v32\n"
    "v_dot2_f32_bf16 v28, v208, v208, v28\n"
    "s_waitcnt vmcnt(19)\n"
    "v_dot2_f32_bf16 v33, v212, s78, v33\n"
    "v_dot2_f32_bf16 v29, v212, v212, v29\n"
    "v_dot2_f32_bf16 v32, v209, s78, v32\n"
    "v_dot2_f32_bf16 v28, v209, v209, v28\n"
    "v_dot2_f32_bf16 v33, v213, s78, v33\n"
    "v_dot2_f32_bf16 v29, v213, v213, v29\n"
    "v_dot2_f32_bf16 v32, v210, s78, v32\n"
    "v_dot2_f32_bf16 v28, v210, v210, v28\n"
    "v_dot2_f32_bf16 v33, v214, s78, v33\n"
    "v_dot2_f32_bf16 v29, v214, v214, v29\n"
    "v_dot2_f32_bf16 v32, v211, s78, v32\n"
    "v_dot2_f32_bf16 v28, v211, v211, v28\n"
    "v_dot2_f32_bf16 v33, v215, s78, v33\n"
    "v_dot2_f32_bf16 v29, v215, v215, v29\n"
    "buffer_load_dwordx4 v[252:255], v47, s[28:31], s71 offen offset:1024\n"
    "s_waitcnt vmcnt(19)\n"
    "v_dot2_f32_bf16 v34, v136, s78, v34\n"
    "v_dot2_f32_bf16 v30, v136, v136, v30\n"
    "s_waitcnt vmcnt(18)\n"
    "v_dot2_f32_bf16 v35, v140, s78, v35\n"
    "v_dot2_f32_bf16 v31, v140, v140, v31\n"
    "v_dot2_f32_bf16 v34, v137, s78, v34\n"
    "v_dot2_f32_bf16 v30, v137, v137, v30\n"
    "v_dot2_f32_bf16 v35, v141, s78, v35\n"
    "v_dot2_f32_bf16 v31, v141, v141, v31\n"
    "v_dot2_f32_bf16 v34, v138, s78, v34\n"
    "v_dot2_f32_bf16 v30, v138, v138, v30\n"
    "v_dot2_f32_bf16 v35, v142, s78, v35\n"
    "v_dot2_f32_bf16 v31, v142, v142, v31\n"
    "v_dot2_f32_bf16 v34, v139, s78, v34\n"
    "v_dot2_f32_bf16 v30, v139, v139, v30\n"
    "v_dot2_f32_bf16 v35, v143, s78, v35\n"
    "v_dot2_f32_bf16 v31, v143, v143, v31\n"
    "buffer_load_dwordx4 v[176:179], v47, s[28:31], s69 offen offset:1024\n"
    "s_waitcnt vmcnt(18)\n"
    "v_dot2_f32_bf16 v32, v216, s78, v32\n"
    "v_dot2_f32_bf16 v28, v216, v216, v28\n"
    "s_waitcnt vmcnt(17)\n"
    "v_dot2_f32_bf16 v33, v220, s78, v33\n"
    "v_dot2_f32_bf16 v29, v220, v220, v29\n"
    "v_dot2_f32_bf16 v32, v217, s78, v32\n"
    "v_dot2_f32_bf16 v28, v217, v217, v28\n"
    "v_dot2_f32_bf16 v33, v221, s78, v33\n"
    "v_dot2_f32_bf16 v29, v221, v221, v29\n"
    "v_dot2_f32_bf16 v32, v218, s78, v32\n"
    "v_dot2_f32_bf16 v28, v218, v218, v28\n"
    "v_dot2_f32_bf16 v33, v222, s78, v33\n"
    "v_dot2_f32_bf16 v29, v222, v222, v29\n"
    "v_dot2_f32_bf16 v32, v219, s78, v32\n"
    "v_dot2_f32_bf16 v28, v219, v219, v28\n"
    "v_dot2_f32_bf16 v33, v223, s78, v33\n"
    "v_dot2_f32_bf16 v29, v223, v223, v29\n"
    "buffer_load_dwordx4 v[180:183], v47, s[28:31], s68 offen offset:1024\n"
    "s_waitcnt vmcnt(17)\n"
    "v_dot2_f32_bf16 v34, v144, s78, v34\n"
    "v_dot2_f32_bf16 v30, v144, v144, v30\n"
    "s_waitcnt vmcnt(16)\n"
    "v_dot2_f32_bf16 v35, v148, s78, v35\n"
    "v_dot2_f32_bf16 v31, v148, v148, v31\n"
    "v_dot2_f32_bf16 v34, v145, s78, v34\n"
    "v_dot2_f32_bf16 v30, v145, v145, v30\n"
    "v_dot2_f32_bf16 v35, v149, s78, v35\n"
    "v_dot2_f32_bf16 v31, v149, v149, v31\n"
    "v_dot2_f32_bf16 v34, v146, s78, v34\n"
    "v_dot2_f32_bf16 v30, v146, v146, v30\n"
    "v_dot2_f32_bf16 v35, v150, s78, v35\n"
    "v_dot2_f32_bf16 v31, v150, v150, v31\n"
    "v_dot2_f32_bf16 v34, v147, s78, v34\n"
    "v_dot2_f32_bf16 v30, v147, v147, v30\n"
    "v_dot2_f32_bf16 v35, v151, s78, v35\n"
    "v_dot2_f32_bf16 v31, v151, v151, v31\n"
    "s_add_u32 m0, s56, 0\n"
    "s_add_u32 s58, s60, 0\n"
    "buffer_load_dwordx4 v2, s[32:35], s58 offen offset:0 lds\n"
    "s_mul_i32 s87, s22, 41472\n"
    "s_add_u32 s68, s87, s59\n"
    "s_lshl_b32 s86, s23, 4\n"
    "s_add_u32 s86, s86, s22\n"
    "s_lshl_b32 s86, s86, 2\n"
    "s_add_u32 s86, s86, s21\n"
    "s_mul_i32 s69, s86, 20480\n"
    "s_add_u32 s69, s69, 23855104\n"
    "s_lshl_b32 s86, s22, 1\n"
    "s_add_u32 s86, s86, s25\n"
    "s_lshl_b32 s86, s86, 4\n"
    "s_add_u32 s87, s86, s23\n"
    "s_mul_i32 s70, s87, 2304\n"
    "buffer_load_dwordx4 v3, s[32:35], s58 offen offset:1024 lds\n"
    "s_add_u32 s70, s70, 393216\n"
    "s_xor_b32 s86, s86, 32\n"
    "s_lshl_b32 s87, s24, 3\n"
    "s_add_u32 s87, s86, s87\n"
    "s_mul_i32 s71, s87, 2304\n"
    "s_add_u32 s71, s71, 393216\n"
    "v_add_u32 v43, 64, v43\n"
    "buffer_store_dword v43, v26, s[48:51], 0 offen offset:0 sc1\n"
    "v_readfirstlane_b32 s27, v43\n"
    "s_waitcnt vmcnt(18)\n"
    "v_dot2_f32_bf16 v32, v224, s78, v32\n"
    "v_dot2_f32_bf16 v28, v224, v224, v28\n"
    "s_waitcnt vmcnt(17)\n"
    "v_dot2_f32_bf16 v33, v228, s78, v33\n"
    "v_dot2_f32_bf16 v29, v228, v228, v29\n"
    "buffer_load_dwordx4 v4, s[32:35], s58 offen offset:2048 lds\n"
    "v_dot2_f32_bf16 v32, v225, s78, v32\n"
    "v_dot2_f32_bf16 v28, v225, v225, v28\n"
    "v_dot2_f32_bf16 v33, v229, s78, v33\n"
    "v_dot2_f32_bf16 v29, v229, v229, v29\n"
    "v_dot2_f32_bf16 v32, v226, s78, v32\n"
    "v_dot2_f32_bf16 v28, v226, v226, v28\n"
    "v_dot2_f32_bf16 v33, v230, s78, v33\n"
    "v_dot2_f32_bf16 v29, v230, v230, v29\n"
    "v_dot2_f32_bf16 v32, v227, s78, v32\n"
    "v_dot2_f32_bf16 v28, v227, v227, v28\n"
    "v_dot2_f32_bf16 v33, v231, s78, v33\n"
    "v_dot2_f32_bf16 v29, v231, v231, v29\n"
    "s_waitcnt vmcnt(17)\n"
    "v_dot2_f32_bf16 v34, v152, s78, v34\n"
    "buffer_load_dwordx4 v5, s[32:35], s58 offen offset:3072 lds\n"
    "v_dot2_f32_bf16 v30, v152, v152, v30\n"
    "s_waitcnt vmcnt(17)\n"
    "v_dot2_f32_bf16 v35, v156, s78, v35\n"
    "v_dot2_f32_bf16 v31, v156, v156, v31\n"
    "v_dot2_f32_bf16 v34, v153, s78, v34\n"
    "v_dot2_f32_bf16 v30, v153, v153, v30\n"
    "L_hop0:\n"
    "s_cbranch_execz L_hop4\n"
    "v_dot2_f32_bf16 v35, v157, s78, v35\n"
    "v_dot2_f32_bf16 v31, v157, v157, v31\n"
    "v_dot2_f32_bf16 v34, v154, s78, v34\n"
    "v_dot2_f32_bf16 v30, v154, v154, v30\n"
    "v_dot2_f32_bf16 v35, v158, s78, v35\n"
    "v_dot2_f32_bf16 v31, v158, v158, v31\n"
    "v_dot2_f32_bf16 v34, v155, s78, v34\n"
    "v_dot2_f32_bf16 v30, v155, v155, v30\n"
    "s_add_u32 m0, s56, 16384\n"
    "s_add_u32 s58, s60, 221184\n"
    "buffer_load_dwordx4 v2, s[32:35], s58 offen offset:0 lds\n"
    "v_dot2_f32_bf16 v35, v159, s78, v35\n"
    "v_dot2_f32_bf16 v31, v159, v159, v31\n"
    "s_waitcnt vmcnt(17)\n"
    "v_dot2_f32_bf16 v32, v232, s78, v32\n"
    "v_dot2_f32_bf16 v28, v232, v232, v28\n"
    "s_waitcnt vmcnt(16)\n"
    "v_dot2_f32_bf16 v33, v236, s78, v33\n"
    "v_dot2_f32_bf16 v29, v236, v236, v29\n"
    "v_dot2_f32_bf16 v32, v233, s78, v32\n"
    "v_dot2_f32_bf16 v28, v233, v233, v28\n"
    "v_dot2_f32_bf16 v33, v237, s78, v33\n"
    "v_dot2_f32_bf16 v29, v237, v237, v29\n"
    "v_dot2_f32_bf16 v32, v234, s78, v32\n"
    "v_dot2_f32_bf16 v28, v234, v234, v28\n"
    "v_dot2_f32_bf16 v33, v238, s78, v33\n"
    "buffer_load_dwordx4 v3, s[32:35], s58 offen offset:1024 lds\n"
    "v_dot2_f32_bf16 v29, v238, v238, v29\n"
    "v_dot2_f32_bf16 v32, v235, s78, v32\n"
    "v_dot2_f32_bf16 v28, v235, v235, v28\n"
    "v_dot2_f32_bf16 v33, v239, s78, v33\n"
    "v_dot2_f32_bf16 v29, v239, v239, v29\n"
    "s_waitcnt vmcnt(16)\n"
    "v_dot2_f32_bf16 v34, v160, s78, v34\n"
    "v_dot2_f32_bf16 v30, v160, v160, v30\n"
    "s_waitcnt vmcnt(15)\n"
    "v_dot2_f32_bf16 v35, v164, s78, v35\n"
    "v_dot2_f32_bf16 v31, v164, v164, v31\n"
    "v_dot2_f32_bf16 v34, v161, s78, v34\n"
    "v_dot2_f32_bf16 v30, v161, v161, v30\n"
    "v_dot2_f32_bf16 v35, v165, s78, v35\n"
    "v_dot2_f32_bf16 v31, v165, v165, v31\n"
    "buffer_load_dwordx4 v4, s[32:35], s58 offen offset:2048 lds\n"
    "v_dot2_f32_bf16 v34, v162, s78, v34\n"
    "v_dot2_f32_bf16 v30, v162, v162, v30\n"
    "v_dot2_f32_bf16 v35, v166, s78, v35\n"
    "v_dot2_f32_bf16 v31, v166, v166, v31\n"
    "v_dot2_f32_bf16 v34, v163, s78, v34\n"
    "v_dot2_f32_bf16 v30, v163, v163, v30\n"
    "v_dot2_f32_bf16 v35, v167, s78, v35\n"
    "v_dot2_f32_bf16 v31, v167, v167, v31\n"
    "v_mfma_f32_16x16x32_bf16 v[48:51], a[18:21], v[184:187], 0\n"
    "v_mfma_f32_16x16x32_bf16 v[52:55], a[16:19], v[184:187], 0\n"
    "v_mfma_f32_16x16x32_bf16 v[56:59], a[18:21], v[188:191], 0\n"
    "v_mfma_f32_16x16x32_bf16 v[60:63], a[16:19], v[188:191], 0\n"
    "v_mfma_f32_16x16x32_bf16 v[64:67], a[18:21], v[112:115], 0\n"
    "buffer_load_dwordx4 v5, s[32:35], s58 offen offset:3072 lds\n"
    "v_mfma_f32_16x16x32_bf16 v[68:71], a[16:19], v[112:115], 0\n"
    "v_mfma_f32_16x16x32_bf16 v[72:75], a[18:21], v[116:119], 0\n"
    "v_mfma_f32_16x16x32_bf16 v[76:79], a[16:19], v[116:119], 0\n"
    "v_mfma_f32_16x16x32_bf16 v[80:83], a[18:21], v[192:195], 0\n"
    "v_mfma_f32_16x16x32_bf16 v[84:87], a[16:19], v[192:195], 0\n"
    "v_mfma_f32_16x16x32_bf16 v[88:91], a[18:21], v[196:199], 0\n"
    "v_mfma_f32_16x16x32_bf16 v[92:95], a[16:19], v[196:199], 0\n"
    "v_mfma_f32_16x16x32_bf16 v[96:99], a[18:21], v[120:123], 0\n"
    "v_mfma_f32_16x16x32_bf16 v[100:103], a[16:19], v[120:123], 0\n"
    "v_mfma_f32_16x16x32_bf16 v[104:107], a[18:21], v[124:127], 0\n"
    "v_mfma_f32_16x16x32_bf16 v[108:111], a[16:19], v[124:127], 0\n"
    "s_waitcnt vmcnt(16)\n"
    "v_dot2_f32_bf16 v32, v240, s78, v32\n"
    "v_dot2_f32_bf16 v28, v240, v240, v28\n"
    "s_waitcnt vmcnt(15)\n"
    "v_dot2_f32_bf16 v33, v244, s78, v33\n"
    "v_dot2_f32_bf16 v29, v244, v244, v29\n"
    "v_dot2_f32_bf16 v32, v241, s78, v32\n"
    "v_dot2_f32_bf16 v28, v241, v241, v28\n"
    "v_dot2_f32_bf16 v33, v245, s78, v33\n"
    "v_dot2_f32_bf16 v29, v245, v245, v29\n"
    "v_dot2_f32_bf16 v32, v242, s78, v32\n"
    "v_dot2_f32_bf16 v28, v242, v242, v28\n"
    "v_dot2_f32_bf16 v33, v246, s78, v33\n"
    "v_dot2_f32_bf16 v29, v246, v246, v29\n"
    "v_dot2_f32_bf16 v32, v243, s78, v32\n"
    "v_dot2_f32_bf16 v28, v243, v243, v28\n"
    "v_dot2_f32_bf16 v33, v247, s78, v33\n"
    "v_dot2_f32_bf16 v29, v247, v247, v29\n"
    "s_waitcnt vmcnt(14)\n"
    "v_dot2_f32_bf16 v34, v168, s78, v34\n"
    "v_dot2_f32_bf16 v30, v168, v168, v30\n"
    "s_waitcnt vmcnt(13)\n"
    "v_dot2_f32_bf16 v35, v172, s78, v35\n"
    "v_dot2_f32_bf16 v31, v172, v172, v31\n"
    "v_dot2_f32_bf16 v34, v169, s78, v34\n"
    "v_dot2_f32_bf16 v30, v169, v169, v30\n"
    "v_dot2_f32_bf16 v35, v173, s78, v35\n"
    "v_dot2_f32_bf16 v31, v173, v173, v31\n"
    "v_dot2_f32_bf16 v34, v170, s78, v34\n"
    "v_dot2_f32_bf16 v30, v170, v170, v30\n"
    "v_dot2_f32_bf16 v35, v174, s78, v35\n"
    "v_dot2_f32_bf16 v31, v174, v174, v31\n"
    "v_dot2_f32_bf16 v34, v171, s78, v34\n"
    "v_dot2_f32_bf16 v30, v171, v171, v30\n"
    "v_dot2_f32_bf16 v35, v175, s78, v35\n"
    "v_dot2_f32_bf16 v31, v175, v175, v31\n"
    "s_waitcnt vmcnt(12)\n"
    "v_dot2_f32_bf16 v32, v248, s78, v32\n"
    "v_dot2_f32_bf16 v28, v248, v248, v28\n"
    "s_waitcnt vmcnt(11)\n"
    "v_dot2_f32_bf16 v33, v252, s78, v33\n"
    "v_dot2_f32_bf16 v29, v252, v252, v29\n"
    "v_dot2_f32_bf16 v32, v249, s78, v32\n"
    "v_dot2_f32_bf16 v28, v249, v249, v28\n"
    "v_dot2_f32_bf16 v33, v253, s78, v33\n"
    "v_dot2_f32_bf16 v29, v253, v253, v29\n"
    "v_dot2_f32_bf16 v32, v250, s78, v32\n"
    "v_dot2_f32_bf16 v28, v250, v250, v28\n"
    "v_dot2_f32_bf16 v33, v254, s78, v33\n"
    "v_dot2_f32_bf16 v29, v254, v254, v29\n"
    "v_dot2_f32_bf16 v32, v251, s78, v32\n"
    "v_dot2_f32_bf16 v28, v251, v251, v28\n"
    "v_dot2_f32_bf16 v33, v255, s78, v33\n"
    "v_dot2_f32_bf16 v29, v255, v255, v29\n"
    "s_waitcnt vmcnt(10)\n"
    "v_dot2_f32_bf16 v34, v176, s78, v34\n"
    "v_dot2_f32_bf16 v30, v176, v176, v30\n"
    "s_waitcnt vmcnt(9)\n"
    "v_dot2_f32_bf16 v35, v180, s78, v35\n"
    "v_dot2_f32_bf16 v31, v180, v180, v31\n"
    "v_dot2_f32_bf16 v34, v177, s78, v34\n"
    "v_dot2_f32_bf16 v30, v177, v177, v30\n"
    "v_dot2_f32_bf16 v35, v181, s78, v35\n"
    "v_dot2_f32_bf16 v31, v181, v181, v31\n"
    "v_dot2_f32_bf16 v34, v178, s78, v34\n"
    "v_dot2_f32_bf16 v30, v178, v178, v30\n"
    "v_dot2_f32_bf16 v35, v182, s78, v35\n"
    "v_dot2_f32_bf16 v31, v182, v182, v31\n"
    "v_dot2_f32_bf16 v34, v179, s78, v34\n"
    "v_dot2_f32_bf16 v30, v179, v179, v30\n"
    "v_dot2_f32_bf16 v35, v183, s78, v35\n"
    "v_dot2_f32_bf16 v31, v183, v183, v31\n"
    "v_accvgpr_read_b32 v20, a28\n"
    "v_accvgpr_read_b32 v21, a29\n"
    "v_accvgpr_read_b32 v22, a30\n"
    "v_accvgpr_read_b32 v23, a31\n"
    "v_add_u32 v24, 57344, v20\n"
    "v_add_u32 v25, 57344, v21\n"
    "v_add_u32 v26, 57344, v22\n"
    "v_add_u32 v27, 57344, v23\n"
    "v_add_f32_dpp v32, v32, v32 quad_perm:[1,0,3,2] row_mask:0xf bank_mask:0xf\n"
    "v_add_f32_dpp v33, v33, v33 quad_perm:[1,0,3,2] row_mask:0xf bank_mask:0xf\n"
    "v_add_f32_dpp v34, v34, v34 quad_perm:[1,0,3,2] row_mask:0xf bank_mask:0xf\n"
    "v_add_f32_dpp v35, v35, v35 quad_perm:[1,0,3,2] row_mask:0xf bank_mask:0xf\n"
    "v_add_f32_dpp v28, v28, v28 quad_perm:[1,0,3,2] row_mask:0xf bank_mask:0xf\n"
    "v_add_f32_dpp v29, v29, v29 quad_perm:[1,0,3,2] row_mask:0xf bank_mask:0xf\n"
    "v_add_f32_dpp v30, v30, v30 quad_perm:[1,0,3,2] row_mask:0xf bank_mask:0xf\n"
    "v_add_f32_dpp v31, v31, v31 quad_perm:[1,0,3,2] row_mask:0xf bank_mask:0xf\n"
    "v_add_f32_dpp v32, v32, v32 quad_perm:[2,3,0,1] row_mask:0xf bank_mask:0xf\n"
    "v_add_f32_dpp v33, v33, v33 quad_perm:[2,3,0,1] row_mask:0xf bank_mask:0xf\n"
    "v_add_f32_dpp v34, v34, v34 quad_perm:[2,3,0,1] row_mask:0xf bank_mask:0xf\n"
    "v_add_f32_dpp v35, v35, v35 quad_perm:[2,3,0,1] row_mask:0xf bank_mask:0xf\n"
    "v_add_f32_dpp v28, v28, v28 quad_perm:[2,3,0,1] row_mask:0xf bank_mask:0xf\n"
    "v_add_f32_dpp v29, v29, v29 quad_perm:[2,3,0,1] row_mask:0xf bank_mask:0xf\n"
    "v_add_f32_dpp v30, v30, v30 quad_perm:[2,3,0,1] row_mask:0xf bank_mask:0xf\n"
    "v_add_f32_dpp v31, v31, v31 quad_perm:[2,3,0,1] row_mask:0xf bank_mask:0xf\n"
    "v_add_f32_dpp v32, v32, v32 row_half_mirror row_mask:0xf bank_mask:0xf\n"
    "v_add_f32_dpp v33, v33, v33 row_half_mirror row_mask:0xf bank_mask:0xf\n"
    "v_add_f32_dpp v34, v34, v34 row_half_mirror row_mask:0xf bank_mask:0xf\n"
    "v_add_f32_dpp v35, v35, v35 row_half_mirror row_mask:0xf bank_mask:0xf\n"
    "v_add_f32_dpp v28, v28, v28 row_half_mirror row_mask:0xf bank_mask:0xf\n"
    "v_add_f32_dpp v29, v29, v29 row_half_mirror row_mask:0xf bank_mask:0xf\n"
    "v_add_f32_dpp v30, v30, v30 row_half_mirror row_mask:0xf bank_mask:0xf\n"
    "v_add_f32_dpp v31, v31, v31 row_half_mirror row_mask:0xf bank_mask:0xf\n"
    "s_mov_b32 s86, 0x3ae38e39\n"
    "s_mov_b32 s87, 0x3727c5ac\n"
    "v_mul_f32 v32, s86, v32\n"
    "v_mul_f32 v28, s86, v28\n"
    "v_mul_f32 v33, s86, v33\n"
    "v_mul_f32 v29, s86, v29\n"
    "v_mul_f32 v34, s86, v34\n"
    "v_mul_f32 v30, s86, v30\n"
    "v_mul_f32 v35, s86, v35\n"
    "v_mul_f32 v31, s86, v31\n"
    "v_fma_f32 v28, -v32, v32, v28\n"
    "v_fma_f32 v29, -v33, v33, v29\n"
    "v_fma_f32 v30, -v34, v34, v30\n"
    "v_fma_f32 v31, -v35, v35, v31\n"
    "v_max_f32 v28, 0, v28\n"
    "v_max_f32 v29, 0, v29\n"
    "v_max_f32 v30, 0, v30\n"
    "v_max_f32 v31, 0, v31\n"
    "v_add_f32 v28, s87, v28\n"
    "v_add_f32 v29, s87, v29\n"
    "v_add_f32 v30, s87, v30\n"
    "v_add_f32 v31, s87, v31\n"
    "v_rsq_f32 v28, v28\n"
    "v_rsq_f32 v29, v29\n"
    "v_rsq_f32 v30, v30\n"
    "v_rsq_f32 v31, v31\n"
    "v_mul_f32 v28, s75, v28\n"
    "v_mul_f32 v29, s75, v29\n"
    "v_mul_f32 v30, s75, v30\n"
    "v_mul_f32 v31, s75, v31\n"
    "v_mul_f32_e64 v32, -v32, v28\n"
    "v_mul_f32_e64 v33, -v33, v29\n"
    "v_mul_f32_e64 v34, -v34, v30\n"
    "v_mul_f32_e64 v35, -v35, v31\n"
    "v_pk_fma_f32 v[48:49], v[48:49], v[28:29], v[32:33] op_sel:[0,0,0] op_sel_hi:[1,0,0]\n"
    "v_pk_fma_f32 v[50:51], v[50:51], v[28:29], v[32:33] op_sel:[0,0,0] op_sel_hi:[1,0,0]\n"
    "v_pk_fma_f32 v[52:53], v[52:53], v[28:29], v[32:33] op_sel:[0,0,0] op_sel_hi:[1,0,0]\n"
    "v_pk_fma_f32 v[54:55], v[54:55], v[28:29], v[32:33] op_sel:[0,0,0] op_sel_hi:[1,0,0]\n"
    "v_cvt_pk_bf16_f32 v184, v48, v49\n"
    "v_cvt_pk_bf16_f32 v185, v50, v51\n"
    "v_cvt_pk_bf16_f32 v186, v52, v53\n"
    "v_cvt_pk_bf16_f32 v187, v54, v55\n"
    "ds_write_b128 v36, v[184:187] offset:0\n"
    "s_add_u32 m0, s56, 32768\n"
    "s_add_u32 s58, s60, 442368\n"
    "v_pk_fma_f32 v[56:57], v[56:57], v[28:29], v[32:33] op_sel:[0,1,1] op_sel_hi:[1,1,1]\n"
    "v_pk_fma_f32 v[58:59], v[58:59], v[28:29], v[32:33] op_sel:[0,1,1] op_sel_hi:[1,1,1]\n"
    "v_pk_fma_f32 v[60:61], v[60:61], v[28:29], v[32:33] op_sel:[0,1,1] op_sel_hi:[1,1,1]\n"
    "v_pk_fma_f32 v[62:63], v[62:63], v[28:29], v[32:33] op_sel:[0,1,1] op_sel_hi:[1,1,1]\n"
    "v_cvt_pk_bf16_f32 v188, v56, v57\n"
    "v_cvt_pk_bf16_f32 v189, v58, v59\n"
    "v_cvt_pk_bf16_f32 v190, v60, v61\n"
    "v_cvt_pk_bf16_f32 v191, v62, v63\n"
    "ds_write_b128 v36, v[188:191] offset:1024\n"
    "buffer_load_dwordx4 v2, s[32:35], s58 offen offset:0 lds\n"
    "buffer_load_dwordx4 v3, s[32:35], s58 offen offset:1024 lds\n"
    "v_pk_fma_f32 v[64:65], v[64:65], v[30:31], v[34:35] op_sel:[0,0,0] op_sel_hi:[1,0,0]\n"
    "v_pk_fma_f32 v[66:67], v[66:67], v[30:31], v[34:35] op_sel:[0,0,0] op_sel_hi:[1,0,0]\n"
    "v_pk_fma_f32 v[68:69], v[68:69], v[30:31], v[34:35] op_sel:[0,0,0] op_sel_hi:[1,0,0]\n"
    "v_pk_fma_f32 v[70:71], v[70:71], v[30:31], v[34:35] op_sel:[0,0,0] op_sel_hi:[1,0,0]\n"
    "v_cvt_pk_bf16_f32 v112, v64, v65\n"
    "v_cvt_pk_bf16_f32 v113, v66, v67\n"
    "v_cvt_pk_bf16_f32 v114, v68, v69\n"
    "v_cvt_pk_bf16_f32 v115, v70, v71\n"
    "ds_write_b128 v36, v[112:115] offset:2048\n"
    "buffer_load_dwordx4 v4, s[32:35], s58 offen offset:2048 lds\n"
    "buffer_load_dwordx4 v5, s[32:35], s58 offen offset:3072 lds\n"
    "v_pk_fma_f32 v[72:73], v[72:73], v[30:31], v[34:35] op_sel:[0,1,1] op_sel_hi:[1,1,1]\n"
    "v_pk_fma_f32 v[74:75], v[74:75], v[30:31], v[34:35] op_sel:[0,1,1] op_sel_hi:[1,1,1]\n"
    "v_pk_fma_f32 v[76:77], v[76:77], v[30:31], v[34:35] op_sel:[0,1,1] op_sel_hi:[1,1,1]\n"
    "v_pk_fma_f32 v[78:79], v[78:79], v[30:31], v[34:35] op_sel:[0,1,1] op_sel_hi:[1,1,1]\n"
    "v_cvt_pk_bf16_f32 v116, v72, v73\n"
    "v_cvt_pk_bf16_f32 v117, v74, v75\n"
    "v_cvt_pk_bf16_f32 v118, v76, v77\n"
    "v_cvt_pk_bf16_f32 v119, v78, v79\n"
    "ds_write_b128 v36, v[116:119] offset:3072\n"
    "s_add_u32 m0, s56, 49152\n"
    "s_add_u32 s58, s60, 663552\n"
    "v_pk_fma_f32 v[80:81], v[80:81], v[28:29], v[32:33] op_sel:[0,0,0] op_sel_hi:[1,0,0]\n"
    "v_pk_fma_f32 v[82:83], v[82:83], v[28:29], v[32:33] op_sel:[0,0,0] op_sel_hi:[1,0,0]\n"
    "v_pk_fma_f32 v[84:85], v[84:85], v[28:29], v[32:33] op_sel:[0,0,0] op_sel_hi:[1,0,0]\n"
    "v_pk_fma_f32 v[86:87], v[86:87], v[28:29], v[32:33] op_sel:[0,0,0] op_sel_hi:[1,0,0]\n"
    "v_cvt_pk_bf16_f32 v192, v80, v81\n"
    "v_cvt_pk_bf16_f32 v193, v82, v83\n"
    "v_cvt_pk_bf16_f32 v194, v84, v85\n"
    "v_cvt_pk_bf16_f32 v195, v86, v87\n"
    "ds_write_b128 v36, v[192:195] offset:16384\n"
    "buffer_load_dwordx4 v2, s[32:35], s58 offen offset:0 lds\n"
    "buffer_load_dwordx4 v3, s[32:35], s58 offen offset:1024 lds\n"
    "v_pk_fma_f32 v[88:89], v[88:89], v[28:29], v[32:33] op_sel:[0,1,1] op_sel_hi:[1,1,1]\n"
    "v_pk_fma_f32 v[90:91], v[90:91], v[28:29], v[32:33] op_sel:[0,1,1] op_sel_hi:[1,1,1]\n"
    "v_pk_fma_f32 v[92:93], v[92:93], v[28:29], v[32:33] op_sel:[0,1,1] op_sel_hi:[1,1,1]\n"
    "v_pk_fma_f32 v[94:95], v[94:95], v[28:29], v[32:33] op_sel:[0,1,1] op_sel_hi:[1,1,1]\n"
    "v_cvt_pk_bf16_f32 v196, v88, v89\n"
    "v_cvt_pk_bf16_f32 v197, v90, v91\n"
    "v_cvt_pk_bf16_f32 v198, v92, v93\n"
    "v_cvt_pk_bf16_f32 v199, v94, v95\n"
    "ds_write_b128 v36, v[196:199] offset:17408\n"
    "buffer_load_dwordx4 v4, s[32:35], s58 offen offset:2048 lds\n"
    "buffer_load_dwordx4 v5, s[32:35], s58 offen offset:3072 lds\n"
    "v_pk_fma_f32 v[96:97], v[96:97], v[30:31], v[34:35] op_sel:[0,0,0] op_sel_hi:[1,0,0]\n"
    "v_pk_fma_f32 v[98:99], v[98:99], v[30:31], v[34:35] op_sel:[0,0,0] op_sel_hi:[1,0,0]\n"
    "v_pk_fma_f32 v[100:101], v[100:101], v[30:31], v[34:35] op_sel:[0,0,0] op_sel_hi:[1,0,0]\n"
    "v_pk_fma_f32 v[102:103], v[102:103], v[30:31], v[34:35] op_sel:[0,0,0] op_sel_hi:[1,0,0]\n"
    "v_cvt_pk_bf16_f32 v120, v96, v97\n"
    "v_cvt_pk_bf16_f32 v121, v98, v99\n"
    "v_cvt_pk_bf16_f32 v122, v100, v101\n"
    "v_cvt_pk_bf16_f32 v123, v102, v103\n"
    "ds_write_b128 v36, v[120:123] offset:18432\n"
    "v_pk_fma_f32 v[104:105], v[104:105], v[30:31], v[34:35] op_sel:[0,1,1] op_sel_hi:[1,1,1]\n"
    "v_pk_fma_f32 v[106:107], v[106:107], v[30:31], v[34:35] op_sel:[0,1,1] op_sel_hi:[1,1,1]\n"
    "v_pk_fma_f32 v[108:109], v[108:109], v[30:31], v[34:35] op_sel:[0,1,1] op_sel_hi:[1,1,1]\n"
    "v_pk_fma_f32 v[110:111], v[110:111], v[30:31], v[34:35] op_sel:[0,1,1] op_sel_hi:[1,1,1]\n"
    "v_cvt_pk_bf16_f32 v124, v104, v105\n"
    "v_cvt_pk_bf16_f32 v125, v106, v107\n"
    "v_cvt_pk_bf16_f32 v126, v108, v109\n"
    "v_cvt_pk_bf16_f32 v127, v110, v111\n"
    "ds_write_b128 v36, v[124:127] offset:19456\n"
    "s_waitcnt vmcnt(12) lgkmcnt(0)\n"
    "s_barrier\n"
    "ds_read_b64_tr_b16 a[0:1], v20 offset:40960\n"
    "ds_read_b64_tr_b16 a[2:3], v20 offset:41984\n"
    "ds_read_b128 v[184:187], v37 offset:0\n"
    "ds_read_b128 a[40:43], v37 offset:2048\n"
    "ds_read_b128 a[112:115], v39 offset:0\n"
    "ds_read_b128 a[184:187], v39 offset:2048\n"
    "ds_read_b64_tr_b16 a[4:5], v21 offset:40960\n"
    "ds_read_b64_tr_b16 a[6:7], v21 offset:41984\n"
    "ds_read_b64_tr_b16 a[8:9], v22 offset:40960\n"
    "ds_read_b64_tr_b16 a[10:11], v22 offset:41984\n"
    "ds_read_b128 v[188:191], v38 offset:0\n"
    "ds_read_b128 a[44:47], v38 offset:2048\n"
    "ds_read_b128 a[116:119], v40 offset:0\n"
    "ds_read_b128 a[188:191], v40 offset:2048\n"
    "ds_read_b64_tr_b16 a[12:13], v23 offset:40960\n"
    "s_waitcnt lgkmcnt(14)\n"
    "ds_read_b64_tr_b16 a[14:15], v23 offset:41984\n"
    "s_waitcnt lgkmcnt(14)\n"
    "ds_read_b64_tr_b16 a[36:37], v20 offset:49152\n"
    "s_waitcnt lgkmcnt(14)\n"
    "ds_read_b64_tr_b16 a[38:39], v20 offset:50176\n"
    "v_mfma_f32_16x16x32_bf16 v[48:51], v[184:187], a[0:3], 0\n"
    "s_add_u32 m0, s56, 65536\n"
    "s_add_u32 s58, s60, 884736\n"
    "v_mfma_f32_16x16x32_bf16 v[112:115], a[18:21], v[200:203], 0\n"
    "s_waitcnt lgkmcnt(14)\n"
    "ds_read_b128 v[192:195], v37 offset:16384\n"
    "v_mfma_f32_16x16x32_bf16 v[116:119], a[16:19], v[200:203], 0\n"
    "s_waitcnt lgkmcnt(14)\n"
    "ds_read_b128 a[48:51], v37 offset:18432\n"
    "v_mfma_f32_16x16x32_bf16 v[120:123], a[18:21], v[204:207], 0\n"
    "s_waitcnt lgkmcnt(14)\n"
    "ds_read_b128 a[120:123], v39 offset:16384\n"
    "v_mfma_f32_16x16x32_bf16 v[124:127], a[16:19], v[204:207], 0\n"
    "v_fma_f32 v112, v112, v28, v32\n"
    "v_fma_f32 v113, v113, v28, v32\n"
    "v_mfma_f32_16x16x32_bf16 v[52:55], a[40:43], a[0:3], 0\n"
    "v_fma_f32 v114, v114, v28, v32\n"
    "v_fma_f32 v115, v115, v28, v32\n"
    "v_fma_f32 v116, v116, v28, v32\n"
    "v_mfma_f32_16x16x32_bf16 v[56:59], a[112:115], a[0:3], 0\n"
    "v_fma_f32 v117, v117, v28, v32\n"
    "v_fma_f32 v118, v118, v28, v32\n"
    "v_mfma_f32_16x16x32_bf16 v[60:63], a[184:187], a[0:3], 0\n"
    "s_waitcnt lgkmcnt(14)\n"
    "ds_read_b64_tr_b16 a[0:1], v21 offset:49152\n"
    "buffer_load_dwordx4 v2, s[32:35], s58 offen offset:0 lds\n"
    "v_fma_f32 v119, v119, v28, v32\n"
    "s_waitcnt lgkmcnt(14)\n"
    "v_mfma_f32_16x16x32_bf16 v[64:67], v[184:187], a[4:7], 0\n"
    "ds_read_b64_tr_b16 a[2:3], v21 offset:50176\n"
    "v_cvt_pk_bf16_f32 v200, v112, v113\n"
    "v_cvt_pk_bf16_f32 v201, v114, v115\n"
    "v_mfma_f32_16x16x32_bf16 v[68:71], a[40:43], a[4:7], 0\n"
    "buffer_load_dwordx4 v3, s[32:35], s58 offen offset:1024 lds\n"
    "v_cvt_pk_bf16_f32 v202, v116, v117\n"
    "v_cvt_pk_bf16_f32 v203, v118, v119\n"
    "v_mfma_f32_16x16x32_bf16 v[112:115], a[18:21], v[128:131], 0\n"
    "s_waitcnt lgkmcnt(14)\n"
    "ds_write_b128 v36, v[200:203] offset:32768\n"
    "v_mfma_f32_16x16x32_bf16 v[116:119], a[16:19], v[128:131], 0\n"
    "v_fma_f32 v120, v120, v29, v33\n"
    "v_fma_f32 v121, v121, v29, v33\n"
    "v_fma_f32 v122, v122, v29, v33\n"
    "v_mfma_f32_16x16x32_bf16 v[72:75], a[112:115], a[4:7], 0\n"
    "v_fma_f32 v123, v123, v29, v33\n"
    "v_fma_f32 v124, v124, v29, v33\n"
    "v_mfma_f32_16x16x32_bf16 v[76:79], a[184:187], a[4:7], 0\n"
    "s_waitcnt lgkmcnt(14)\n"
    "ds_read_b64_tr_b16 a[4:5], v22 offset:49152\n"
    "buffer_load_dwordx4 v4, s[32:35], s58 offen offset:2048 lds\n"
    "v_fma_f32 v125, v125, v29, v33\n"
    "v_mfma_f32_16x16x32_bf16 v[80:83], v[184:187], a[8:11], 0\n"
    "s_waitcnt lgkmcnt(14)\n"
    "ds_read_b64_tr_b16 a[6:7], v22 offset:50176\n"
    "v_fma_f32 v126, v126, v29, v33\n"
    "v_fma_f32 v127, v127, v29, v33\n"
    "v_mfma_f32_16x16x32_bf16 v[84:87], a[40:43], a[8:11], 0\n"
    "s_and_b32 s59, s23, 15\n"
    "buffer_load_dwordx4 v5, s[32:35], s58 offen offset:3072 lds\n"
    "s_lshl_b32 s59, s59, 1\n"
    "s_add_u32 s59, s59, s25\n"
    "v_mfma_f32_16x16x32_bf16 v[88:91], a[112:115], a[8:11], 0\n"
    "s_mul_i32 s59, s59, 165888\n"
    "s_mul_i32 s58, s25, 294912\n"
    "s_sub_u32 s59, s59, s58\n"
    "s_add_u32 s59, s59, s61\n"
    "v_mfma_f32_16x16x32_bf16 v[92:95], a[184:187], a[8:11], 0\n"
    "s_waitcnt lgkmcnt(14)\n"
    "ds_read_b64_tr_b16 a[8:9], v23 offset:49152\n"
    "s_add_u32 s59, s59, 0\n"
    "v_min_u32 v1, 17, v0\n"
    "v_mul_u32_u24 v1, 9216, v1\n"
    "s_waitcnt lgkmcnt(11)\n"
    "v_mfma_f32_16x16x32_bf16 v[96:99], a[12:15], v[184:187], 0\n"
    "ds_read_b64_tr_b16 a[10:11], v23 offset:50176\n"
    "buffer_load_dword a23, v1, s[36:39], s59 offen offset:0\n"
    "v_cvt_pk_bf16_f32 v204, v120, v121\n"
    "v_cvt_pk_bf16_f32 v205, v122, v123\n"
    "v_mfma_f32_16x16x32_bf16 v[100:103], a[12:15], a[40:43], 0\n"
    "v_cvt_pk_bf16_f32 v206, v124, v125\n"
    "v_cvt_pk_bf16_f32 v207, v126, v127\n"
    "ds_write_b128 v36, v[204:207] offset:33792\n"
    "v_mfma_f32_16x16x32_bf16 v[120:123], a[18:21], v[132:135], 0\n"
    "ds_read_b128 a[192:195], v39 offset:18432\n"
    "v_mfma_f32_16x16x32_bf16 v[124:127], a[16:19], v[132:135], 0\n"
    "v_fma_f32 v112, v112, v30, v34\n"
    "v_fma_f32 v113, v113, v30, v34\n"
    "v_fma_f32 v114, v114, v30, v34\n"
    "v_fma_f32 v115, v115, v30, v34\n"
    "v_mfma_f32_16x16x32_bf16 v[104:107], a[12:15], a[112:115], 0\n"
    "v_fma_f32 v116, v116, v30, v34\n"
    "v_fma_f32 v117, v117, v30, v34\n"
    "v_fma_f32 v118, v118, v30, v34\n"
    "v_fma_f32 v119, v119, v30, v34\n"
    "v_cvt_pk_bf16_f32 v128, v112, v113\n"
    "v_mfma_f32_16x16x32_bf16 v[108:111], a[12:15], a[184:187], 0\n"
    "v_cvt_pk_bf16_f32 v129, v114, v115\n"
    "v_cvt_pk_bf16_f32 v130, v116, v117\n"
    "v_cvt_pk_bf16_f32 v131, v118, v119\n"
    "v_mfma_f32_16x16x32_bf16 v[112:115], a[18:21], v[208:211], 0\n"
    "ds_write_b128 v36, v[128:131] offset:34816\n"
    "v_mfma_f32_16x16x32_bf16 v[116:119], a[16:19], v[208:211], 0\n"
    "s_waitcnt lgkmcnt(13)\n"
    "v_fma_f32 v120, v120, v31, v35\n"
    "v_fma_f32 v121, v121, v31, v35\n"
    "v_fma_f32 v122, v122, v31, v35\n"
    "v_fma_f32 v123, v123, v31, v35\n"
    "v_mfma_f32_16x16x32_bf16 v[48:51], v[188:191], a[36:39], v[48:51]\n"
    "v_fma_f32 v124, v124, v31, v35\n"
    "v_fma_f32 v125, v125, v31, v35\n"
    "v_fma_f32 v126, v126, v31, v35\n"
    "v_fma_f32 v127, v127, v31, v35\n"
    "v_mfma_f32_16x16x32_bf16 v[52:55], a[44:47], a[36:39], v[52:55]\n"
    "v_cvt_pk_bf16_f32 v132, v120, v121\n"
    "v_cvt_pk_bf16_f32 v133, v122, v123\n"
    "v_cvt_pk_bf16_f32 v134, v124, v125\n"
    "v_cvt_pk_bf16_f32 v135, v126, v127\n"
    "v_mfma_f32_16x16x32_bf16 v[120:123], a[18:21], v[212:215], 0\n"
    "ds_write_b128 v36, v[132:135] offset:35840\n"
    "ds_read_b128 v[196:199], v38 offset:16384\n"
    "s_waitcnt lgkmcnt(14)\n"
    "ds_read_b128 a[52:55], v38 offset:18432\n"
    "v_mfma_f32_16x16x32_bf16 v[124:127], a[16:19], v[212:215], 0\n"
    "s_waitcnt lgkmcnt(14)\n"
    "ds_read_b128 a[124:127], v40 offset:16384\n"
    "s_waitcnt lgkmcnt(14)\n"
    "ds_read_b128 a[196:199], v40 offset:18432\n"
    "v_fma_f32 v112, v112, v28, v32\n"
    "v_fma_f32 v113, v113, v28, v32\n"
    "v_mfma_f32_16x16x32_bf16 v[56:59], a[116:119], a[36:39], v[56:59]\n"
    "v_fma_f32 v114, v114, v28, v32\n"
    "v_fma_f32 v115, v115, v28, v32\n"
    "v_mfma_f32_16x16x32_bf16 v[60:63], a[188:191], a[36:39], v[60:63]\n"
    "s_waitcnt lgkmcnt(13)\n"
    "v_fma_f32 v116, v116, v28, v32\n"
    "v_fma_f32 v117, v117, v28, v32\n"
    "v_mfma_f32_16x16x32_bf16 v[64:67], v[188:191], a[0:3], v[64:67]\n"
    "v_fma_f32 v118, v118, v28, v32\n"
    "v_fma_f32 v119, v119, v28, v32\n"
    "v_cvt_pk_bf16_f32 v208, v112, v113\n"
    "v_mfma_f32_16x16x32_bf16 v[68:71], a[44:47], a[0:3], v[68:71]\n"
    "v_cvt_pk_bf16_f32 v209, v114, v115\n"
    "v_cvt_pk_bf16_f32 v210, v116, v117\n"
    "v_mfma_f32_16x16x32_bf16 v[72:75], a[116:119], a[0:3], v[72:75]\n"
    "v_cvt_pk_bf16_f32 v211, v118, v119\n"
    "v_mfma_f32_16x16x32_bf16 v[112:115], a[18:21], v[136:139], 0\n"
    "v_mfma_f32_16x16x32_bf16 v[76:79], a[188:191], a[0:3], v[76:79]\n"
    "v_mfma_f32_16x16x32_bf16 v[116:119], a[16:19], v[136:139], 0\n"
    "s_waitcnt lgkmcnt(10)\n"
    "v_fma_f32 v120, v120, v29, v33\n"
    "v_fma_f32 v121, v121, v29, v33\n"
    "v_mfma_f32_16x16x32_bf16 v[80:83], v[188:191], a[4:7], v[80:83]\n"
    "v_fma_f32 v122, v122, v29, v33\n"
    "v_fma_f32 v123, v123, v29, v33\n"
    "v_mfma_f32_16x16x32_bf16 v[84:87], a[44:47], a[4:7], v[84:87]\n"
    "v_fma_f32 v124, v124, v29, v33\n"
    "v_fma_f32 v125, v125, v29, v33\n"
    "v_mfma_f32_16x16x32_bf16 v[88:91], a[116:119], a[4:7], v[88:91]\n"
    "v_fma_f32 v126, v126, v29, v33\n"
    "v_fma_f32 v127, v127, v29, v33\n"
    "v_cvt_pk_bf16_f32 v212, v120, v121\n"
    "v_mfma_f32_16x16x32_bf16 v[92:95], a[188:191], a[4:7], v[92:95]\n"
    "s_waitcnt vmcnt(13) lgkmcnt(0)\n"
    "s_barrier\n"
    "ds_read_b64_tr_b16 a[12:13], v24 offset:0\n"
    "ds_read_b64_tr_b16 a[14:15], v24 offset:1024\n"
    "v_mfma_f32_16x16x32_bf16 v[96:99], a[8:11], v[188:191], v[96:99]\n"
    "s_add_u32 m0, s56, 0\n"
    "ds_read_b64_tr_b16 a[36:37], v25 offset:0\n"
    "ds_read_b64_tr_b16 a[38:39], v25 offset:1024\n"
    "v_mfma_f32_16x16x32_bf16 v[100:103], a[8:11], a[44:47], v[100:103]\n"
    "s_add_u32 s58, s60, 1105920\n"
    "ds_read_b64_tr_b16 a[0:1], v26 offset:0\n"
    "v_cvt_pk_bf16_f32 v213, v122, v123\n"
    "v_mfma_f32_16x16x32_bf16 v[104:107], a[8:11], a[116:119], v[104:107]\n"
    "ds_read_b64_tr_b16 a[2:3], v26 offset:1024\n"
    "buffer_load_dwordx4 v2, s[32:35], s58 offen offset:0 lds\n"
    "v_mfma_f32_16x16x32_bf16 v[108:111], a[8:11], a[188:191], v[108:111]\n"
    "s_waitcnt lgkmcnt(4)\n"
    "ds_read_b64_tr_b16 a[4:5], v27 offset:0\n"
    "v_cvt_pk_bf16_f32 v214, v124, v125\n"
    "v_mfma_f32_16x16x32_bf16 v[48:51], v[192:195], a[12:15], v[48:51]\n"
    "ds_read_b64_tr_b16 a[6:7], v27 offset:1024\n"
    "v_cvt_pk_bf16_f32 v215, v126, v127\n"
    "v_mfma_f32_16x16x32_bf16 v[120:123], a[18:21], v[140:143], 0\n"
    "ds_write_b128 v36, v[208:211] offset:49152\n"
    "v_mfma_f32_16x16x32_bf16 v[124:127], a[16:19], v[140:143], 0\n"
    "ds_write_b128 v36, v[212:215] offset:50176\n"
    "v_mfma_f32_16x16x32_bf16 v[52:55], a[48:51], a[12:15], v[52:55]\n"
    "ds_read_b64_tr_b16 a[8:9], v24 offset:8192\n"
    "v_fma_f32 v112, v112, v30, v34\n"
    "v_fma_f32 v113, v113, v30, v34\n"
    "v_mfma_f32_16x16x32_bf16 v[56:59], a[120:123], a[12:15], v[56:59]\n"
    "ds_read_b64_tr_b16 a[10:11], v24 offset:9216\n"
    "v_fma_f32 v114, v114, v30, v34\n"
    "v_mfma_f32_16x16x32_bf16 v[60:63], a[192:195], a[12:15], v[60:63]\n"
    "s_waitcnt lgkmcnt(8)\n"
    "ds_read_b64_tr_b16 a[12:13], v25 offset:8192\n"
    "buffer_load_dwordx4 v3, s[32:35], s58 offen offset:1024 lds\n"
    "v_mfma_f32_16x16x32_bf16 v[64:67], v[192:195], a[36:39], v[64:67]\n"
    "ds_read_b64_tr_b16 a[14:15], v25 offset:9216\n"
    "v_fma_f32 v115, v115, v30, v34\n"
    "v_mfma_f32_16x16x32_bf16 v[68:71], a[48:51], a[36:39], v[68:71]\n"
    "v_fma_f32 v116, v116, v30, v34\n"
    "v_fma_f32 v117, v117, v30, v34\n"
    "v_mfma_f32_16x16x32_bf16 v[72:75], a[120:123], a[36:39], v[72:75]\n"
    "v_fma_f32 v118, v118, v30, v34\n"
    "v_fma_f32 v119, v119, v30, v34\n"
    "v_mfma_f32_16x16x32_bf16 v[76:79], a[192:195], a[36:39], v[76:79]\n"
    "s_waitcnt lgkmcnt(8)\n"
    "ds_read_b64_tr_b16 a[36:37], v26 offset:8192\n"
    "v_cvt_pk_bf16_f32 v136, v112, v113\n"
    "v_cvt_pk_bf16_f32 v137, v114, v115\n"
    "v_mfma_f32_16x16x32_bf16 v[80:83], v[192:195], a[0:3], v[80:83]\n"
    "ds_read_b64_tr_b16 a[38:39], v26 offset:9216\n"
    "buffer_load_dwordx4 v4, s[32:35], s58 offen offset:2048 lds\n"
    "v_mfma_f32_16x16x32_bf16 v[84:87], a[48:51], a[0:3], v[84:87]\n"
    "v_cvt_pk_bf16_f32 v138, v116, v117\n"
    "v_cvt_pk_bf16_f32 v139, v118, v119\n"
    "v_mfma_f32_16x16x32_bf16 v[112:115], a[18:21], v[216:219], 0\n"
    "ds_write_b128 v36, v[136:139] offset:51200\n"
    "v_mfma_f32_16x16x32_bf16 v[116:119], a[16:19], v[216:219], 0\n"
    "v_fma_f32 v120, v120, v31, v35\n"
    "v_fma_f32 v121, v121, v31, v35\n"
    "v_mfma_f32_16x16x32_bf16 v[88:91], a[120:123], a[0:3], v[88:91]\n"
    "v_fma_f32 v122, v122, v31, v35\n"
    "v_fma_f32 v123, v123, v31, v35\n"
    "v_mfma_f32_16x16x32_bf16 v[92:95], a[192:195], a[0:3], v[92:95]\n"
    "s_waitcnt lgkmcnt(9)\n"
    "ds_read_b64_tr_b16 a[0:1], v27 offset:8192\n"
    "v_fma_f32 v124, v124, v31, v35\n"
    "v_mfma_f32_16x16x32_bf16 v[96:99], a[4:7], v[192:195], v[96:99]\n"
    "ds_read_b64_tr_b16 a[2:3], v27 offset:9216\n"
    "v_fma_f32 v125, v125, v31, v35\n"
    "L_hop1:\n"
    "s_cbranch_execz L_hop5\n"
    "v_mfma_f32_16x16x32_bf16 v[100:103], a[4:7], a[48:51], v[100:103]\n"
    "buffer_load_dwordx4 v5, s[32:35], s58 offen offset:3072 lds\n"
    "v_fma_f32 v126, v126, v31, v35\n"
    "v_fma_f32 v127, v127, v31, v35\n"
    "v_mfma_f32_16x16x32_bf16 v[104:107], a[4:7], a[120:123], v[104:107]\n"
    "v_cvt_pk_bf16_f32 v140, v120, v121\n"
    "v_cvt_pk_bf16_f32 v141, v122, v123\n"
    "v_mfma_f32_16x16x32_bf16 v[108:111], a[4:7], a[192:195], v[108:111]\n"
    "v_cvt_pk_bf16_f32 v142, v124, v125\n"
    "v_cvt_pk_bf16_f32 v143, v126, v127\n"
    "v_mfma_f32_16x16x32_bf16 v[120:123], a[18:21], v[220:223], 0\n"
    "ds_write_b128 v36, v[140:143] offset:52224\n"
    "v_mfma_f32_16x16x32_bf16 v[124:127], a[16:19], v[220:223], 0\n"
    "s_waitcnt lgkmcnt(8)\n"
    "ds_read_b128 v[200:203], v37 offset:32768\n"
    "ds_read_b128 a[56:59], v37 offset:34816\n"
    "v_mfma_f32_16x16x32_bf16 v[48:51], v[196:199], a[8:11], v[48:51]\n"
    "ds_read_b128 a[128:131], v39 offset:32768\n"
    "ds_read_b128 a[200:203], v39 offset:34816\n"
    "v_mfma_f32_16x16x32_bf16 v[52:55], a[52:55], a[8:11], v[52:55]\n"
    "ds_read_b128 v[204:207], v38 offset:32768\n"
    "ds_read_b128 a[60:63], v38 offset:34816\n"
    "v_mfma_f32_16x16x32_bf16 v[56:59], a[124:127], a[8:11], v[56:59]\n"
    "ds_read_b128 a[132:135], v40 offset:32768\n"
    "s_waitcnt lgkmcnt(14)\n"
    "ds_read_b128 a[204:207], v40 offset:34816\n"
    "v_mfma_f32_16x16x32_bf16 v[60:63], a[196:199], a[8:11], v[60:63]\n"
    "s_waitcnt lgkmcnt(14)\n"
    "v_fma_f32 v112, v112, v28, v32\n"
    "v_fma_f32 v113, v113, v28, v32\n"
    "v_mfma_f32_16x16x32_bf16 v[64:67], v[196:199], a[12:15], v[64:67]\n"
    "v_fma_f32 v114, v114, v28, v32\n"
    "v_fma_f32 v115, v115, v28, v32\n"
    "v_fma_f32 v116, v116, v28, v32\n"
    "v_mfma_f32_16x16x32_bf16 v[68:71], a[52:55], a[12:15], v[68:71]\n"
    "v_fma_f32 v117, v117, v28, v32\n"
    "v_fma_f32 v118, v118, v28, v32\n"
    "v_mfma_f32_16x16x32_bf16 v[72:75], a[124:127], a[12:15], v[72:75]\n"
    "v_fma_f32 v119, v119, v28, v32\n"
    "v_cvt_pk_bf16_f32 v216, v112, v113\n"
    "v_mfma_f32_16x16x32_bf16 v[76:79], a[196:199], a[12:15], v[76:79]\n"
    "s_waitcnt lgkmcnt(12)\n"
    "v_cvt_pk_bf16_f32 v217, v114, v115\n"
    "v_cvt_pk_bf16_f32 v218, v116, v117\n"
    "v_mfma_f32_16x16x32_bf16 v[80:83], v[196:199], a[36:39], v[80:83]\n"
    "v_cvt_pk_bf16_f32 v219, v118, v119\n"
    "v_mfma_f32_16x16x32_bf16 v[112:115], a[18:21], v[144:147], 0\n"
    "v_mfma_f32_16x16x32_bf16 v[84:87], a[52:55], a[36:39], v[84:87]\n"
    "v_mfma_f32_16x16x32_bf16 v[116:119], a[16:19], v[144:147], 0\n"
    "v_fma_f32 v120, v120, v29, v33\n"
    "v_fma_f32 v121, v121, v29, v33\n"
    "v_mfma_f32_16x16x32_bf16 v[88:91], a[124:127], a[36:39], v[88:91]\n"
    "v_fma_f32 v122, v122, v29, v33\n"
    "v_fma_f32 v123, v123, v29, v33\n"
    "v_fma_f32 v124, v124, v29, v33\n"
    "v_mfma_f32_16x16x32_bf16 v[92:95], a[196:199], a[36:39], v[92:95]\n"
    "s_waitcnt vmcnt(13) lgkmcnt(0)\n"
    "s_barrier\n"
    "ds_read_b64_tr_b16 a[4:5], v24 offset:16384\n"
    "ds_read_b64_tr_b16 a[6:7], v24 offset:17408\n"
    "v_mfma_f32_16x16x32_bf16 v[96:99], a[0:3], v[196:199], v[96:99]\n"
    "s_add_u32 m0, s56, 16384\n"
    "ds_read_b64_tr_b16 a[8:9], v25 offset:16384\n"
    "ds_read_b64_tr_b16 a[10:11], v25 offset:17408\n"
    "v_mfma_f32_16x16x32_bf16 v[100:103], a[0:3], a[52:55], v[100:103]\n"
    "s_add_u32 s58, s60, 1327104\n"
    "ds_read_b64_tr_b16 a[12:13], v26 offset:16384\n"
    "v_fma_f32 v125, v125, v29, v33\n"
    "v_mfma_f32_16x16x32_bf16 v[104:107], a[0:3], a[124:127], v[104:107]\n"
    "ds_read_b64_tr_b16 a[14:15], v26 offset:17408\n"
    "buffer_load_dwordx4 v2, s[32:35], s58 offen offset:0 lds\n"
    "v_mfma_f32_16x16x32_bf16 v[108:111], a[0:3], a[196:199], v[108:111]\n"
    "s_waitcnt lgkmcnt(4)\n"
    "ds_read_b64_tr_b16 a[36:37], v27 offset:16384\n"
    "v_fma_f32 v126, v126, v29, v33\n"
    "v_mfma_f32_16x16x32_bf16 v[48:51], v[200:203], a[4:7], v[48:51]\n"
    "ds_read_b64_tr_b16 a[38:39], v27 offset:17408\n"
    "ds_write_b128 v36, v[216:219] offset:0\n"
    "v_mfma_f32_16x16x32_bf16 v[52:55], a[56:59], a[4:7], v[52:55]\n"
    "ds_read_b64_tr_b16 a[0:1], v24 offset:24576\n"
    "v_fma_f32 v127, v127, v29, v33\n"
    "v_mfma_f32_16x16x32_bf16 v[56:59], a[128:131], a[4:7], v[56:59]\n"
    "ds_read_b64_tr_b16 a[2:3], v24 offset:25600\n"
    "v_cvt_pk_bf16_f32 v220, v120, v121\n"
    "v_mfma_f32_16x16x32_bf16 v[60:63], a[200:203], a[4:7], v[60:63]\n"
    "s_waitcnt lgkmcnt(7)\n"
    "ds_read_b64_tr_b16 a[4:5], v25 offset:24576\n"
    "buffer_load_dwordx4 v3, s[32:35], s58 offen offset:1024 lds\n"
    "v_mfma_f32_16x16x32_bf16 v[64:67], v[200:203], a[8:11], v[64:67]\n"
    "ds_read_b64_tr_b16 a[6:7], v25 offset:25600\n"
    "v_cvt_pk_bf16_f32 v221, v122, v123\n"
    "v_mfma_f32_16x16x32_bf16 v[68:71], a[56:59], a[8:11], v[68:71]\n"
    "v_cvt_pk_bf16_f32 v222, v124, v125\n"
    "v_cvt_pk_bf16_f32 v223, v126, v127\n"
    "v_mfma_f32_16x16x32_bf16 v[120:123], a[18:21], v[148:151], 0\n"
    "ds_write_b128 v36, v[220:223] offset:1024\n"
    "v_mfma_f32_16x16x32_bf16 v[124:127], a[16:19], v[148:151], 0\n"
    "v_fma_f32 v112, v112, v30, v34\n"
    "v_fma_f32 v113, v113, v30, v34\n"
    "v_fma_f32 v114, v114, v30, v34\n"
    "v_mfma_f32_16x16x32_bf16 v[72:75], a[128:131], a[8:11], v[72:75]\n"
    "v_fma_f32 v115, v115, v30, v34\n"
    "v_fma_f32 v116, v116, v30, v34\n"
    "v_mfma_f32_16x16x32_bf16 v[76:79], a[200:203], a[8:11], v[76:79]\n"
    "s_waitcnt lgkmcnt(8)\n"
    "ds_read_b64_tr_b16 a[8:9], v26 offset:24576\n"
    "v_fma_f32 v117, v117, v30, v34\n"
    "v_mfma_f32_16x16x32_bf16 v[80:83], v[200:203], a[12:15], v[80:83]\n"
    "ds_read_b64_tr_b16 a[10:11], v26 offset:25600\n"
    "buffer_load_dwordx4 v4, s[32:35], s58 offen offset:2048 lds\n"
    "v_mfma_f32_16x16x32_bf16 v[84:87], a[56:59], a[12:15], v[84:87]\n"
    "v_fma_f32 v118, v118, v30, v34\n"
    "v_fma_f32 v119, v119, v30, v34\n"
    "v_mfma_f32_16x16x32_bf16 v[88:91], a[128:131], a[12:15], v[88:91]\n"
    "v_cvt_pk_bf16_f32 v144, v112, v113\n"
    "v_cvt_pk_bf16_f32 v145, v114, v115\n"
    "v_mfma_f32_16x16x32_bf16 v[92:95], a[200:203], a[12:15], v[92:95]\n"
    "ds_read_b64_tr_b16 a[12:13], v27 offset:24576\n"
    "v_cvt_pk_bf16_f32 v146, v116, v117\n"
    "v_cvt_pk_bf16_f32 v147, v118, v119\n"
    "v_mfma_f32_16x16x32_bf16 v[112:115], a[18:21], v[224:227], 0\n"
    "ds_write_b128 v36, v[144:147] offset:2048\n"
    "v_mfma_f32_16x16x32_bf16 v[116:119], a[16:19], v[224:227], 0\n"
    "s_waitcnt lgkmcnt(10)\n"
    "v_fma_f32 v120, v120, v31, v35\n"
    "v_fma_f32 v121, v121, v31, v35\n"
    "v_fma_f32 v122, v122, v31, v35\n"
    "v_mfma_f32_16x16x32_bf16 v[96:99], a[36:39], v[200:203], v[96:99]\n"
    "ds_read_b64_tr_b16 a[14:15], v27 offset:25600\n"
    "v_fma_f32 v123, v123, v31, v35\n"
    "v_mfma_f32_16x16x32_bf16 v[100:103], a[36:39], a[56:59], v[100:103]\n"
    "buffer_load_dwordx4 v5, s[32:35], s58 offen offset:3072 lds\n"
    "v_fma_f32 v124, v124, v31, v35\n"
    "v_mfma_f32_16x16x32_bf16 v[104:107], a[36:39], a[128:131], v[104:107]\n"
    "v_fma_f32 v125, v125, v31, v35\n"
    "v_fma_f32 v126, v126, v31, v35\n"
    "v_mfma_f32_16x16x32_bf16 v[108:111], a[36:39], a[200:203], v[108:111]\n"
    "s_waitcnt lgkmcnt(8)\n"
    "v_fma_f32 v127, v127, v31, v35\n"
    "v_cvt_pk_bf16_f32 v148, v120, v121\n"
    "v_mfma_f32_16x16x32_bf16 v[48:51], v[204:207], a[0:3], v[48:51]\n"
    "v_cvt_pk_bf16_f32 v149, v122, v123\n"
    "v_cvt_pk_bf16_f32 v150, v124, v125\n"
    "v_cvt_pk_bf16_f32 v151, v126, v127\n"
    "v_mfma_f32_16x16x32_bf16 v[120:123], a[18:21], v[228:231], 0\n"
    "ds_write_b128 v36, v[148:151] offset:3072\n"
    "ds_read_b128 v[208:211], v37 offset:49152\n"
    "v_mfma_f32_16x16x32_bf16 v[124:127], a[16:19], v[228:231], 0\n"
    "ds_read_b128 a[64:67], v37 offset:51200\n"
    "ds_read_b128 a[136:139], v39 offset:49152\n"
    "ds_read_b128 a[208:211], v39 offset:51200\n"
    "v_mfma_f32_16x16x32_bf16 v[52:55], a[60:63], a[0:3], v[52:55]\n"
    "ds_read_b128 v[212:215], v38 offset:49152\n"
    "ds_read_b128 a[68:71], v38 offset:51200\n"
    "v_mfma_f32_16x16x32_bf16 v[56:59], a[132:135], a[0:3], v[56:59]\n"
    "s_waitcnt lgkmcnt(14)\n"
    "ds_read_b128 a[140:143], v40 offset:49152\n"
    "s_waitcnt lgkmcnt(14)\n"
    "ds_read_b128 a[212:215], v40 offset:51200\n"
    "v_mfma_f32_16x16x32_bf16 v[60:63], a[204:207], a[0:3], v[60:63]\n"
    "v_fma_f32 v112, v112, v28, v32\n"
    "v_fma_f32 v113, v113, v28, v32\n"
    "v_mfma_f32_16x16x32_bf16 v[64:67], v[204:207], a[4:7], v[64:67]\n"
    "v_fma_f32 v114, v114, v28, v32\n"
    "v_fma_f32 v115, v115, v28, v32\n"
    "v_mfma_f32_16x16x32_bf16 v[68:71], a[60:63], a[4:7], v[68:71]\n"
    "v_fma_f32 v116, v116, v28, v32\n"
    "v_fma_f32 v117, v117, v28, v32\n"
    "v_mfma_f32_16x16x32_bf16 v[72:75], a[132:135], a[4:7], v[72:75]\n"
    "v_fma_f32 v118, v118, v28, v32\n"
    "v_fma_f32 v119, v119, v28, v32\n"
    "v_cvt_pk_bf16_f32 v224, v112, v113\n"
    "v_mfma_f32_16x16x32_bf16 v[76:79], a[204:207], a[4:7], v[76:79]\n"
    "s_waitcnt lgkmcnt(12)\n"
    "v_cvt_pk_bf16_f32 v225, v114, v115\n"
    "v_cvt_pk_bf16_f32 v226, v116, v117\n"
    "v_mfma_f32_16x16x32_bf16 v[80:83], v[204:207], a[8:11], v[80:83]\n"
    "v_cvt_pk_bf16_f32 v227, v118, v119\n"
    "v_mfma_f32_16x16x32_bf16 v[112:115], a[18:21], v[152:155], 0\n"
    "v_mfma_f32_16x16x32_bf16 v[84:87], a[60:63], a[8:11], v[84:87]\n"
    "v_mfma_f32_16x16x32_bf16 v[116:119], a[16:19], v[152:155], 0\n"
    "v_fma_f32 v120, v120, v29, v33\n"
    "v_fma_f32 v121, v121, v29, v33\n"
    "v_mfma_f32_16x16x32_bf16 v[88:91], a[132:135], a[8:11], v[88:91]\n"
    "v_fma_f32 v122, v122, v29, v33\n"
    "v_fma_f32 v123, v123, v29, v33\n"
    "v_mfma_f32_16x16x32_bf16 v[92:95], a[204:207], a[8:11], v[92:95]\n"
    "s_waitcnt vmcnt(13) lgkmcnt(0)\n"
    "s_barrier\n"
    "ds_read_b64_tr_b16 a[36:37], v24 offset:32768\n"
    "ds_read_b64_tr_b16 a[38:39], v24 offset:33792\n"
    "v_mfma_f32_16x16x32_bf16 v[96:99], a[12:15], v[204:207], v[96:99]\n"
    "s_add_u32 m0, s56, 32768\n"
    "ds_read_b64_tr_b16 a[0:1], v25 offset:32768\n"
    "ds_read_b64_tr_b16 a[2:3], v25 offset:33792\n"
    "s_add_u32 s58, s60, 1548288\n"
    "v_mfma_f32_16x16x32_bf16 v[100:103], a[12:15], a[60:63], v[100:103]\n"
    "ds_read_b64_tr_b16 a[4:5], v26 offset:32768\n"
    "v_fma_f32 v124, v124, v29, v33\n"
    "v_mfma_f32_16x16x32_bf16 v[104:107], a[12:15], a[132:135], v[104:107]\n"
    "ds_read_b64_tr_b16 a[6:7], v26 offset:33792\n"
    "buffer_load_dwordx4 v2, s[32:35], s58 offen offset:0 lds\n"
    "v_mfma_f32_16x16x32_bf16 v[108:111], a[12:15], a[204:207], v[108:111]\n"
    "s_waitcnt lgkmcnt(4)\n"
    "ds_read_b64_tr_b16 a[8:9], v27 offset:32768\n"
    "v_fma_f32 v125, v125, v29, v33\n"
    "v_mfma_f32_16x16x32_bf16 v[48:51], v[208:211], a[36:39], v[48:51]\n"
    "ds_read_b64_tr_b16 a[10:11], v27 offset:33792\n"
    "v_fma_f32 v126, v126, v29, v33\n"
    "v_mfma_f32_16x16x32_bf16 v[52:55], a[64:67], a[36:39], v[52:55]\n"
    "ds_read_b64_tr_b16 a[12:13], v24 offset:40960\n"
    "ds_write_b128 v36, v[224:227] offset:16384\n"
    "v_mfma_f32_16x16x32_bf16 v[56:59], a[136:139], a[36:39], v[56:59]\n"
    "ds_read_b64_tr_b16 a[14:15], v24 offset:41984\n"
    "v_fma_f32 v127, v127, v29, v33\n"
    "v_mfma_f32_16x16x32_bf16 v[60:63], a[208:211], a[36:39], v[60:63]\n"
    "s_waitcnt lgkmcnt(7)\n"
    "ds_read_b64_tr_b16 a[36:37], v25 offset:40960\n"
    "buffer_load_dwordx4 v3, s[32:35], s58 offen offset:1024 lds\n"
    "v_mfma_f32_16x16x32_bf16 v[64:67], v[208:211], a[0:3], v[64:67]\n"
    "ds_read_b64_tr_b16 a[38:39], v25 offset:41984\n"
    "v_cvt_pk_bf16_f32 v228, v120, v121\n"
    "v_mfma_f32_16x16x32_bf16 v[68:71], a[64:67], a[0:3], v[68:71]\n"
    "v_cvt_pk_bf16_f32 v229, v122, v123\n"
    "v_cvt_pk_bf16_f32 v230, v124, v125\n"
    "v_mfma_f32_16x16x32_bf16 v[72:75], a[136:139], a[0:3], v[72:75]\n"
    "v_cvt_pk_bf16_f32 v231, v126, v127\n"
    "ds_write_b128 v36, v[228:231] offset:17408\n"
    "v_mfma_f32_16x16x32_bf16 v[120:123], a[18:21], v[156:159], 0\n"
    "ds_read_b128 v[216:219], v37 offset:0\n"
    "v_mfma_f32_16x16x32_bf16 v[124:127], a[16:19], v[156:159], 0\n"
    "v_fma_f32 v112, v112, v30, v34\n"
    "v_fma_f32 v113, v113, v30, v34\n"
    "v_fma_f32 v114, v114, v30, v34\n"
    "v_mfma_f32_16x16x32_bf16 v[76:79], a[208:211], a[0:3], v[76:79]\n"
    "s_waitcnt lgkmcnt(9)\n"
    "ds_read_b64_tr_b16 a[0:1], v26 offset:40960\n"
    "v_fma_f32 v115, v115, v30, v34\n"
    "v_mfma_f32_16x16x32_bf16 v[80:83], v[208:211], a[4:7], v[80:83]\n"
    "ds_read_b64_tr_b16 a[2:3], v26 offset:41984\n"
    "buffer_load_dwordx4 v4, s[32:35], s58 offen offset:2048 lds\n"
    "v_mfma_f32_16x16x32_bf16 v[84:87], a[64:67], a[4:7], v[84:87]\n"
    "v_fma_f32 v116, v116, v30, v34\n"
    "v_fma_f32 v117, v117, v30, v34\n"
    "v_mfma_f32_16x16x32_bf16 v[88:91], a[136:139], a[4:7], v[88:91]\n"
    "v_fma_f32 v118, v118, v30, v34\n"
    "v_fma_f32 v119, v119, v30, v34\n"
    "v_mfma_f32_16x16x32_bf16 v[92:95], a[208:211], a[4:7], v[92:95]\n"
    "s_waitcnt lgkmcnt(9)\n"
    "ds_read_b64_tr_b16 a[4:5], v27 offset:40960\n"
    "v_cvt_pk_bf16_f32 v152, v112, v113\n"
    "v_cvt_pk_bf16_f32 v153, v114, v115\n"
    "v_mfma_f32_16x16x32_bf16 v[96:99], a[8:11], v[208:211], v[96:99]\n"
    "ds_read_b64_tr_b16 a[6:7], v27 offset:41984\n"
    "v_cvt_pk_bf16_f32 v154, v116, v117\n"
    "v_mfma_f32_16x16x32_bf16 v[100:103], a[8:11], a[64:67], v[100:103]\n"
    "buffer_load_dwordx4 v5, s[32:35], s58 offen offset:3072 lds\n"
    "v_cvt_pk_bf16_f32 v155, v118, v119\n"
    "v_mfma_f32_16x16x32_bf16 v[112:115], a[18:21], v[232:235], 0\n"
    "ds_write_b128 v36, v[152:155] offset:18432\n"
    "v_mfma_f32_16x16x32_bf16 v[116:119], a[16:19], v[232:235], 0\n"
    "v_fma_f32 v120, v120, v31, v35\n"
    "v_fma_f32 v121, v121, v31, v35\n"
    "v_mfma_f32_16x16x32_bf16 v[104:107], a[8:11], a[136:139], v[104:107]\n"
    "v_fma_f32 v122, v122, v31, v35\n"
    "v_fma_f32 v123, v123, v31, v35\n"
    "v_fma_f32 v124, v124, v31, v35\n"
    "v_mfma_f32_16x16x32_bf16 v[108:111], a[8:11], a[208:211], v[108:111]\n"
    "s_waitcnt lgkmcnt(9)\n"
    "v_fma_f32 v125, v125, v31, v35\n"
    "v_fma_f32 v126, v126, v31, v35\n"
    "v_mfma_f32_16x16x32_bf16 v[48:51], v[212:215], a[12:15], v[48:51]\n"
    "v_fma_f32 v127, v127, v31, v35\n"
    "v_cvt_pk_bf16_f32 v156, v120, v121\n"
    "v_mfma_f32_16x16x32_bf16 v[52:55], a[68:71], a[12:15], v[52:55]\n"
    "v_cvt_pk_bf16_f32 v157, v122, v123\n"
    "v_cvt_pk_bf16_f32 v158, v124, v125\n"
    "v_cvt_pk_bf16_f32 v159, v126, v127\n"
    "v_mfma_f32_16x16x32_bf16 v[120:123], a[18:21], v[236:239], 0\n"
    "ds_write_b128 v36, v[156:159] offset:19456\n"
    "ds_read_b128 a[72:75], v37 offset:2048\n"
    "v_mfma_f32_16x16x32_bf16 v[124:127], a[16:19], v[236:239], 0\n"
    "ds_read_b128 a[144:147], v39 offset:0\n"
    "ds_read_b128 a[216:219], v39 offset:2048\n"
    "ds_read_b128 v[220:223], v38 offset:0\n"
    "v_mfma_f32_16x16x32_bf16 v[56:59], a[140:143], a[12:15], v[56:59]\n"
    "ds_read_b128 a[76:79], v38 offset:2048\n"
    "s_waitcnt lgkmcnt(14)\n"
    "ds_read_b128 a[148:151], v40 offset:0\n"
    "s_waitcnt lgkmcnt(14)\n"
    "ds_read_b128 a[220:223], v40 offset:2048\n"
    "v_mfma_f32_16x16x32_bf16 v[60:63], a[212:215], a[12:15], v[60:63]\n"
    "v_fma_f32 v112, v112, v28, v32\n"
    "v_fma_f32 v113, v113, v28, v32\n"
    "v_mfma_f32_16x16x32_bf16 v[64:67], v[212:215], a[36:39], v[64:67]\n"
    "v_fma_f32 v114, v114, v28, v32\n"
    "v_fma_f32 v115, v115, v28, v32\n"
    "v_mfma_f32_16x16x32_bf16 v[68:71], a[68:71], a[36:39], v[68:71]\n"
    "v_fma_f32 v116, v116, v28, v32\n"
    "v_fma_f32 v117, v117, v28, v32\n"
    "v_mfma_f32_16x16x32_bf16 v[72:75], a[140:143], a[36:39], v[72:75]\n"
    "v_fma_f32 v118, v118, v28, v32\n"
    "v_fma_f32 v119, v119, v28, v32\n"
    "v_mfma_f32_16x16x32_bf16 v[76:79], a[212:215], a[36:39], v[76:79]\n"
    "s_waitcnt lgkmcnt(11)\n"
    "v_cvt_pk_bf16_f32 v232, v112, v113\n"
    "v_cvt_pk_bf16_f32 v233, v114, v115\n"
    "v_mfma_f32_16x16x32_bf16 v[80:83], v[212:215], a[0:3], v[80:83]\n"
    "v_cvt_pk_bf16_f32 v234, v116, v117\n"
    "v_cvt_pk_bf16_f32 v235, v118, v119\n"
    "v_mfma_f32_16x16x32_bf16 v[112:115], a[18:21], v[160:163], 0\n"
    "v_mfma_f32_16x16x32_bf16 v[84:87], a[68:71], a[0:3], v[84:87]\n"
    "v_mfma_f32_16x16x32_bf16 v[116:119], a[16:19], v[160:163], 0\n"
    "v_fma_f32 v120, v120, v29, v33\n"
    "v_fma_f32 v121, v121, v29, v33\n"
    "v_mfma_f32_16x16x32_bf16 v[88:91], a[140:143], a[0:3], v[88:91]\n"
    "v_fma_f32 v122, v122, v29, v33\n"
    "v_fma_f32 v123, v123, v29, v33\n"
    "v_mfma_f32_16x16x32_bf16 v[92:95], a[212:215], a[0:3], v[92:95]\n"
    "s_waitcnt vmcnt(13) lgkmcnt(0)\n"
    "s_barrier\n"
    "ds_read_b64_tr_b16 a[8:9], v24 offset:49152\n"
    "ds_read_b64_tr_b16 a[10:11], v24 offset:50176\n"
    "v_mfma_f32_16x16x32_bf16 v[96:99], a[4:7], v[212:215], v[96:99]\n"
    "s_add_u32 m0, s56, 49152\n"
    "ds_read_b64_tr_b16 a[12:13], v25 offset:49152\n"
    "ds_read_b64_tr_b16 a[14:15], v25 offset:50176\n"
    "v_mfma_f32_16x16x32_bf16 v[100:103], a[4:7], a[68:71], v[100:103]\n"
    "s_add_u32 s58, s60, 1769472\n"
    "ds_read_b64_tr_b16 a[36:37], v26 offset:49152\n"
    "ds_write_b128 v36, v[232:235] offset:32768\n"
    "v_mfma_f32_16x16x32_bf16 v[104:107], a[4:7], a[140:143], v[104:107]\n"
    "ds_read_b64_tr_b16 a[38:39], v26 offset:50176\n"
    "buffer_load_dwordx4 v2, s[32:35], s58 offen offset:0 lds\n"
    "v_mfma_f32_16x16x32_bf16 v[108:111], a[4:7], a[212:215], v[108:111]\n"
    "s_waitcnt lgkmcnt(5)\n"
    "ds_read_b64_tr_b16 a[0:1], v27 offset:49152\n"
    "v_fma_f32 v124, v124, v29, v33\n"
    "v_mfma_f32_16x16x32_bf16 v[48:51], v[216:219], a[8:11], v[48:51]\n"
    "ds_read_b64_tr_b16 a[2:3], v27 offset:50176\n"
    "v_fma_f32 v125, v125, v29, v33\n"
    "v_mfma_f32_16x16x32_bf16 v[52:55], a[72:75], a[8:11], v[52:55]\n"
    "ds_read_b64_tr_b16 a[4:5], v24 offset:57344\n"
    "v_fma_f32 v126, v126, v29, v33\n"
    "v_fma_f32 v127, v127, v29, v33\n"
    "v_mfma_f32_16x16x32_bf16 v[56:59], a[144:147], a[8:11], v[56:59]\n"
    "ds_read_b64_tr_b16 a[6:7], v24 offset:58368\n"
    "v_cvt_pk_bf16_f32 v236, v120, v121\n"
    "v_mfma_f32_16x16x32_bf16 v[60:63], a[216:219], a[8:11], v[60:63]\n"
    "s_waitcnt lgkmcnt(7)\n"
    "ds_read_b64_tr_b16 a[8:9], v25 offset:57344\n"
    "buffer_load_dwordx4 v3, s[32:35], s58 offen offset:1024 lds\n"
    "v_mfma_f32_16x16x32_bf16 v[64:67], v[216:219], a[12:15], v[64:67]\n"
    "ds_read_b64_tr_b16 a[10:11], v25 offset:58368\n"
    "v_cvt_pk_bf16_f32 v237, v122, v123\n"
    "v_mfma_f32_16x16x32_bf16 v[68:71], a[72:75], a[12:15], v[68:71]\n"
    "v_cvt_pk_bf16_f32 v238, v124, v125\n"
    "v_cvt_pk_bf16_f32 v239, v126, v127\n"
    "v_mfma_f32_16x16x32_bf16 v[120:123], a[18:21], v[164:167], 0\n"
    "ds_write_b128 v36, v[236:239] offset:33792\n"
    "v_mfma_f32_16x16x32_bf16 v[124:127], a[16:19], v[164:167], 0\n"
    "v_fma_f32 v112, v112, v30, v34\n"
    "v_fma_f32 v113, v113, v30, v34\n"
    "v_mfma_f32_16x16x32_bf16 v[72:75], a[144:147], a[12:15], v[72:75]\n"
    "v_fma_f32 v114, v114, v30, v34\n"
    "v_fma_f32 v115, v115, v30, v34\n"
    "v_fma_f32 v116, v116, v30, v34\n"
    "v_mfma_f32_16x16x32_bf16 v[76:79], a[216:219], a[12:15], v[76:79]\n"
    "s_waitcnt lgkmcnt(7)\n"
    "ds_read_b64_tr_b16 a[12:13], v26 offset:57344\n"
    "v_fma_f32 v117, v117, v30, v34\n"
    "v_mfma_f32_16x16x32_bf16 v[80:83], v[216:219], a[36:39], v[80:83]\n"
    "ds_read_b64_tr_b16 a[14:15], v26 offset:58368\n"
    "buffer_load_dwordx4 v4, s[32:35], s58 offen offset:2048 lds\n"
    "v_mfma_f32_16x16x32_bf16 v[84:87], a[72:75], a[36:39], v[84:87]\n"
    "v_fma_f32 v118, v118, v30, v34\n"
    "v_fma_f32 v119, v119, v30, v34\n"
    "v_mfma_f32_16x16x32_bf16 v[88:91], a[144:147], a[36:39], v[88:91]\n"
    "v_cvt_pk_bf16_f32 v160, v112, v113\n"
    "v_cvt_pk_bf16_f32 v161, v114, v115\n"
    "v_mfma_f32_16x16x32_bf16 v[92:95], a[216:219], a[36:39], v[92:95]\n"
    "ds_read_b64_tr_b16 a[36:37], v27 offset:57344\n"
    "v_cvt_pk_bf16_f32 v162, v116, v117\n"
    "v_cvt_pk_bf16_f32 v163, v118, v119\n"
    "v_mfma_f32_16x16x32_bf16 v[112:115], a[18:21], v[240:243], 0\n"
    "ds_write_b128 v36, v[160:163] offset:34816\n"
    "v_mfma_f32_16x16x32_bf16 v[116:119], a[16:19], v[240:243], 0\n"
    "s_waitcnt lgkmcnt(9)\n"
    "v_fma_f32 v120, v120, v31, v35\n"
    "v_fma_f32 v121, v121, v31, v35\n"
    "v_fma_f32 v122, v122, v31, v35\n"
    "v_mfma_f32_16x16x32_bf16 v[96:99], a[0:3], v[216:219], v[96:99]\n"
    "ds_read_b64_tr_b16 a[38:39], v27 offset:58368\n"
    "v_fma_f32 v123, v123, v31, v35\n"
    "v_mfma_f32_16x16x32_bf16 v[100:103], a[0:3], a[72:75], v[100:103]\n"
    "buffer_load_dwordx4 v5, s[32:35], s58 offen offset:3072 lds\n"
    "v_fma_f32 v124, v124, v31, v35\n"
    "v_mfma_f32_16x16x32_bf16 v[104:107], a[0:3], a[144:147], v[104:107]\n"
    "v_fma_f32 v125, v125, v31, v35\n"
    "v_fma_f32 v126, v126, v31, v35\n"
    "v_mfma_f32_16x16x32_bf16 v[108:111], a[0:3], a[216:219], v[108:111]\n"
    "s_waitcnt lgkmcnt(8)\n"
    "v_fma_f32 v127, v127, v31, v35\n"
    "v_cvt_pk_bf16_f32 v164, v120, v121\n"
    "v_mfma_f32_16x16x32_bf16 v[48:51], v[220:223], a[4:7], v[48:51]\n"
    "v_cvt_pk_bf16_f32 v165, v122, v123\n"
    "v_cvt_pk_bf16_f32 v166, v124, v125\n"
    "v_cvt_pk_bf16_f32 v167, v126, v127\n"
    "v_mfma_f32_16x16x32_bf16 v[120:123], a[18:21], v[244:247], 0\n"
    "ds_write_b128 v36, v[164:167] offset:35840\n"
    "ds_read_b128 v[224:227], v37 offset:16384\n"
    "v_mfma_f32_16x16x32_bf16 v[124:127], a[16:19], v[244:247], 0\n"
    "ds_read_b128 a[80:83], v37 offset:18432\n"
    "ds_read_b128 a[152:155], v39 offset:16384\n"
    "ds_read_b128 a[224:227], v39 offset:18432\n"
    "v_mfma_f32_16x16x32_bf16 v[52:55], a[76:79], a[4:7], v[52:55]\n"
    "v_accvgpr_read_b32 v2, a32\n"
    "v_accvgpr_read_b32 v3, a33\n"
    "v_mfma_f32_16x16x32_bf16 v[56:59], a[148:151], a[4:7], v[56:59]\n"
    "v_accvgpr_read_b32 v4, a34\n"
    "v_accvgpr_read_b32 v5, a35\n"
    "v_mfma_f32_16x16x32_bf16 v[60:63], a[220:223], a[4:7], v[60:63]\n"
    "s_waitcnt lgkmcnt(11)\n"
    "ds_read_b128 v[228:231], v38 offset:16384\n"
    "ds_read_b128 a[84:87], v38 offset:18432\n"
    "v_mfma_f32_16x16x32_bf16 v[64:67], v[220:223], a[8:11], v[64:67]\n"
    "ds_read_b128 a[156:159], v40 offset:16384\n"
    "ds_read_b128 a[228:231], v40 offset:18432\n"
    "v_mfma_f32_16x16x32_bf16 v[68:71], a[76:79], a[8:11], v[68:71]\n"
    "buffer_load_dword v42, v18, s[48:51], 0 offen offset:128 sc1\n"
    "v_fma_f32 v112, v112, v28, v32\n"
    "v_mfma_f32_16x16x32_bf16 v[72:75], a[148:151], a[8:11], v[72:75]\n"
    "v_fma_f32 v113, v113, v28, v32\n"
    "v_fma_f32 v114, v114, v28, v32\n"
    "v_mfma_f32_16x16x32_bf16 v[76:79], a[220:223], a[8:11], v[76:79]\n"
    "s_waitcnt lgkmcnt(12)\n"
    "v_fma_f32 v115, v115, v28, v32\n"
    "v_fma_f32 v116, v116, v28, v32\n"
    "v_fma_f32 v117, v117, v28, v32\n"
    "v_mfma_f32_16x16x32_bf16 v[80:83], v[220:223], a[12:15], v[80:83]\n"
    "v_fma_f32 v118, v118, v28, v32\n"
    "v_fma_f32 v119, v119, v28, v32\n"
    "v_mfma_f32_16x16x32_bf16 v[84:87], a[76:79], a[12:15], v[84:87]\n"
    "v_cvt_pk_bf16_f32 v240, v112, v113\n"
    "v_cvt_pk_bf16_f32 v241, v114, v115\n"
    "v_mfma_f32_16x16x32_bf16 v[88:91], a[148:151], a[12:15], v[88:91]\n"
    "v_cvt_pk_bf16_f32 v242, v116, v117\n"
    "v_cvt_pk_bf16_f32 v243, v118, v119\n"
    "v_mfma_f32_16x16x32_bf16 v[112:115], a[18:21], v[168:171], 0\n"
    "v_mfma_f32_16x16x32_bf16 v[92:95], a[220:223], a[12:15], v[92:95]\n"
    "s_waitcnt vmcnt(13) lgkmcnt(0)\n"
    "s_barrier\n"
    "ds_read_b64_tr_b16 a[0:1], v20 offset:40960\n"
    "ds_read_b64_tr_b16 a[2:3], v20 offset:41984\n"
    "v_mfma_f32_16x16x32_bf16 v[116:119], a[16:19], v[168:171], 0\n"
    "s_add_u32 m0, s56, 65536\n"
    "s_add_u32 s58, s61, 0\n"
    "v_fma_f32 v120, v120, v29, v33\n"
    "v_mfma_f32_16x16x32_bf16 v[96:99], a[36:39], v[220:223], v[96:99]\n"
    "ds_read_b64_tr_b16 a[4:5], v21 offset:40960\n"
    "ds_read_b64_tr_b16 a[6:7], v21 offset:41984\n"
    "v_fma_f32 v121, v121, v29, v33\n"
    "v_mfma_f32_16x16x32_bf16 v[100:103], a[36:39], a[76:79], v[100:103]\n"
    "ds_read_b64_tr_b16 a[8:9], v22 offset:40960\n"
    "v_fma_f32 v122, v122, v29, v33\n"
    "v_mfma_f32_16x16x32_bf16 v[104:107], a[36:39], a[148:151], v[104:107]\n"
    "ds_read_b64_tr_b16 a[10:11], v22 offset:41984\n"
    "buffer_load_dwordx4 v2, s[36:39], s58 offen offset:0 lds\n"
    "v_mfma_f32_16x16x32_bf16 v[108:111], a[36:39], a[220:223], v[108:111]\n"
    "s_waitcnt lgkmcnt(4)\n"
    "ds_read_b64_tr_b16 a[12:13], v23 offset:40960\n"
    "v_fma_f32 v123, v123, v29, v33\n"
    "v_mfma_f32_16x16x32_bf16 v[48:51], v[224:227], a[0:3], v[48:51]\n"
    "ds_read_b64_tr_b16 a[14:15], v23 offset:41984\n"
    "v_fma_f32 v124, v124, v29, v33\n"
    "v_mfma_f32_16x16x32_bf16 v[52:55], a[80:83], a[0:3], v[52:55]\n"
    "ds_read_b64_tr_b16 a[36:37], v20 offset:49152\n"
    "ds_write_b128 v36, v[240:243] offset:49152\n"
    "v_mfma_f32_16x16x32_bf16 v[56:59], a[152:155], a[0:3], v[56:59]\n"
    "ds_read_b64_tr_b16 a[38:39], v20 offset:50176\n"
    "v_fma_f32 v125, v125, v29, v33\n"
    "v_mfma_f32_16x16x32_bf16 v[60:63], a[224:227], a[0:3], v[60:63]\n"
    "s_waitcnt lgkmcnt(7)\n"
    "ds_read_b64_tr_b16 a[0:1], v21 offset:49152\n"
    "buffer_load_dwordx4 v3, s[36:39], s58 offen offset:1024 lds\n"
    "v_mfma_f32_16x16x32_bf16 v[64:67], v[224:227], a[4:7], v[64:67]\n"
    "ds_read_b64_tr_b16 a[2:3], v21 offset:50176\n"
    "v_fma_f32 v126, v126, v29, v33\n"
    "v_mfma_f32_16x16x32_bf16 v[68:71], a[80:83], a[4:7], v[68:71]\n"
    "v_fma_f32 v127, v127, v29, v33\n"
    "v_cvt_pk_bf16_f32 v244, v120, v121\n"
    "v_mfma_f32_16x16x32_bf16 v[72:75], a[152:155], a[4:7], v[72:75]\n"
    "v_cvt_pk_bf16_f32 v245, v122, v123\n"
    "v_cvt_pk_bf16_f32 v246, v124, v125\n"
    "v_cvt_pk_bf16_f32 v247, v126, v127\n"
    "v_mfma_f32_16x16x32_bf16 v[120:123], a[18:21], v[172:175], 0\n"
    "ds_write_b128 v36, v[244:247] offset:50176\n"
    "v_mfma_f32_16x16x32_bf16 v[124:127], a[16:19], v[172:175], 0\n"
    "v_fma_f32 v112, v112, v30, v34\n"
    "v_fma_f32 v113, v113, v30, v34\n"
    "v_fma_f32 v114, v114, v30, v34\n"
    "v_mfma_f32_16x16x32_bf16 v[76:79], a[224:227], a[4:7], v[76:79]\n"
    "s_waitcnt lgkmcnt(8)\n"
    "ds_read_b64_tr_b16 a[4:5], v22 offset:49152\n"
    "v_fma_f32 v115, v115, v30, v34\n"
    "v_mfma_f32_16x16x32_bf16 v[80:83], v[224:227], a[8:11], v[80:83]\n"
    "ds_read_b64_tr_b16 a[6:7], v22 offset:50176\n"
    "buffer_load_dwordx4 v4, s[36:39], s58 offen offset:2048 lds\n"
    "v_mfma_f32_16x16x32_bf16 v[84:87], a[80:83], a[8:11], v[84:87]\n"
    "v_fma_f32 v116, v116, v30, v34\n"
    "v_fma_f32 v117, v117, v30, v34\n"
    "v_mfma_f32_16x16x32_bf16 v[88:91], a[152:155], a[8:11], v[88:91]\n"
    "v_fma_f32 v118, v118, v30, v34\n"
    "v_fma_f32 v119, v119, v30, v34\n"
    "v_mfma_f32_16x16x32_bf16 v[92:95], a[224:227], a[8:11], v[92:95]\n"
    "s_waitcnt lgkmcnt(8)\n"
    "ds_read_b64_tr_b16 a[8:9], v23 offset:49152\n"
    "v_cvt_pk_bf16_f32 v168, v112, v113\n"
    "v_cvt_pk_bf16_f32 v169, v114, v115\n"
    "v_mfma_f32_16x16x32_bf16 v[96:99], a[12:15], v[224:227], v[96:99]\n"
    "ds_read_b64_tr_b16 a[10:11], v23 offset:50176\n"
    "v_cvt_pk_bf16_f32 v170, v116, v117\n"
    "v_mfma_f32_16x16x32_bf16 v[100:103], a[12:15], a[80:83], v[100:103]\n"
    "buffer_load_dwordx4 v5, s[36:39], s58 offen offset:3072 lds\n"
    "v_cvt_pk_bf16_f32 v171, v118, v119\n"
    "v_mfma_f32_16x16x32_bf16 v[112:115], a[18:21], v[248:251], 0\n"
    "ds_write_b128 v36, v[168:171] offset:51200\n"
    "v_mfma_f32_16x16x32_bf16 v[116:119], a[16:19], v[248:251], 0\n"
    "v_fma_f32 v120, v120, v31, v35\n"
    "v_fma_f32 v121, v121, v31, v35\n"
    "v_mfma_f32_16x16x32_bf16 v[104:107], a[12:15], a[152:155], v[104:107]\n"
    "L_hop2:\n"
    "s_cbranch_execz L_hop6\n"
    "v_fma_f32 v122, v122, v31, v35\n"
    "v_fma_f32 v123, v123, v31, v35\n"
    "v_fma_f32 v124, v124, v31, v35\n"
    "v_mfma_f32_16x16x32_bf16 v[108:111], a[12:15], a[224:227], v[108:111]\n"
    "s_waitcnt lgkmcnt(8)\n"
    "v_fma_f32 v125, v125, v31, v35\n"
    "v_fma_f32 v126, v126, v31, v35\n"
    "v_mfma_f32_16x16x32_bf16 v[48:51], v[228:231], a[36:39], v[48:51]\n"
    "v_fma_f32 v127, v127, v31, v35\n"
    "v_cvt_pk_bf16_f32 v172, v120, v121\n"
    "v_mfma_f32_16x16x32_bf16 v[52:55], a[84:87], a[36:39], v[52:55]\n"
    "v_cvt_pk_bf16_f32 v173, v122, v123\n"
    "v_cvt_pk_bf16_f32 v174, v124, v125\n"
    "v_cvt_pk_bf16_f32 v175, v126, v127\n"
    "v_mfma_f32_16x16x32_bf16 v[120:123], a[18:21], v[252:255], 0\n"
    "ds_write_b128 v36, v[172:175] offset:52224\n"
    "ds_read_b128 v[232:235], v37 offset:32768\n"
    "v_mfma_f32_16x16x32_bf16 v[124:127], a[16:19], v[252:255], 0\n"
    "ds_read_b128 a[88:91], v37 offset:34816\n"
    "ds_read_b128 a[160:163], v39 offset:32768\n"
    "ds_read_b128 a[232:235], v39 offset:34816\n"
    "v_mfma_f32_16x16x32_bf16 v[56:59], a[156:159], a[36:39], v[56:59]\n"
    "ds_read_b128 v[236:239], v38 offset:32768\n"
    "ds_read_b128 a[92:95], v38 offset:34816\n"
    "v_mfma_f32_16x16x32_bf16 v[60:63], a[228:231], a[36:39], v[60:63]\n"
    "s_waitcnt lgkmcnt(14)\n"
    "ds_read_b128 a[164:167], v40 offset:32768\n"
    "s_waitcnt lgkmcnt(14)\n"
    "ds_read_b128 a[236:239], v40 offset:34816\n"
    "v_mfma_f32_16x16x32_bf16 v[64:67], v[228:231], a[0:3], v[64:67]\n"
    "v_fma_f32 v112, v112, v28, v32\n"
    "v_fma_f32 v113, v113, v28, v32\n"
    "v_fma_f32 v114, v114, v28, v32\n"
    "v_mfma_f32_16x16x32_bf16 v[68:71], a[84:87], a[0:3], v[68:71]\n"
    "v_fma_f32 v115, v115, v28, v32\n"
    "v_fma_f32 v116, v116, v28, v32\n"
    "v_mfma_f32_16x16x32_bf16 v[72:75], a[156:159], a[0:3], v[72:75]\n"
    "v_fma_f32 v117, v117, v28, v32\n"
    "v_fma_f32 v118, v118, v28, v32\n"
    "v_mfma_f32_16x16x32_bf16 v[76:79], a[228:231], a[0:3], v[76:79]\n"
    "s_waitcnt lgkmcnt(12)\n"
    "v_fma_f32 v119, v119, v28, v32\n"
    "v_cvt_pk_bf16_f32 v248, v112, v113\n"
    "v_mfma_f32_16x16x32_bf16 v[80:83], v[228:231], a[4:7], v[80:83]\n"
    "v_cvt_pk_bf16_f32 v249, v114, v115\n"
    "v_cvt_pk_bf16_f32 v250, v116, v117\n"
    "v_mfma_f32_16x16x32_bf16 v[84:87], a[84:87], a[4:7], v[84:87]\n"
    "v_cvt_pk_bf16_f32 v251, v118, v119\n"
    "v_mfma_f32_16x16x32_bf16 v[112:115], a[18:21], v[176:179], 0\n"
    "v_mfma_f32_16x16x32_bf16 v[88:91], a[156:159], a[4:7], v[88:91]\n"
    "v_mfma_f32_16x16x32_bf16 v[116:119], a[16:19], v[176:179], 0\n"
    "v_fma_f32 v120, v120, v29, v33\n"
    "v_fma_f32 v121, v121, v29, v33\n"
    "v_mfma_f32_16x16x32_bf16 v[92:95], a[228:231], a[4:7], v[92:95]\n"
    "s_waitcnt vmcnt(13) lgkmcnt(0)\n"
    "s_barrier\n"
    "ds_read_b64_tr_b16 a[12:13], v24 offset:0\n"
    "ds_read_b64_tr_b16 a[14:15], v24 offset:1024\n"
    "v_mfma_f32_16x16x32_bf16 v[96:99], a[8:11], v[228:231], v[96:99]\n"
    "s_add_u32 m0, s56, 0\n"
    "ds_read_b64_tr_b16 a[36:37], v25 offset:0\n"
    "ds_read_b64_tr_b16 a[38:39], v25 offset:1024\n"
    "v_mfma_f32_16x16x32_bf16 v[100:103], a[8:11], a[84:87], v[100:103]\n"
    "s_add_u32 s58, s61, 589824\n"
    "ds_read_b64_tr_b16 a[0:1], v26 offset:0\n"
    "ds_write_b128 v36, v[248:251] offset:0\n"
    "v_mfma_f32_16x16x32_bf16 v[104:107], a[8:11], a[156:159], v[104:107]\n"
    "ds_read_b64_tr_b16 a[2:3], v26 offset:1024\n"
    "buffer_load_dwordx4 v2, s[36:39], s58 offen offset:0 lds\n"
    "v_mfma_f32_16x16x32_bf16 v[108:111], a[8:11], a[228:231], v[108:111]\n"
    "s_waitcnt lgkmcnt(5)\n"
    "ds_read_b64_tr_b16 a[4:5], v27 offset:0\n"
    "v_fma_f32 v122, v122, v29, v33\n"
    "v_mfma_f32_16x16x32_bf16 v[48:51], v[232:235], a[12:15], v[48:51]\n"
    "ds_read_b64_tr_b16 a[6:7], v27 offset:1024\n"
    "v_fma_f32 v123, v123, v29, v33\n"
    "v_fma_f32 v124, v124, v29, v33\n"
    "v_mfma_f32_16x16x32_bf16 v[52:55], a[88:91], a[12:15], v[52:55]\n"
    "ds_read_b64_tr_b16 a[8:9], v24 offset:8192\n"
    "v_fma_f32 v125, v125, v29, v33\n"
    "v_mfma_f32_16x16x32_bf16 v[56:59], a[160:163], a[12:15], v[56:59]\n"
    "ds_read_b64_tr_b16 a[10:11], v24 offset:9216\n"
    "v_fma_f32 v126, v126, v29, v33\n"
    "v_mfma_f32_16x16x32_bf16 v[60:63], a[232:235], a[12:15], v[60:63]\n"
    "s_waitcnt lgkmcnt(7)\n"
    "ds_read_b64_tr_b16 a[12:13], v25 offset:8192\n"
    "buffer_load_dwordx4 v3, s[36:39], s58 offen offset:1024 lds\n"
    "v_fma_f32 v127, v127, v29, v33\n"
    "v_mfma_f32_16x16x32_bf16 v[64:67], v[232:235], a[36:39], v[64:67]\n"
    "ds_read_b64_tr_b16 a[14:15], v25 offset:9216\n"
    "v_cvt_pk_bf16_f32 v252, v120, v121\n"
    "v_mfma_f32_16x16x32_bf16 v[68:71], a[88:91], a[36:39], v[68:71]\n"
    "v_cvt_pk_bf16_f32 v253, v122, v123\n"
    "v_cvt_pk_bf16_f32 v254, v124, v125\n"
    "v_mfma_f32_16x16x32_bf16 v[72:75], a[160:163], a[36:39], v[72:75]\n"
    "v_cvt_pk_bf16_f32 v255, v126, v127\n"
    "ds_write_b128 v36, v[252:255] offset:1024\n"
    "v_mfma_f32_16x16x32_bf16 v[120:123], a[18:21], v[180:183], 0\n"
    "ds_read_b128 v[240:243], v37 offset:49152\n"
    "v_mfma_f32_16x16x32_bf16 v[124:127], a[16:19], v[180:183], 0\n"
    "v_fma_f32 v112, v112, v30, v34\n"
    "v_fma_f32 v113, v113, v30, v34\n"
    "v_fma_f32 v114, v114, v30, v34\n"
    "v_mfma_f32_16x16x32_bf16 v[76:79], a[232:235], a[36:39], v[76:79]\n"
    "s_waitcnt lgkmcnt(8)\n"
    "ds_read_b64_tr_b16 a[36:37], v26 offset:8192\n"
    "v_fma_f32 v115, v115, v30, v34\n"
    "v_mfma_f32_16x16x32_bf16 v[80:83], v[232:235], a[0:3], v[80:83]\n"
    "ds_read_b64_tr_b16 a[38:39], v26 offset:9216\n"
    "buffer_load_dwordx4 v4, s[36:39], s58 offen offset:2048 lds\n"
    "v_fma_f32 v116, v116, v30, v34\n"
    "v_mfma_f32_16x16x32_bf16 v[84:87], a[88:91], a[0:3], v[84:87]\n"
    "v_fma_f32 v117, v117, v30, v34\n"
    "v_fma_f32 v118, v118, v30, v34\n"
    "v_mfma_f32_16x16x32_bf16 v[88:91], a[160:163], a[0:3], v[88:91]\n"
    "v_fma_f32 v119, v119, v30, v34\n"
    "v_cvt_pk_bf16_f32 v176, v112, v113\n"
    "v_cvt_pk_bf16_f32 v177, v114, v115\n"
    "v_mfma_f32_16x16x32_bf16 v[92:95], a[232:235], a[0:3], v[92:95]\n"
    "s_waitcnt lgkmcnt(8)\n"
    "ds_read_b64_tr_b16 a[0:1], v27 offset:8192\n"
    "v_cvt_pk_bf16_f32 v178, v116, v117\n"
    "v_mfma_f32_16x16x32_bf16 v[96:99], a[4:7], v[232:235], v[96:99]\n"
    "ds_read_b64_tr_b16 a[2:3], v27 offset:9216\n"
    "v_cvt_pk_bf16_f32 v179, v118, v119\n"
    "v_fma_f32 v120, v120, v31, v35\n"
    "v_mfma_f32_16x16x32_bf16 v[100:103], a[4:7], a[88:91], v[100:103]\n"
    "buffer_load_dwordx4 v5, s[36:39], s58 offen offset:3072 lds\n"
    "ds_write_b128 v36, v[176:179] offset:2048\n"
    "v_mfma_f32_16x16x32_bf16 v[104:107], a[4:7], a[160:163], v[104:107]\n"
    "v_fma_f32 v121, v121, v31, v35\n"
    "v_fma_f32 v122, v122, v31, v35\n"
    "v_mfma_f32_16x16x32_bf16 v[108:111], a[4:7], a[232:235], v[108:111]\n"
    "s_waitcnt lgkmcnt(9)\n"
    "v_fma_f32 v123, v123, v31, v35\n"
    "v_fma_f32 v124, v124, v31, v35\n"
    "v_fma_f32 v125, v125, v31, v35\n"
    "v_mfma_f32_16x16x32_bf16 v[48:51], v[236:239], a[8:11], v[48:51]\n"
    "v_fma_f32 v126, v126, v31, v35\n"
    "v_fma_f32 v127, v127, v31, v35\n"
    "v_cvt_pk_bf16_f32 v180, v120, v121\n"
    "v_mfma_f32_16x16x32_bf16 v[52:55], a[92:95], a[8:11], v[52:55]\n"
    "v_cvt_pk_bf16_f32 v181, v122, v123\n"
    "v_cvt_pk_bf16_f32 v182, v124, v125\n"
    "v_cvt_pk_bf16_f32 v183, v126, v127\n"
    "v_mfma_f32_16x16x32_bf16 v[56:59], a[164:167], a[8:11], v[56:59]\n"
    "ds_write_b128 v36, v[180:183] offset:3072\n"
    "v_mfma_f32_16x16x32_bf16 v[60:63], a[236:239], a[8:11], v[60:63]\n"
    "s_waitcnt lgkmcnt(8)\n"
    "ds_read_b128 a[96:99], v37 offset:51200\n"
    "ds_read_b128 a[168:171], v39 offset:49152\n"
    "v_mfma_f32_16x16x32_bf16 v[64:67], v[236:239], a[12:15], v[64:67]\n"
    "ds_read_b128 a[240:243], v39 offset:51200\n"
    "ds_read_b128 v[244:247], v38 offset:49152\n"
    "v_mfma_f32_16x16x32_bf16 v[68:71], a[92:95], a[12:15], v[68:71]\n"
    "ds_read_b128 a[100:103], v38 offset:51200\n"
    "ds_read_b128 a[172:175], v40 offset:49152\n"
    "v_mfma_f32_16x16x32_bf16 v[72:75], a[164:167], a[12:15], v[72:75]\n"
    "ds_read_b128 a[244:247], v40 offset:51200\n"
    "v_mfma_f32_16x16x32_bf16 v[76:79], a[236:239], a[12:15], v[76:79]\n"
    "s_waitcnt lgkmcnt(11)\n"
    "v_mfma_f32_16x16x32_bf16 v[80:83], v[236:239], a[36:39], v[80:83]\n"
    "v_mfma_f32_16x16x32_bf16 v[84:87], a[92:95], a[36:39], v[84:87]\n"
    "v_mfma_f32_16x16x32_bf16 v[88:91], a[164:167], a[36:39], v[88:91]\n"
    "v_mfma_f32_16x16x32_bf16 v[92:95], a[236:239], a[36:39], v[92:95]\n"
    "s_waitcnt vmcnt(13) lgkmcnt(0)\n"
    "s_barrier\n"
    "ds_read_b64_tr_b16 a[4:5], v24 offset:16384\n"
    "ds_read_b64_tr_b16 a[6:7], v24 offset:17408\n"
    "v_mfma_f32_16x16x32_bf16 v[96:99], a[0:3], v[236:239], v[96:99]\n"
    "s_add_u32 m0, s56, 16384\n"
    "ds_read_b64_tr_b16 a[8:9], v25 offset:16384\n"
    "ds_read_b64_tr_b16 a[10:11], v25 offset:17408\n"
    "v_mfma_f32_16x16x32_bf16 v[100:103], a[0:3], a[92:95], v[100:103]\n"
    "s_add_u32 s58, s61, 1179648\n"
    "ds_read_b64_tr_b16 a[12:13], v26 offset:16384\n"
    "ds_read_b128 v[248:251], v37 offset:0\n"
    "v_mfma_f32_16x16x32_bf16 v[104:107], a[0:3], a[164:167], v[104:107]\n"
    "ds_read_b64_tr_b16 a[14:15], v26 offset:17408\n"
    "buffer_load_dwordx4 v2, s[36:39], s58 offen offset:0 lds\n"
    "v_mfma_f32_16x16x32_bf16 v[108:111], a[0:3], a[236:239], v[108:111]\n"
    "s_waitcnt lgkmcnt(5)\n"
    "ds_read_b64_tr_b16 a[36:37], v27 offset:16384\n"
    "ds_read_b128 a[104:107], v37 offset:2048\n"
    "v_mfma_f32_16x16x32_bf16 v[48:51], v[240:243], a[4:7], v[48:51]\n"
    "ds_read_b64_tr_b16 a[38:39], v27 offset:17408\n"
    "ds_read_b128 a[176:179], v39 offset:0\n"
    "v_mfma_f32_16x16x32_bf16 v[52:55], a[96:99], a[4:7], v[52:55]\n"
    "ds_read_b64_tr_b16 a[0:1], v24 offset:24576\n"
    "ds_read_b128 a[248:251], v39 offset:2048\n"
    "v_mfma_f32_16x16x32_bf16 v[56:59], a[168:171], a[4:7], v[56:59]\n"
    "ds_read_b64_tr_b16 a[2:3], v24 offset:25600\n"
    "ds_read_b128 v[252:255], v38 offset:0\n"
    "v_mfma_f32_16x16x32_bf16 v[60:63], a[240:243], a[4:7], v[60:63]\n"
    "s_waitcnt lgkmcnt(11)\n"
    "ds_read_b64_tr_b16 a[4:5], v25 offset:24576\n"
    "buffer_load_dwordx4 v3, s[36:39], s58 offen offset:1024 lds\n"
    "v_mfma_f32_16x16x32_bf16 v[64:67], v[240:243], a[8:11], v[64:67]\n"
    "ds_read_b64_tr_b16 a[6:7], v25 offset:25600\n"
    "ds_read_b128 a[108:111], v38 offset:2048\n"
    "v_mfma_f32_16x16x32_bf16 v[68:71], a[96:99], a[8:11], v[68:71]\n"
    "ds_read_b128 a[180:183], v40 offset:0\n"
    "s_waitcnt lgkmcnt(14)\n"
    "ds_read_b128 a[252:255], v40 offset:2048\n"
    "v_mfma_f32_16x16x32_bf16 v[72:75], a[168:171], a[8:11], v[72:75]\n"
    "v_mov_b32 v43, s27\n"
    "s_waitcnt vmcnt(10)\n"
    "v_xor_b32 v44, v42, v43\n"
    "v_mfma_f32_16x16x32_bf16 v[76:79], a[240:243], a[8:11], v[76:79]\n"
    "s_waitcnt lgkmcnt(14)\n"
    "ds_read_b64_tr_b16 a[8:9], v26 offset:24576\n"
    "v_xor_b32 v44, 0x5bd1e995, v44\n"
    "s_waitcnt lgkmcnt(14)\n"
    "v_mfma_f32_16x16x32_bf16 v[80:83], v[240:243], a[12:15], v[80:83]\n"
    "ds_read_b64_tr_b16 a[10:11], v26 offset:25600\n"
    "buffer_load_dwordx4 v4, s[36:39], s58 offen offset:2048 lds\n"
    "v_mfma_f32_16x16x32_bf16 v[84:87], a[96:99], a[12:15], v[84:87]\n"
    "buffer_store_dwordx3 v[42:44], v41, s[48:51], 0 offen offset:0 sc1\n"
    "v_mov_b32 v45, 0x80000000\n"
    "v_mfma_f32_16x16x32_bf16 v[88:91], a[168:171], a[12:15], v[88:91]\n"
    "v_writelane_b32 v45, s79, 0\n"
    "buffer_store_dword v43, v45, s[48:51], 0 offen offset:0 sc1\n"
    "v_mfma_f32_16x16x32_bf16 v[92:95], a[240:243], a[12:15], v[92:95]\n"
    "s_waitcnt lgkmcnt(14)\n"
    "ds_read_b64_tr_b16 a[12:13], v27 offset:24576\n"
    "s_waitcnt lgkmcnt(13)\n"
    "v_mfma_f32_16x16x32_bf16 v[96:99], a[36:39], v[240:243], v[96:99]\n"
    "ds_read_b64_tr_b16 a[14:15], v27 offset:25600\n"
    "v_mfma_f32_16x16x32_bf16 v[100:103], a[36:39], a[96:99], v[100:103]\n"
    "s_and_b32 s59, s23, 15\n"
    "buffer_load_dwordx4 v5, s[36:39], s58 offen offset:3072 lds\n"
    "s_lshl_b32 s59, s59, 1\n"
    "v_mfma_f32_16x16x32_bf16 v[104:107], a[36:39], a[168:171], v[104:107]\n"
    "s_add_u32 s59, s59, s25\n"
    "s_mul_i32 s59, s59, 165888\n"
    "s_mul_i32 s58, s25, 294912\n"
    "v_mfma_f32_16x16x32_bf16 v[108:111], a[36:39], a[240:243], v[108:111]\n"
    "s_waitcnt lgkmcnt(10)\n"
    "s_sub_u32 s59, s59, s58\n"
    "s_add_u32 s59, s59, s61\n"
    "v_mfma_f32_16x16x32_bf16 v[48:51], v[244:247], a[0:3], v[48:51]\n"
    "s_add_u32 s59, s59, 128\n"
    "v_min_u32 v1, 17, v0\n"
    "v_mul_u32_u24 v1, 9216, v1\n"
    "v_mfma_f32_16x16x32_bf16 v[52:55], a[100:103], a[0:3], v[52:55]\n"
    "buffer_load_dword a23, v1, s[36:39], s59 offen offset:0\n"
    "v_mfma_f32_16x16x32_bf16 v[56:59], a[172:175], a[0:3], v[56:59]\n"
    "v_mfma_f32_16x16x32_bf16 v[60:63], a[244:247], a[0:3], v[60:63]\n"
    "s_waitcnt lgkmcnt(7)\n"
    "v_mfma_f32_16x16x32_bf16 v[64:67], v[244:247], a[4:7], v[64:67]\n"
    "v_mfma_f32_16x16x32_bf16 v[68:71], a[100:103], a[4:7], v[68:71]\n"
    "v_mfma_f32_16x16x32_bf16 v[72:75], a[172:175], a[4:7], v[72:75]\n"
    "v_mfma_f32_16x16x32_bf16 v[76:79], a[244:247], a[4:7], v[76:79]\n"
    "s_waitcnt lgkmcnt(2)\n"
    "v_mfma_f32_16x16x32_bf16 v[80:83], v[244:247], a[8:11], v[80:83]\n"
    "v_mfma_f32_16x16x32_bf16 v[84:87], a[100:103], a[8:11], v[84:87]\n"
    "v_mfma_f32_16x16x32_bf16 v[88:91], a[172:175], a[8:11], v[88:91]\n"
    "v_mfma_f32_16x16x32_bf16 v[92:95], a[244:247], a[8:11], v[92:95]\n"
    "s_waitcnt lgkmcnt(0)\n"
    "s_barrier\n"
    "ds_read_b64_tr_b16 a[36:37], v24 offset:32768\n"
    "ds_read_b64_tr_b16 a[38:39], v24 offset:33792\n"
    "v_mfma_f32_16x16x32_bf16 v[96:99], a[12:15], v[244:247], v[96:99]\n"
    "s_add_u32 m0, s56, 32768\n"
    "ds_read_b64_tr_b16 a[0:1], v25 offset:32768\n"
    "ds_read_b64_tr_b16 a[2:3], v25 offset:33792\n"
    "v_mfma_f32_16x16x32_bf16 v[100:103], a[12:15], a[100:103], v[100:103]\n"
    "s_add_u32 s58, s61, 1769472\n"
    "ds_read_b64_tr_b16 a[4:5], v26 offset:32768\n"
    "v_mov_b32 v47, s73\n"
    "v_mfma_f32_16x16x32_bf16 v[104:107], a[12:15], a[172:175], v[104:107]\n"
    "ds_read_b64_tr_b16 a[6:7], v26 offset:33792\n"
    "buffer_load_dwordx4 v2, s[36:39], s58 offen offset:0 lds\n"
    "v_mfma_f32_16x16x32_bf16 v[108:111], a[12:15], a[244:247], v[108:111]\n"
    "s_waitcnt lgkmcnt(4)\n"
    "ds_read_b64_tr_b16 a[8:9], v27 offset:32768\n"
    "v_mov_b32 v19, s78\n"
    "v_mfma_f32_16x16x32_bf16 v[48:51], v[248:251], a[36:39], v[48:51]\n"
    "s_lshl_b32 s86, s21, 11\n"
    "ds_read_b64_tr_b16 a[10:11], v27 offset:33792\n"
    "v_and_b32 v34, 15, v0\n"
    "v_mfma_f32_16x16x32_bf16 v[52:55], a[104:107], a[36:39], v[52:55]\n"
    "s_lshl_b32 s87, s25, 2\n"
    "ds_read_b64_tr_b16 a[12:13], v24 offset:40960\n"
    "s_add_u32 s87, s87, s86\n"
    "v_mfma_f32_16x16x32_bf16 v[56:59], a[176:179], a[36:39], v[56:59]\n"
    "ds_read_b64_tr_b16 a[14:15], v24 offset:41984\n"
    "v_lshl_add_u32 v33, v34, 3, s87\n"
    "v_mfma_f32_16x16x32_bf16 v[60:63], a[248:251], a[36:39], v[60:63]\n"
    "s_waitcnt lgkmcnt(6)\n"
    "ds_read_b64_tr_b16 a[36:37], v25 offset:40960\n"
    "buffer_load_dwordx4 v3, s[36:39], s58 offen offset:1024 lds\n"
    "v_mfma_f32_16x16x32_bf16 v[64:67], v[248:251], a[0:3], v[64:67]\n"
    "ds_read_b64_tr_b16 a[38:39], v25 offset:41984\n"
    "v_add_u32 v33, 65536, v33\n"
    "v_mfma_f32_16x16x32_bf16 v[68:71], a[104:107], a[0:3], v[68:71]\n"
    "v_lshrrev_b32 v35, 4, v0\n"
    "v_lshlrev_b32 v35, 5, v35\n"
    "v_mfma_f32_16x16x32_bf16 v[72:75], a[176:179], a[0:3], v[72:75]\n"
    "v_lshl_add_u32 v32, v34, 1, v35\n"
    "v_add_u32 v32, s86, v32\n"
    "v_mfma_f32_16x16x32_bf16 v[76:79], a[248:251], a[0:3], v[76:79]\n"
    "s_waitcnt lgkmcnt(6)\n"
    "ds_read_b64_tr_b16 a[0:1], v26 offset:40960\n"
    "v_add_u32 v32, 65520, v32\n"
    "v_mfma_f32_16x16x32_bf16 v[80:83], v[248:251], a[4:7], v[80:83]\n"
    "ds_read_b64_tr_b16 a[2:3], v26 offset:41984\n"
    "buffer_load_dwordx4 v4, s[36:39], s58 offen offset:2048 lds\n"
    "v_mfma_f32_16x16x32_bf16 v[84:87], a[104:107], a[4:7], v[84:87]\n"
    "v_lshrrev_b32 v34, 2, v34\n"
    "v_cmp_eq_u32_e64 vcc, 2, v34\n"
    "v_mfma_f32_16x16x32_bf16 v[88:91], a[176:179], a[4:7], v[88:91]\n"
    "s_mul_i32 s86, s21, 3072\n"
    "v_add_u32 v35, 1040, v32\n"
    "v_cndmask_b32_e64 v32, v35, v32, vcc\n"
    "v_mfma_f32_16x16x32_bf16 v[92:95], a[248:251], a[4:7], v[92:95]\n"
    "s_waitcnt lgkmcnt(6)\n"
    "ds_read_b64_tr_b16 a[4:5], v27 offset:40960\n"
    "s_add_u32 s86, s86, 16384\n"
    "v_mfma_f32_16x16x32_bf16 v[96:99], a[8:11], v[248:251], v[96:99]\n"
    "s_xor_b32 s87, s21, 2\n"
    "ds_read_b64_tr_b16 a[6:7], v27 offset:41984\n"
    "s_mul_i32 s87, s87, 3072\n"
    "v_mfma_f32_16x16x32_bf16 v[100:103], a[8:11], a[104:107], v[100:103]\n"
    "s_add_u32 s87, s87, 16384\n"
    "buffer_load_dwordx4 v5, s[36:39], s58 offen offset:3072 lds\n"
    "v_lshl_add_u32 v36, v0, 4, s86\n"
    "v_mfma_f32_16x16x32_bf16 v[104:107], a[8:11], a[176:179], v[104:107]\n"
    "s_lshl_b32 s86, s24, 31\n"
    "v_lshl_add_u32 v37, v0, 4, s87\n"
    "v_lshl_add_u32 v38, v0, 4, s86\n"
    "v_mfma_f32_16x16x32_bf16 v[108:111], a[8:11], a[248:251], v[108:111]\n"
    "s_waitcnt lgkmcnt(6)\n"
    "v_lshrrev_b32 v39, 4, v0\n"
    "v_cmp_eq_u32_e64 vcc, 1, v39\n"
    "v_mfma_f32_16x16x32_bf16 v[48:51], v[252:255], a[12:15], v[48:51]\n"
    "v_mov_b32 v34, 0x80000000\n"
    "v_add_u32 v39, 1792, v38\n"
    "v_mfma_f32_16x16x32_bf16 v[52:55], a[108:111], a[12:15], v[52:55]\n"
    "v_cndmask_b32_e64 v39, v34, v39, vcc\n"
    "v_mfma_f32_16x16x32_bf16 v[56:59], a[180:183], a[12:15], v[56:59]\n"
    "v_mfma_f32_16x16x32_bf16 v[60:63], a[252:255], a[12:15], v[60:63]\n"
    "s_waitcnt lgkmcnt(4)\n"
    "v_mfma_f32_16x16x32_bf16 v[64:67], v[252:255], a[36:39], v[64:67]\n"
    "v_mfma_f32_16x16x32_bf16 v[68:71], a[108:111], a[36:39], v[68:71]\n"
    "v_mfma_f32_16x16x32_bf16 v[72:75], a[180:183], a[36:39], v[72:75]\n"
    "v_mfma_f32_16x16x32_bf16 v[76:79], a[252:255], a[36:39], v[76:79]\n"
    "s_waitcnt lgkmcnt(2)\n"
    "v_mfma_f32_16x16x32_bf16 v[80:83], v[252:255], a[0:3], v[80:83]\n"
    "v_mfma_f32_16x16x32_bf16 v[84:87], a[108:111], a[0:3], v[84:87]\n"
    "v_mfma_f32_16x16x32_bf16 v[88:91], a[180:183], a[0:3], v[88:91]\n"
    "v_mfma_f32_16x16x32_bf16 v[92:95], a[252:255], a[0:3], v[92:95]\n"
    "s_waitcnt vmcnt(15) lgkmcnt(0)\n"
    "s_barrier\n"
    "ds_read_b64_tr_b16 a[8:9], v8 offset:49152\n"
    "ds_read_b64_tr_b16 a[10:11], v8 offset:49664\n"
    "v_mfma_f32_16x16x32_bf16 v[96:99], a[4:7], v[252:255], v[96:99]\n"
    "s_add_u32 m0, s56, 49152\n"
    "ds_read_b64_tr_b16 a[12:13], v9 offset:49152\n"
    "ds_read_b64_tr_b16 a[14:15], v9 offset:49664\n"
    "v_mfma_f32_16x16x32_bf16 v[100:103], a[4:7], a[108:111], v[100:103]\n"
    "s_add_u32 s58, s61, 2359296\n"
    "ds_read_b64_tr_b16 a[36:37], v8 offset:57344\n"
    "v_mfma_f32_16x16x32_bf16 v[104:107], a[4:7], a[180:183], v[104:107]\n"
    "ds_read_b64_tr_b16 a[38:39], v8 offset:57856\n"
    "v_mfma_f32_16x16x32_bf16 v[108:111], a[4:7], a[252:255], v[108:111]\n"
    "s_waitcnt lgkmcnt(4)\n"
    "ds_read_b64_tr_b16 a[0:1], v9 offset:57344\n"
    "buffer_load_dwordx4 v2, s[36:39], s58 offen offset:0 lds\n"
    "v_mfma_f32_16x16x32_bf16 v[112:115], a[8:11], v[184:187], 0\n"
    "ds_read_b64_tr_b16 a[2:3], v9 offset:57856\n"
    "v_mfma_f32_16x16x32_bf16 v[116:119], a[8:11], a[40:43], 0\n"
    "ds_read_b64_tr_b16 a[4:5], v8 offset:53248\n"
    "v_mfma_f32_16x16x32_bf16 v[120:123], a[8:11], a[112:115], 0\n"
    "ds_read_b64_tr_b16 a[6:7], v8 offset:53760\n"
    "buffer_load_dwordx4 v3, s[36:39], s58 offen offset:1024 lds\n"
    "v_mfma_f32_16x16x32_bf16 v[124:127], a[8:11], a[184:187], 0\n"
    "s_waitcnt lgkmcnt(6)\n"
    "ds_read_b64_tr_b16 a[8:9], v9 offset:53248\n"
    "v_cvt_pk_bf16_f32 v64, v64, v65\n"
    "v_mfma_f32_16x16x32_bf16 v[128:131], a[12:15], v[184:187], 0\n"
    "ds_read_b64_tr_b16 a[10:11], v9 offset:53760\n"
    "v_cvt_pk_bf16_f32 v65, v66, v67\n"
    "v_mfma_f32_16x16x32_bf16 v[132:135], a[12:15], a[40:43], 0\n"
    "buffer_load_dwordx4 v4, s[36:39], s58 offen offset:2048 lds\n"
    "v_cvt_pk_bf16_f32 v66, v68, v69\n"
    "v_mfma_f32_16x16x32_bf16 v[136:139], a[12:15], a[112:115], 0\n"
    "v_cvt_pk_bf16_f32 v67, v70, v71\n"
    "v_cvt_pk_bf16_f32 v72, v72, v73\n"
    "v_mfma_f32_16x16x32_bf16 v[140:143], a[12:15], a[184:187], 0\n"
    "s_waitcnt lgkmcnt(6)\n"
    "ds_read_b64_tr_b16 a[12:13], v8 offset:61440\n"
    "v_cvt_pk_bf16_f32 v73, v74, v75\n"
    "v_mfma_f32_16x16x32_bf16 v[144:147], a[36:39], v[184:187], 0\n"
    "ds_read_b64_tr_b16 a[14:15], v8 offset:61952\n"
    "buffer_load_dwordx4 v5, s[36:39], s58 offen offset:3072 lds\n"
    "s_add_u32 m0, s56, 4294926336\n"
    "v_mfma_f32_16x16x32_bf16 v[148:151], a[36:39], a[40:43], 0\n"
    "s_add_u32 s58, s61, 2949120\n"
    "v_cvt_pk_bf16_f32 v74, v76, v77\n"
    "v_cvt_pk_bf16_f32 v75, v78, v79\n"
    "v_cvt_pk_bf16_f32 v68, v80, v81\n"
    "v_mfma_f32_16x16x32_bf16 v[152:155], a[36:39], a[112:115], 0\n"
    "v_cvt_pk_bf16_f32 v69, v82, v83\n"
    "v_cvt_pk_bf16_f32 v70, v84, v85\n"
    "v_mfma_f32_16x16x32_bf16 v[156:159], a[36:39], a[184:187], 0\n"
    "s_waitcnt lgkmcnt(6)\n"
    "ds_read_b64_tr_b16 a[36:37], v9 offset:61440\n"
    "v_cvt_pk_bf16_f32 v71, v86, v87\n"
    "v_mfma_f32_16x16x32_bf16 v[160:163], a[0:3], v[184:187], 0\n"
    "ds_read_b64_tr_b16 a[38:39], v9 offset:61952\n"
    "buffer_load_dwordx4 v2, s[36:39], s58 offen offset:0 lds\n"
    "v_mfma_f32_16x16x32_bf16 v[164:167], a[0:3], a[40:43], 0\n"
    "v_cndmask_b32_e64 v68, v19, v68, s[80:81]\n"
    "v_cndmask_b32_e64 v69, v19, v69, s[80:81]\n"
    "v_mfma_f32_16x16x32_bf16 v[168:171], a[0:3], a[112:115], 0\n"
    "v_cndmask_b32_e64 v70, v19, v70, s[80:81]\n"
    "v_cndmask_b32_e64 v71, v19, v71, s[80:81]\n"
    "v_mfma_f32_16x16x32_bf16 v[172:175], a[0:3], a[184:187], 0\n"
    "s_waitcnt lgkmcnt(6)\n"
    "buffer_load_dwordx4 v3, s[36:39], s58 offen offset:1024 lds\n"
    "v_cvt_pk_bf16_f32 v76, v88, v89\n"
    "v_mfma_f32_16x16x32_bf16 v[112:115], a[4:7], v[188:191], v[112:115]\n"
    "v_cvt_pk_bf16_f32 v77, v90, v91\n"
    "v_cvt_pk_bf16_f32 v78, v92, v93\n"
    "v_mfma_f32_16x16x32_bf16 v[116:119], a[4:7], a[44:47], v[116:119]\n"
    "v_cvt_pk_bf16_f32 v79, v94, v95\n"
    "v_cndmask_b32_e64 v76, v19, v76, s[80:81]\n"
    "v_cndmask_b32_e64 v77, v19, v77, s[80:81]\n"
    "v_mfma_f32_16x16x32_bf16 v[120:123], a[4:7], a[116:119], v[120:123]\n"
    "buffer_load_dwordx4 v4, s[36:39], s58 offen offset:2048 lds\n"
    "v_cndmask_b32_e64 v78, v19, v78, s[80:81]\n"
    "v_mfma_f32_16x16x32_bf16 v[124:127], a[4:7], a[188:191], v[124:127]\n"
    "s_waitcnt lgkmcnt(4)\n"
    "v_cndmask_b32_e64 v79, v19, v79, s[80:81]\n"
    "s_mov_b32 s80, 0xffff0000\n"
    "v_mfma_f32_16x16x32_bf16 v[128:131], a[8:11], v[188:191], v[128:131]\n"
    "s_mov_b32 s81, 0xffff0000\n"
    "v_exp_f32_e64 v28, -v48 clamp\n"
    "v_mfma_f32_16x16x32_bf16 v[132:135], a[8:11], a[44:47], v[132:135]\n"
    "buffer_load_dwordx4 v5, s[36:39], s58 offen offset:3072 lds\n"
    "v_mfma_f32_16x16x32_bf16 v[136:139], a[8:11], a[116:119], v[136:139]\n"
    "v_exp_f32_e64 v29, -v49 clamp\n"
    "v_mfma_f32_16x16x32_bf16 v[140:143], a[8:11], a[188:191], v[140:143]\n"
    "s_waitcnt lgkmcnt(2)\n"
    "v_exp_f32_e64 v30, -v50 clamp\n"
    "v_mfma_f32_16x16x32_bf16 v[144:147], a[12:15], v[188:191], v[144:147]\n"
    "v_exp_f32_e64 v31, -v51 clamp\n"
    "v_mfma_f32_16x16x32_bf16 v[148:151], a[12:15], a[44:47], v[148:151]\n"
    "v_fma_f32 v48, v48, v47, s74\n"
    "v_fma_f32 v49, v49, v47, s74\n"
    "v_fma_f32 v50, v50, v47, s74\n"
    "v_mfma_f32_16x16x32_bf16 v[152:155], a[12:15], a[116:119], v[152:155]\n"
    "v_fma_f32 v51, v51, v47, s74\n"
    "v_max_f32 v48, s74, v48\n"
    "v_mfma_f32_16x16x32_bf16 v[156:159], a[12:15], a[188:191], v[156:159]\n"
    "s_waitcnt vmcnt(19) lgkmcnt(0)\n"
    "s_barrier\n"
    "ds_read_b64_tr_b16 a[0:1], v6 offset:40960\n"
    "ds_read_b64_tr_b16 a[2:3], v6 offset:41472\n"
    "v_mfma_f32_16x16x32_bf16 v[160:163], a[36:39], v[188:191], v[160:163]\n"
    "s_add_u32 m0, s56, 65536\n"
    "ds_read_b64_tr_b16 a[4:5], v7 offset:40960\n"
    "ds_read_b64_tr_b16 a[6:7], v7 offset:41472\n"
    "v_mfma_f32_16x16x32_bf16 v[164:167], a[36:39], a[44:47], v[164:167]\n"
    "s_add_u32 s58, s61, 3538944\n"
    "ds_read_b64_tr_b16 a[8:9], v6 offset:49152\n"
    "v_max_f32 v49, s74, v49\n"
    "v_mfma_f32_16x16x32_bf16 v[168:171], a[36:39], a[116:119], v[168:171]\n"
    "ds_read_b64_tr_b16 a[10:11], v6 offset:49664\n"
    "v_max_f32 v50, s74, v50\n"
    "v_mfma_f32_16x16x32_bf16 v[172:175], a[36:39], a[188:191], v[172:175]\n"
    "s_waitcnt lgkmcnt(4)\n"
    "ds_read_b64_tr_b16 a[12:13], v7 offset:49152\n"
    "buffer_load_dwordx4 v2, s[36:39], s58 offen offset:0 lds\n"
    "v_mfma_f32_16x16x32_bf16 v[112:115], a[0:3], v[192:195], v[112:115]\n"
    "ds_read_b64_tr_b16 a[14:15], v7 offset:49664\n"
    "v_max_f32 v51, s74, v51\n"
    "v_add_f32 v48, v48, v28\n"
    "v_mfma_f32_16x16x32_bf16 v[116:119], a[0:3], a[48:51], v[116:119]\n"
    "ds_read_b64_tr_b16 a[36:37], v6 offset:45056\n"
    "v_add_f32 v49, v49, v29\n"
    "v_mfma_f32_16x16x32_bf16 v[120:123], a[0:3], a[120:123], v[120:123]\n"
    "ds_read_b64_tr_b16 a[38:39], v6 offset:45568\n"
    "buffer_load_dwordx4 v3, s[36:39], s58 offen offset:1024 lds\n"
    "v_mfma_f32_16x16x32_bf16 v[124:127], a[0:3], a[192:195], v[124:127]\n"
    "s_waitcnt lgkmcnt(6)\n"
    "ds_read_b64_tr_b16 a[0:1], v7 offset:45056\n"
    "v_add_f32 v50, v50, v30\n"
    "v_mfma_f32_16x16x32_bf16 v[128:131], a[4:7], v[192:195], v[128:131]\n"
    "ds_read_b64_tr_b16 a[2:3], v7 offset:45568\n"
    "v_add_f32 v51, v51, v31\n"
    "v_mfma_f32_16x16x32_bf16 v[132:135], a[4:7], a[48:51], v[132:135]\n"
    "buffer_load_dwordx4 v4, s[36:39], s58 offen offset:2048 lds\n"
    "v_mfma_f32_16x16x32_bf16 v[136:139], a[4:7], a[120:123], v[136:139]\n"
    "v_exp_f32_e64 v28, -v52 clamp\n"
    "v_mfma_f32_16x16x32_bf16 v[140:143], a[4:7], a[192:195], v[140:143]\n"
    "s_waitcnt lgkmcnt(6)\n"
    "ds_read_b64_tr_b16 a[4:5], v6 offset:53248\n"
    "v_mfma_f32_16x16x32_bf16 v[144:147], a[8:11], v[192:195], v[144:147]\n"
    "ds_read_b64_tr_b16 a[6:7], v6 offset:53760\n"
    "buffer_load_dwordx4 v5, s[36:39], s58 offen offset:3072 lds\n"
    "s_add_u32 m0, s56, 4294942720\n"
    "s_add_u32 s58, s61, 4128768\n"
    "v_mfma_f32_16x16x32_bf16 v[148:151], a[8:11], a[48:51], v[148:151]\n"
    "v_exp_f32_e64 v29, -v53 clamp\n"
    "v_mfma_f32_16x16x32_bf16 v[152:155], a[8:11], a[120:123], v[152:155]\n"
    "v_exp_f32_e64 v30, -v54 clamp\n"
    "v_mfma_f32_16x16x32_bf16 v[156:159], a[8:11], a[192:195], v[156:159]\n"
    "s_waitcnt lgkmcnt(6)\n"
    "ds_read_b64_tr_b16 a[8:9], v7 offset:53248\n"
    "v_mfma_f32_16x16x32_bf16 v[160:163], a[12:15], v[192:195], v[160:163]\n"
    "ds_read_b64_tr_b16 a[10:11], v7 offset:53760\n"
    "buffer_load_dwordx4 v2, s[36:39], s58 offen offset:0 lds\n"
    "v_mfma_f32_16x16x32_bf16 v[164:167], a[12:15], a[48:51], v[164:167]\n"
    "v_exp_f32_e64 v31, -v55 clamp\n"
    "v_mfma_f32_16x16x32_bf16 v[168:171], a[12:15], a[120:123], v[168:171]\n"
    "v_fma_f32 v52, v52, v47, s74\n"
    "v_fma_f32 v53, v53, v47, s74\n"
    "v_mfma_f32_16x16x32_bf16 v[172:175], a[12:15], a[192:195], v[172:175]\n"
    "s_waitcnt lgkmcnt(6)\n"
    "buffer_load_dwordx4 v3, s[36:39], s58 offen offset:1024 lds\n"
    "v_fma_f32 v54, v54, v47, s74\n"
    "v_fma_f32 v55, v55, v47, s74\n"
    "v_mfma_f32_16x16x32_bf16 v[112:115], a[36:39], v[196:199], v[112:115]\n"
    "v_max_f32 v52, s74, v52\n"
    "v_max_f32 v53, s74, v53\n"
    "v_mfma_f32_16x16x32_bf16 v[116:119], a[36:39], a[52:55], v[116:119]\n"
    "v_max_f32 v54, s74, v54\n"
    "v_max_f32 v55, s74, v55\n"
    "v_mfma_f32_16x16x32_bf16 v[120:123], a[36:39], a[124:127], v[120:123]\n"
    "buffer_load_dwordx4 v4, s[36:39], s58 offen offset:2048 lds\n"
    "v_add_f32 v52, v52, v28\n"
    "v_mfma_f32_16x16x32_bf16 v[124:127], a[36:39], a[196:199], v[124:127]\n"
    "s_waitcnt lgkmcnt(4)\n"
    "v_add_f32 v53, v53, v29\n"
    "v_add_f32 v54, v54, v30\n"
    "v_mfma_f32_16x16x32_bf16 v[128:131], a[0:3], v[196:199], v[128:131]\n"
    "v_add_f32 v55, v55, v31\n"
    "v_mfma_f32_16x16x32_bf16 v[132:135], a[0:3], a[52:55], v[132:135]\n"
    "buffer_load_dwordx4 v5, s[36:39], s58 offen offset:3072 lds\n"
    "v_mfma_f32_16x16x32_bf16 v[136:139], a[0:3], a[124:127], v[136:139]\n"
    "v_exp_f32_e64 v28, -v56 clamp\n"
    "v_mfma_f32_16x16x32_bf16 v[140:143], a[0:3], a[196:199], v[140:143]\n"
    "s_waitcnt lgkmcnt(2)\n"
    "v_exp_f32_e64 v29, -v57 clamp\n"
    "v_mfma_f32_16x16x32_bf16 v[144:147], a[4:7], v[196:199], v[144:147]\n"
    "v_exp_f32_e64 v30, -v58 clamp\n"
    "v_mfma_f32_16x16x32_bf16 v[148:151], a[4:7], a[52:55], v[148:151]\n"
    "v_exp_f32_e64 v31, -v59 clamp\n"
    "v_mfma_f32_16x16x32_bf16 v[152:155], a[4:7], a[124:127], v[152:155]\n"
    "v_fma_f32 v56, v56, v47, s74\n"
    "v_fma_f32 v57, v57, v47, s74\n"
    "v_mfma_f32_16x16x32_bf16 v[156:159], a[4:7], a[196:199], v[156:159]\n"
    "s_waitcnt vmcnt(21) lgkmcnt(0)\n"
    "s_barrier\n"
    "ds_read_b64_tr_b16 a[12:13], v8 offset:0\n"
    "ds_read_b64_tr_b16 a[14:15], v8 offset:512\n"
    "L_hop3:\n"
    "s_cbranch_execz L_hop7\n"
    "v_mfma_f32_16x16x32_bf16 v[160:163], a[8:11], v[196:199], v[160:163]\n"
    "s_add_u32 m0, s56, 0\n"
    "ds_read_b64_tr_b16 a[36:37], v9 offset:0\n"
    "ds_read_b64_tr_b16 a[38:39], v9 offset:512\n"
    "v_mfma_f32_16x16x32_bf16 v[164:167], a[8:11], a[52:55], v[164:167]\n"
    "s_add_u32 s58, s61, 4718592\n"
    "ds_read_b64_tr_b16 a[0:1], v8 offset:8192\n"
    "v_fma_f32 v58, v58, v47, s74\n"
    "v_mfma_f32_16x16x32_bf16 v[168:171], a[8:11], a[124:127], v[168:171]\n"
    "ds_read_b64_tr_b16 a[2:3], v8 offset:8704\n"
    "buffer_load_dwordx4 v2, s[36:39], s58 offen offset:0 lds\n"
    "v_mfma_f32_16x16x32_bf16 v[172:175], a[8:11], a[196:199], v[172:175]\n"
    "s_waitcnt lgkmcnt(4)\n"
    "ds_read_b64_tr_b16 a[4:5], v9 offset:8192\n"
    "v_fma_f32 v59, v59, v47, s74\n"
    "v_mfma_f32_16x16x32_bf16 v[112:115], a[12:15], v[200:203], v[112:115]\n"
    "ds_read_b64_tr_b16 a[6:7], v9 offset:8704\n"
    "v_max_f32 v56, s74, v56\n"
    "v_max_f32 v57, s74, v57\n"
    "v_mfma_f32_16x16x32_bf16 v[116:119], a[12:15], a[56:59], v[116:119]\n"
    "ds_read_b64_tr_b16 a[8:9], v8 offset:4096\n"
    "v_max_f32 v58, s74, v58\n"
    "v_mfma_f32_16x16x32_bf16 v[120:123], a[12:15], a[128:131], v[120:123]\n"
    "ds_read_b64_tr_b16 a[10:11], v8 offset:4608\n"
    "v_max_f32 v59, s74, v59\n"
    "v_mfma_f32_16x16x32_bf16 v[124:127], a[12:15], a[200:203], v[124:127]\n"
    "s_waitcnt lgkmcnt(6)\n"
    "ds_read_b64_tr_b16 a[12:13], v9 offset:4096\n"
    "buffer_load_dwordx4 v3, s[36:39], s58 offen offset:1024 lds\n"
    "v_mfma_f32_16x16x32_bf16 v[128:131], a[36:39], v[200:203], v[128:131]\n"
    "ds_read_b64_tr_b16 a[14:15], v9 offset:4608\n"
    "v_add_f32 v56, v56, v28\n"
    "v_mfma_f32_16x16x32_bf16 v[132:135], a[36:39], a[56:59], v[132:135]\n"
    "v_add_f32 v57, v57, v29\n"
    "v_add_f32 v58, v58, v30\n"
    "v_mfma_f32_16x16x32_bf16 v[136:139], a[36:39], a[128:131], v[136:139]\n"
    "v_add_f32 v59, v59, v31\n"
    "v_exp_f32_e64 v28, -v60 clamp\n"
    "v_mfma_f32_16x16x32_bf16 v[140:143], a[36:39], a[200:203], v[140:143]\n"
    "s_waitcnt lgkmcnt(6)\n"
    "ds_read_b64_tr_b16 a[36:37], v8 offset:12288\n"
    "v_mfma_f32_16x16x32_bf16 v[144:147], a[0:3], v[200:203], v[144:147]\n"
    "ds_read_b64_tr_b16 a[38:39], v8 offset:12800\n"
    "buffer_load_dwordx4 v4, s[36:39], s58 offen offset:2048 lds\n"
    "v_mfma_f32_16x16x32_bf16 v[148:151], a[0:3], a[56:59], v[148:151]\n"
    "v_exp_f32_e64 v29, -v61 clamp\n"
    "v_mfma_f32_16x16x32_bf16 v[152:155], a[0:3], a[128:131], v[152:155]\n"
    "v_exp_f32_e64 v30, -v62 clamp\n"
    "v_mfma_f32_16x16x32_bf16 v[156:159], a[0:3], a[200:203], v[156:159]\n"
    "s_waitcnt lgkmcnt(6)\n"
    "ds_read_b64_tr_b16 a[0:1], v9 offset:12288\n"
    "v_exp_f32_e64 v31, -v63 clamp\n"
    "v_mfma_f32_16x16x32_bf16 v[160:163], a[4:7], v[200:203], v[160:163]\n"
    "ds_read_b64_tr_b16 a[2:3], v9 offset:12800\n"
    "v_fma_f32 v60, v60, v47, s74\n"
    "v_mfma_f32_16x16x32_bf16 v[164:167], a[4:7], a[56:59], v[164:167]\n"
    "buffer_load_dwordx4 v5, s[36:39], s58 offen offset:3072 lds\n"
    "v_fma_f32 v61, v61, v47, s74\n"
    "v_mfma_f32_16x16x32_bf16 v[168:171], a[4:7], a[128:131], v[168:171]\n"
    "v_fma_f32 v62, v62, v47, s74\n"
    "v_fma_f32 v63, v63, v47, s74\n"
    "v_mfma_f32_16x16x32_bf16 v[172:175], a[4:7], a[200:203], v[172:175]\n"
    "s_waitcnt lgkmcnt(6)\n"
    "v_max_f32 v60, s74, v60\n"
    "v_max_f32 v61, s74, v61\n"
    "v_mfma_f32_16x16x32_bf16 v[112:115], a[8:11], v[204:207], v[112:115]\n"
    "v_max_f32 v62, s74, v62\n"
    "v_max_f32 v63, s74, v63\n"
    "v_add_f32 v60, v60, v28\n"
    "v_mfma_f32_16x16x32_bf16 v[116:119], a[8:11], a[60:63], v[116:119]\n"
    "v_add_f32 v61, v61, v29\n"
    "v_add_f32 v62, v62, v30\n"
    "v_mfma_f32_16x16x32_bf16 v[120:123], a[8:11], a[132:135], v[120:123]\n"
    "v_add_f32 v63, v63, v31\n"
    "v_cvt_pk_bf16_f32 v48, v48, v49\n"
    "v_mfma_f32_16x16x32_bf16 v[124:127], a[8:11], a[204:207], v[124:127]\n"
    "s_waitcnt lgkmcnt(4)\n"
    "v_cvt_pk_bf16_f32 v49, v50, v51\n"
    "v_cvt_pk_bf16_f32 v50, v52, v53\n"
    "v_mfma_f32_16x16x32_bf16 v[128:131], a[12:15], v[204:207], v[128:131]\n"
    "v_cvt_pk_bf16_f32 v51, v54, v55\n"
    "v_cvt_pk_bf16_f32 v56, v56, v57\n"
    "v_mfma_f32_16x16x32_bf16 v[132:135], a[12:15], a[60:63], v[132:135]\n"
    "v_cvt_pk_bf16_f32 v57, v58, v59\n"
    "v_cvt_pk_bf16_f32 v58, v60, v61\n"
    "v_cvt_pk_bf16_f32 v59, v62, v63\n"
    "v_mfma_f32_16x16x32_bf16 v[136:139], a[12:15], a[132:135], v[136:139]\n"
    "v_exp_f32_e64 v28, -v80 clamp\n"
    "v_mfma_f32_16x16x32_bf16 v[140:143], a[12:15], a[204:207], v[140:143]\n"
    "s_waitcnt lgkmcnt(2)\n"
    "v_exp_f32_e64 v29, -v81 clamp\n"
    "v_mfma_f32_16x16x32_bf16 v[144:147], a[36:39], v[204:207], v[144:147]\n"
    "v_exp_f32_e64 v30, -v82 clamp\n"
    "v_mfma_f32_16x16x32_bf16 v[148:151], a[36:39], a[60:63], v[148:151]\n"
    "v_exp_f32_e64 v31, -v83 clamp\n"
    "v_mfma_f32_16x16x32_bf16 v[152:155], a[36:39], a[132:135], v[152:155]\n"
    "v_fma_f32 v80, v80, v47, s74\n"
    "v_fma_f32 v81, v81, v47, s74\n"
    "v_fma_f32 v82, v82, v47, s74\n"
    "v_mfma_f32_16x16x32_bf16 v[156:159], a[36:39], a[204:207], v[156:159]\n"
    "s_waitcnt vmcnt(20) lgkmcnt(0)\n"
    "s_barrier\n"
    "ds_read_b64_tr_b16 a[4:5], v8 offset:16384\n"
    "ds_read_b64_tr_b16 a[6:7], v8 offset:16896\n"
    "v_mfma_f32_16x16x32_bf16 v[160:163], a[0:3], v[204:207], v[160:163]\n"
    "s_add_u32 m0, s56, 16384\n"
    "ds_read_b64_tr_b16 a[8:9], v9 offset:16384\n"
    "ds_read_b64_tr_b16 a[10:11], v9 offset:16896\n"
    "v_mfma_f32_16x16x32_bf16 v[164:167], a[0:3], a[60:63], v[164:167]\n"
    "s_add_u32 s58, s61, 128\n"
    "ds_read_b64_tr_b16 a[12:13], v8 offset:24576\n"
    "v_fma_f32 v83, v83, v47, s74\n"
    "v_mfma_f32_16x16x32_bf16 v[168:171], a[0:3], a[132:135], v[168:171]\n"
    "ds_read_b64_tr_b16 a[14:15], v8 offset:25088\n"
    "buffer_load_dwordx4 v2, s[36:39], s58 offen offset:0 lds\n"
    "v_mfma_f32_16x16x32_bf16 v[172:175], a[0:3], a[204:207], v[172:175]\n"
    "s_waitcnt lgkmcnt(4)\n"
    "ds_read_b64_tr_b16 a[36:37], v9 offset:24576\n"
    "v_max_f32 v80, s74, v80\n"
    "v_max_f32 v81, s74, v81\n"
    "v_mfma_f32_16x16x32_bf16 v[112:115], a[4:7], v[208:211], v[112:115]\n"
    "ds_read_b64_tr_b16 a[38:39], v9 offset:25088\n"
    "v_max_f32 v82, s74, v82\n"
    "v_mfma_f32_16x16x32_bf16 v[116:119], a[4:7], a[64:67], v[116:119]\n"
    "ds_read_b64_tr_b16 a[0:1], v8 offset:20480\n"
    "v_max_f32 v83, s74, v83\n"
    "v_mfma_f32_16x16x32_bf16 v[120:123], a[4:7], a[136:139], v[120:123]\n"
    "ds_read_b64_tr_b16 a[2:3], v8 offset:20992\n"
    "v_add_f32 v80, v80, v28\n"
    "v_mfma_f32_16x16x32_bf16 v[124:127], a[4:7], a[208:211], v[124:127]\n"
    "s_waitcnt lgkmcnt(6)\n"
    "ds_read_b64_tr_b16 a[4:5], v9 offset:20480\n"
    "buffer_load_dwordx4 v3, s[36:39], s58 offen offset:1024 lds\n"
    "v_mfma_f32_16x16x32_bf16 v[128:131], a[8:11], v[208:211], v[128:131]\n"
    "ds_read_b64_tr_b16 a[6:7], v9 offset:20992\n"
    "v_add_f32 v81, v81, v29\n"
    "v_mfma_f32_16x16x32_bf16 v[132:135], a[8:11], a[64:67], v[132:135]\n"
    "v_add_f32 v82, v82, v30\n"
    "v_add_f32 v83, v83, v31\n"
    "v_mfma_f32_16x16x32_bf16 v[136:139], a[8:11], a[136:139], v[136:139]\n"
    "v_exp_f32_e64 v28, -v84 clamp\n"
    "v_mfma_f32_16x16x32_bf16 v[140:143], a[8:11], a[208:211], v[140:143]\n"
    "s_waitcnt lgkmcnt(6)\n"
    "ds_read_b64_tr_b16 a[8:9], v8 offset:28672\n"
    "v_mfma_f32_16x16x32_bf16 v[144:147], a[12:15], v[208:211], v[144:147]\n"
    "ds_read_b64_tr_b16 a[10:11], v8 offset:29184\n"
    "buffer_load_dwordx4 v4, s[36:39], s58 offen offset:2048 lds\n"
    "v_mfma_f32_16x16x32_bf16 v[148:151], a[12:15], a[64:67], v[148:151]\n"
    "v_exp_f32_e64 v29, -v85 clamp\n"
    "v_mfma_f32_16x16x32_bf16 v[152:155], a[12:15], a[136:139], v[152:155]\n"
    "v_exp_f32_e64 v30, -v86 clamp\n"
    "v_mfma_f32_16x16x32_bf16 v[156:159], a[12:15], a[208:211], v[156:159]\n"
    "s_waitcnt lgkmcnt(6)\n"
    "ds_read_b64_tr_b16 a[12:13], v9 offset:28672\n"
    "v_mfma_f32_16x16x32_bf16 v[160:163], a[36:39], v[208:211], v[160:163]\n"
    "ds_read_b64_tr_b16 a[14:15], v9 offset:29184\n"
    "v_mfma_f32_16x16x32_bf16 v[164:167], a[36:39], a[64:67], v[164:167]\n"
    "buffer_load_dwordx4 v5, s[36:39], s58 offen offset:3072 lds\n"
    "v_exp_f32_e64 v31, -v87 clamp\n"
    "v_mfma_f32_16x16x32_bf16 v[168:171], a[36:39], a[136:139], v[168:171]\n"
    "v_fma_f32 v84, v84, v47, s74\n"
    "v_fma_f32 v85, v85, v47, s74\n"
    "v_mfma_f32_16x16x32_bf16 v[172:175], a[36:39], a[208:211], v[172:175]\n"
    "s_waitcnt lgkmcnt(6)\n"
    "v_fma_f32 v86, v86, v47, s74\n"
    "v_fma_f32 v87, v87, v47, s74\n"
    "v_mfma_f32_16x16x32_bf16 v[112:115], a[0:3], v[212:215], v[112:115]\n"
    "v_max_f32 v84, s74, v84\n"
    "v_max_f32 v85, s74, v85\n"
    "v_max_f32 v86, s74, v86\n"
    "v_mfma_f32_16x16x32_bf16 v[116:119], a[0:3], a[68:71], v[116:119]\n"
    "v_max_f32 v87, s74, v87\n"
    "v_add_f32 v84, v84, v28\n"
    "v_mfma_f32_16x16x32_bf16 v[120:123], a[0:3], a[140:143], v[120:123]\n"
    "v_add_f32 v85, v85, v29\n"
    "v_add_f32 v86, v86, v30\n"
    "v_mfma_f32_16x16x32_bf16 v[124:127], a[0:3], a[212:215], v[124:127]\n"
    "s_waitcnt lgkmcnt(4)\n"
    "v_add_f32 v87, v87, v31\n"
    "v_exp_f32_e64 v28, -v88 clamp\n"
    "v_mfma_f32_16x16x32_bf16 v[128:131], a[4:7], v[212:215], v[128:131]\n"
    "v_exp_f32_e64 v29, -v89 clamp\n"
    "v_mfma_f32_16x16x32_bf16 v[132:135], a[4:7], a[68:71], v[132:135]\n"
    "v_exp_f32_e64 v30, -v90 clamp\n"
    "v_mfma_f32_16x16x32_bf16 v[136:139], a[4:7], a[140:143], v[136:139]\n"
    "v_exp_f32_e64 v31, -v91 clamp\n"
    "v_mfma_f32_16x16x32_bf16 v[140:143], a[4:7], a[212:215], v[140:143]\n"
    "s_waitcnt lgkmcnt(2)\n"
    "v_fma_f32 v88, v88, v47, s74\n"
    "v_fma_f32 v89, v89, v47, s74\n"
    "v_fma_f32 v90, v90, v47, s74\n"
    "v_mfma_f32_16x16x32_bf16 v[144:147], a[8:11], v[212:215], v[144:147]\n"
    "v_fma_f32 v91, v91, v47, s74\n"
    "v_max_f32 v88, s74, v88\n"
    "v_mfma_f32_16x16x32_bf16 v[148:151], a[8:11], a[68:71], v[148:151]\n"
    "v_max_f32 v89, s74, v89\n"
    "v_max_f32 v90, s74, v90\n"
    "v_mfma_f32_16x16x32_bf16 v[152:155], a[8:11], a[140:143], v[152:155]\n"
    "v_max_f32 v91, s74, v91\n"
    "v_add_f32 v88, v88, v28\n"
    "v_add_f32 v89, v89, v29\n"
    "v_mfma_f32_16x16x32_bf16 v[156:159], a[8:11], a[212:215], v[156:159]\n"
    "s_waitcnt vmcnt(20) lgkmcnt(0)\n"
    "s_barrier\n"
    "ds_read_b64_tr_b16 a[36:37], v8 offset:32768\n"
    "ds_read_b64_tr_b16 a[38:39], v8 offset:33280\n"
    "v_mfma_f32_16x16x32_bf16 v[160:163], a[12:15], v[212:215], v[160:163]\n"
    "s_add_u32 m0, s56, 32768\n"
    "ds_read_b64_tr_b16 a[0:1], v9 offset:32768\n"
    "ds_read_b64_tr_b16 a[2:3], v9 offset:33280\n"
    "v_mfma_f32_16x16x32_bf16 v[164:167], a[12:15], a[68:71], v[164:167]\n"
    "s_add_u32 s58, s61, 589952\n"
    "ds_read_b64_tr_b16 a[4:5], v8 offset:40960\n"
    "v_add_f32 v90, v90, v30\n"
    "v_add_f32 v91, v91, v31\n"
    "v_mfma_f32_16x16x32_bf16 v[168:171], a[12:15], a[140:143], v[168:171]\n"
    "ds_read_b64_tr_b16 a[6:7], v8 offset:41472\n"
    "buffer_load_dwordx4 v2, s[36:39], s58 offen offset:0 lds\n"
    "v_mfma_f32_16x16x32_bf16 v[172:175], a[12:15], a[212:215], v[172:175]\n"
    "s_waitcnt lgkmcnt(4)\n"
    "ds_read_b64_tr_b16 a[8:9], v9 offset:40960\n"
    "v_mfma_f32_16x16x32_bf16 v[112:115], a[36:39], v[216:219], v[112:115]\n"
    "ds_read_b64_tr_b16 a[10:11], v9 offset:41472\n"
    "v_exp_f32_e64 v28, -v92 clamp\n"
    "v_mfma_f32_16x16x32_bf16 v[116:119], a[36:39], a[72:75], v[116:119]\n"
    "ds_read_b64_tr_b16 a[12:13], v8 offset:36864\n"
    "v_mfma_f32_16x16x32_bf16 v[120:123], a[36:39], a[144:147], v[120:123]\n"
    "ds_read_b64_tr_b16 a[14:15], v8 offset:37376\n"
    "v_exp_f32_e64 v29, -v93 clamp\n"
    "v_mfma_f32_16x16x32_bf16 v[124:127], a[36:39], a[216:219], v[124:127]\n"
    "s_waitcnt lgkmcnt(6)\n"
    "ds_read_b64_tr_b16 a[36:37], v9 offset:36864\n"
    "buffer_load_dwordx4 v3, s[36:39], s58 offen offset:1024 lds\n"
    "v_mfma_f32_16x16x32_bf16 v[128:131], a[0:3], v[216:219], v[128:131]\n"
    "ds_read_b64_tr_b16 a[38:39], v9 offset:37376\n"
    "v_exp_f32_e64 v30, -v94 clamp\n"
    "v_mfma_f32_16x16x32_bf16 v[132:135], a[0:3], a[72:75], v[132:135]\n"
    "v_exp_f32_e64 v31, -v95 clamp\n"
    "v_fma_f32 v92, v92, v47, s74\n"
    "v_mfma_f32_16x16x32_bf16 v[136:139], a[0:3], a[144:147], v[136:139]\n"
    "v_fma_f32 v93, v93, v47, s74\n"
    "v_fma_f32 v94, v94, v47, s74\n"
    "v_mfma_f32_16x16x32_bf16 v[140:143], a[0:3], a[216:219], v[140:143]\n"
    "s_waitcnt lgkmcnt(6)\n"
    "ds_read_b64_tr_b16 a[0:1], v8 offset:45056\n"
    "v_fma_f32 v95, v95, v47, s74\n"
    "v_max_f32 v92, s74, v92\n"
    "v_mfma_f32_16x16x32_bf16 v[144:147], a[4:7], v[216:219], v[144:147]\n"
    "ds_read_b64_tr_b16 a[2:3], v8 offset:45568\n"
    "buffer_load_dwordx4 v4, s[36:39], s58 offen offset:2048 lds\n"
    "v_mfma_f32_16x16x32_bf16 v[148:151], a[4:7], a[72:75], v[148:151]\n"
    "v_max_f32 v93, s74, v93\n"
    "v_max_f32 v94, s74, v94\n"
    "v_max_f32 v95, s74, v95\n"
    "v_mfma_f32_16x16x32_bf16 v[152:155], a[4:7], a[144:147], v[152:155]\n"
    "v_add_f32 v92, v92, v28\n"
    "v_add_f32 v93, v93, v29\n"
    "v_add_f32 v94, v94, v30\n"
    "v_mfma_f32_16x16x32_bf16 v[156:159], a[4:7], a[216:219], v[156:159]\n"
    "s_waitcnt lgkmcnt(6)\n"
    "ds_read_b64_tr_b16 a[4:5], v9 offset:45056\n"
    "v_add_f32 v95, v95, v31\n"
    "v_cvt_pk_bf16_f32 v80, v80, v81\n"
    "v_mfma_f32_16x16x32_bf16 v[160:163], a[8:11], v[216:219], v[160:163]\n"
    "ds_read_b64_tr_b16 a[6:7], v9 offset:45568\n"
    "v_cvt_pk_bf16_f32 v81, v82, v83\n"
    "v_mfma_f32_16x16x32_bf16 v[164:167], a[8:11], a[72:75], v[164:167]\n"
    "buffer_load_dwordx4 v5, s[36:39], s58 offen offset:3072 lds\n"
    "v_cvt_pk_bf16_f32 v82, v84, v85\n"
    "v_cvt_pk_bf16_f32 v83, v86, v87\n"
    "v_mfma_f32_16x16x32_bf16 v[168:171], a[8:11], a[144:147], v[168:171]\n"
    "v_cvt_pk_bf16_f32 v88, v88, v89\n"
    "v_cvt_pk_bf16_f32 v89, v90, v91\n"
    "v_cvt_pk_bf16_f32 v90, v92, v93\n"
    "v_cvt_pk_bf16_f32 v91, v94, v95\n"
    "v_mfma_f32_16x16x32_bf16 v[52:55], v[48:51], v[64:67], 0\n"
    "v_mfma_f32_16x16x32_bf16 v[172:175], a[8:11], a[216:219], v[172:175]\n"
    "v_mfma_f32_16x16x32_bf16 v[60:63], v[80:83], v[64:67], 0\n"
    "s_waitcnt lgkmcnt(6)\n"
    "v_mfma_f32_16x16x32_bf16 v[112:115], a[12:15], v[220:223], v[112:115]\n"
    "v_mfma_f32_16x16x32_bf16 v[84:87], v[48:51], v[68:71], 0\n"
    "v_mfma_f32_16x16x32_bf16 v[116:119], a[12:15], a[76:79], v[116:119]\n"
    "v_mfma_f32_16x16x32_bf16 v[92:95], v[80:83], v[68:71], 0\n"
    "v_mfma_f32_16x16x32_bf16 v[120:123], a[12:15], a[148:151], v[120:123]\n"
    "v_mfma_f32_16x16x32_bf16 v[52:55], v[56:59], v[72:75], v[52:55]\n"
    "v_mfma_f32_16x16x32_bf16 v[124:127], a[12:15], a[220:223], v[124:127]\n"
    "v_mfma_f32_16x16x32_bf16 v[60:63], v[88:91], v[72:75], v[60:63]\n"
    "s_waitcnt lgkmcnt(4)\n"
    "v_mfma_f32_16x16x32_bf16 v[128:131], a[36:39], v[220:223], v[128:131]\n"
    "v_mfma_f32_16x16x32_bf16 v[84:87], v[56:59], v[76:79], v[84:87]\n"
    "v_mfma_f32_16x16x32_bf16 v[132:135], a[36:39], a[76:79], v[132:135]\n"
    "v_mfma_f32_16x16x32_bf16 v[92:95], v[88:91], v[76:79], v[92:95]\n"
    "ds_write_b16 v32, v80 offset:0\n"
    "ds_write_b16_d16_hi v32, v80 offset:8\n"
    "v_mfma_f32_16x16x32_bf16 v[136:139], a[36:39], a[148:151], v[136:139]\n"
    "ds_write_b16 v32, v81 offset:16\n"
    "ds_write_b16_d16_hi v32, v81 offset:24\n"
    "v_mfma_f32_16x16x32_bf16 v[140:143], a[36:39], a[220:223], v[140:143]\n"
    "s_waitcnt lgkmcnt(6)\n"
    "ds_write_b16 v32, v82 offset:128\n"
    "ds_write_b16_d16_hi v32, v82 offset:136\n"
    "v_mfma_f32_16x16x32_bf16 v[144:147], a[0:3], v[220:223], v[144:147]\n"
    "ds_write_b16 v32, v83 offset:144\n"
    "ds_write_b16_d16_hi v32, v83 offset:152\n"
    "v_mfma_f32_16x16x32_bf16 v[148:151], a[0:3], a[76:79], v[148:151]\n"
    "v_mfma_f32_16x16x32_bf16 v[152:155], a[0:3], a[148:151], v[152:155]\n"
    "v_mfma_f32_16x16x32_bf16 v[156:159], a[0:3], a[220:223], v[156:159]\n"
    "s_waitcnt vmcnt(20) lgkmcnt(0)\n"
    "s_barrier\n"
    "ds_read_b64_tr_b16 a[8:9], v6 offset:0\n"
    "ds_read_b64_tr_b16 a[10:11], v6 offset:512\n"
    "v_mfma_f32_16x16x32_bf16 v[160:163], a[4:7], v[220:223], v[160:163]\n"
    "s_add_u32 m0, s56, 49152\n"
    "ds_read_b64_tr_b16 a[12:13], v7 offset:0\n"
    "ds_read_b64_tr_b16 a[14:15], v7 offset:512\n"
    "v_mfma_f32_16x16x32_bf16 v[164:167], a[4:7], a[76:79], v[164:167]\n"
    "s_add_u32 s58, s61, 1179776\n"
    "ds_read_b64_tr_b16 a[36:37], v6 offset:8192\n"
    "ds_write_b16 v32, v88 offset:256\n"
    "v_mfma_f32_16x16x32_bf16 v[168:171], a[4:7], a[148:151], v[168:171]\n"
    "ds_read_b64_tr_b16 a[38:39], v6 offset:8704\n"
    "buffer_load_dwordx4 v2, s[36:39], s58 offen offset:0 lds\n"
    "v_mfma_f32_16x16x32_bf16 v[172:175], a[4:7], a[220:223], v[172:175]\n"
    "s_waitcnt lgkmcnt(5)\n"
    "ds_read_b64_tr_b16 a[0:1], v7 offset:8192\n"
    "ds_write_b16_d16_hi v32, v88 offset:264\n"
    "v_mfma_f32_16x16x32_bf16 v[112:115], a[8:11], v[224:227], v[112:115]\n"
    "ds_read_b64_tr_b16 a[2:3], v7 offset:8704\n"
    "ds_write_b16 v32, v89 offset:272\n"
    "v_mfma_f32_16x16x32_bf16 v[116:119], a[8:11], a[80:83], v[116:119]\n"
    "ds_read_b64_tr_b16 a[4:5], v6 offset:4096\n"
    "ds_write_b16_d16_hi v32, v89 offset:280\n"
    "v_mfma_f32_16x16x32_bf16 v[120:123], a[8:11], a[152:155], v[120:123]\n"
    "ds_read_b64_tr_b16 a[6:7], v6 offset:4608\n"
    "ds_write_b16 v32, v90 offset:384\n"
    "v_mfma_f32_16x16x32_bf16 v[124:127], a[8:11], a[224:227], v[124:127]\n"
    "s_waitcnt lgkmcnt(11)\n"
    "ds_read_b64_tr_b16 a[8:9], v7 offset:4096\n"
    "buffer_load_dwordx4 v3, s[36:39], s58 offen offset:1024 lds\n"
    "v_mfma_f32_16x16x32_bf16 v[128:131], a[12:15], v[224:227], v[128:131]\n"
    "ds_read_b64_tr_b16 a[10:11], v7 offset:4608\n"
    "ds_write_b16_d16_hi v32, v90 offset:392\n"
    "v_mfma_f32_16x16x32_bf16 v[132:135], a[12:15], a[80:83], v[132:135]\n"
    "ds_write_b16 v32, v91 offset:400\n"
    "s_waitcnt lgkmcnt(14)\n"
    "ds_write_b16_d16_hi v32, v91 offset:408\n"
    "v_mfma_f32_16x16x32_bf16 v[136:139], a[12:15], a[152:155], v[136:139]\n"
    "v_cndmask_b32_e64 v64, v60, v62, s[82:83]\n"
    "v_cndmask_b32_e64 v65, v61, v63, s[82:83]\n"
    "v_mfma_f32_16x16x32_bf16 v[140:143], a[12:15], a[224:227], v[140:143]\n"
    "s_waitcnt lgkmcnt(14)\n"
    "ds_read_b64_tr_b16 a[12:13], v6 offset:12288\n"
    "v_cndmask_b32_e64 v66, v92, v94, s[82:83]\n"
    "s_waitcnt lgkmcnt(14)\n"
    "v_mfma_f32_16x16x32_bf16 v[144:147], a[36:39], v[224:227], v[144:147]\n"
    "ds_read_b64_tr_b16 a[14:15], v6 offset:12800\n"
    "buffer_load_dwordx4 v4, s[36:39], s58 offen offset:2048 lds\n"
    "v_mfma_f32_16x16x32_bf16 v[148:151], a[36:39], a[80:83], v[148:151]\n"
    "v_cndmask_b32_e64 v67, v93, v95, s[82:83]\n"
    "v_mfma_f32_16x16x32_bf16 v[152:155], a[36:39], a[152:155], v[152:155]\n"
    "s_waitcnt lgkmcnt(14)\n"
    "ds_write_b128 v36, v[52:55] offset:0\n"
    "v_mfma_f32_16x16x32_bf16 v[156:159], a[36:39], a[224:227], v[156:159]\n"
    "s_waitcnt lgkmcnt(14)\n"
    "ds_read_b64_tr_b16 a[36:37], v7 offset:12288\n"
    "s_waitcnt lgkmcnt(14)\n"
    "v_mfma_f32_16x16x32_bf16 v[160:163], a[0:3], v[224:227], v[160:163]\n"
    "ds_read_b64_tr_b16 a[38:39], v7 offset:12800\n"
    "v_mfma_f32_16x16x32_bf16 v[164:167], a[0:3], a[80:83], v[164:167]\n"
    "buffer_load_dwordx4 v5, s[36:39], s58 offen offset:3072 lds\n"
    "v_mfma_f32_16x16x32_bf16 v[168:171], a[0:3], a[152:155], v[168:171]\n"
    "s_waitcnt lgkmcnt(14)\n"
    "ds_write_b128 v36, v[84:87] offset:1024\n"
    "v_mfma_f32_16x16x32_bf16 v[172:175], a[0:3], a[224:227], v[172:175]\n"
    "s_waitcnt lgkmcnt(14)\n"
    "ds_write_b128 v36, v[64:67] offset:2048\n"
    "s_waitcnt lgkmcnt(13)\n"
    "v_mfma_f32_16x16x32_bf16 v[112:115], a[4:7], v[228:231], v[112:115]\n"
    "v_mfma_f32_16x16x32_bf16 v[116:119], a[4:7], a[84:87], v[116:119]\n"
    "v_mfma_f32_16x16x32_bf16 v[120:123], a[4:7], a[156:159], v[120:123]\n"
    "v_mfma_f32_16x16x32_bf16 v[124:127], a[4:7], a[228:231], v[124:127]\n"
    "s_waitcnt lgkmcnt(10)\n"
    "v_mfma_f32_16x16x32_bf16 v[128:131], a[8:11], v[228:231], v[128:131]\n"
    "v_mfma_f32_16x16x32_bf16 v[132:135], a[8:11], a[84:87], v[132:135]\n"
    "v_mfma_f32_16x16x32_bf16 v[136:139], a[8:11], a[156:159], v[136:139]\n"
    "v_mfma_f32_16x16x32_bf16 v[140:143], a[8:11], a[228:231], v[140:143]\n"
    "s_waitcnt lgkmcnt(5)\n"
    "v_mfma_f32_16x16x32_bf16 v[144:147], a[12:15], v[228:231], v[144:147]\n"
    "v_mfma_f32_16x16x32_bf16 v[148:151], a[12:15], a[84:87], v[148:151]\n"
    "v_mfma_f32_16x16x32_bf16 v[152:155], a[12:15], a[156:159], v[152:155]\n"
    "v_mfma_f32_16x16x32_bf16 v[156:159], a[12:15], a[228:231], v[156:159]\n"
    "s_waitcnt vmcnt(20) lgkmcnt(0)\n"
    "s_barrier\n"
    "ds_read_b64_tr_b16 a[0:1], v8 offset:49152\n"
    "ds_read_b64_tr_b16 a[2:3], v8 offset:49664\n"
    "v_mfma_f32_16x16x32_bf16 v[160:163], a[36:39], v[228:231], v[160:163]\n"
    "s_add_u32 m0, s56, 4294926336\n"
    "ds_read_b64_tr_b16 a[4:5], v9 offset:49152\n"
    "ds_read_b64_tr_b16 a[6:7], v9 offset:49664\n"
    "v_mfma_f32_16x16x32_bf16 v[164:167], a[36:39], a[84:87], v[164:167]\n"
    "s_add_u32 s58, s61, 1769600\n"
    "ds_read_b64_tr_b16 a[8:9], v8 offset:57344\n"
    "ds_read_b128 v[72:75], v37 offset:0\n"
    "v_mfma_f32_16x16x32_bf16 v[168:171], a[36:39], a[156:159], v[168:171]\n"
    "ds_read_b64_tr_b16 a[10:11], v8 offset:57856\n"
    "buffer_load_dwordx4 v2, s[36:39], s58 offen offset:0 lds\n"
    "v_mfma_f32_16x16x32_bf16 v[172:175], a[36:39], a[228:231], v[172:175]\n"
    "s_waitcnt lgkmcnt(5)\n"
    "ds_read_b64_tr_b16 a[12:13], v9 offset:57344\n"
    "ds_read_b128 v[68:71], v37 offset:1024\n"
    "v_mfma_f32_16x16x32_bf16 v[112:115], a[0:3], v[232:235], v[112:115]\n"
    "ds_read_b64_tr_b16 a[14:15], v9 offset:57856\n"
    "ds_read_b128 v[76:79], v37 offset:2048\n"
    "v_mfma_f32_16x16x32_bf16 v[116:119], a[0:3], a[88:91], v[116:119]\n"
    "ds_read_b64_tr_b16 a[36:37], v8 offset:53248\n"
    "s_waitcnt lgkmcnt(6)\n"
    "v_add_f32 v52, v52, v72\n"
    "v_mfma_f32_16x16x32_bf16 v[120:123], a[0:3], a[160:163], v[120:123]\n"
    "ds_read_b64_tr_b16 a[38:39], v8 offset:53760\n"
    "v_add_f32 v53, v53, v73\n"
    "v_mfma_f32_16x16x32_bf16 v[124:127], a[0:3], a[232:235], v[124:127]\n"
    "ds_read_b64_tr_b16 a[0:1], v9 offset:53248\n"
    "buffer_load_dwordx4 v3, s[36:39], s58 offen offset:1024 lds\n"
    "v_mfma_f32_16x16x32_bf16 v[128:131], a[4:7], v[232:235], v[128:131]\n"
    "ds_read_b64_tr_b16 a[2:3], v9 offset:53760\n"
    "v_add_f32 v54, v54, v74\n"
    "v_mfma_f32_16x16x32_bf16 v[132:135], a[4:7], a[88:91], v[132:135]\n"
    "v_add_f32 v55, v55, v75\n"
    "s_waitcnt lgkmcnt(6)\n"
    "v_add_f32 v84, v84, v68\n"
    "v_mfma_f32_16x16x32_bf16 v[136:139], a[4:7], a[160:163], v[136:139]\n"
    "v_add_f32 v85, v85, v69\n"
    "v_add_f32 v86, v86, v70\n"
    "v_mfma_f32_16x16x32_bf16 v[140:143], a[4:7], a[232:235], v[140:143]\n"
    "ds_read_b64_tr_b16 a[4:5], v8 offset:61440\n"
    "v_add_f32 v87, v87, v71\n"
    "v_mfma_f32_16x16x32_bf16 v[144:147], a[8:11], v[232:235], v[144:147]\n"
    "ds_read_b64_tr_b16 a[6:7], v8 offset:61952\n"
    "buffer_load_dwordx4 v4, s[36:39], s58 offen offset:2048 lds\n"
    "v_mfma_f32_16x16x32_bf16 v[148:151], a[8:11], a[88:91], v[148:151]\n"
    "s_waitcnt lgkmcnt(6)\n"
    "v_add_f32 v64, v64, v76\n"
    "v_add_f32 v65, v65, v77\n"
    "v_mfma_f32_16x16x32_bf16 v[152:155], a[8:11], a[160:163], v[152:155]\n"
    "v_add_f32 v66, v66, v78\n"
    "v_add_f32 v67, v67, v79\n"
    "v_mfma_f32_16x16x32_bf16 v[156:159], a[8:11], a[232:235], v[156:159]\n"
    "ds_read_b64_tr_b16 a[8:9], v9 offset:61440\n"
    "buffer_store_dwordx4 v[52:55], v38, s[48:51], s70 offen offset:0 sc1\n"
    "v_mfma_f32_16x16x32_bf16 v[160:163], a[12:15], v[232:235], v[160:163]\n"
    "ds_read_b64_tr_b16 a[10:11], v9 offset:61952\n"
    "buffer_store_dwordx4 v[84:87], v38, s[48:51], s70 offen offset:1024 sc1\n"
    "v_mfma_f32_16x16x32_bf16 v[164:167], a[12:15], a[88:91], v[164:167]\n"
    "buffer_load_dwordx4 v5, s[36:39], s58 offen offset:3072 lds\n"
    "v_mfma_f32_16x16x32_bf16 v[168:171], a[12:15], a[160:163], v[168:171]\n"
    "buffer_store_dwordx4 v[64:67], v39, s[48:51], s70 offen offset:0 sc1\n"
    "v_mfma_f32_16x16x32_bf16 v[172:175], a[12:15], a[232:235], v[172:175]\n"
    "s_waitcnt lgkmcnt(6)\n"
    "v_exp_f32_e64 v28, -v96 clamp\n"
    "v_mfma_f32_16x16x32_bf16 v[112:115], a[36:39], v[236:239], v[112:115]\n"
    "v_exp_f32_e64 v29, -v97 clamp\n"
    "v_mfma_f32_16x16x32_bf16 v[116:119], a[36:39], a[92:95], v[116:119]\n"
    "v_exp_f32_e64 v30, -v98 clamp\n"
    "v_mfma_f32_16x16x32_bf16 v[120:123], a[36:39], a[164:167], v[120:123]\n"
    "v_exp_f32_e64 v31, -v99 clamp\n"
    "v_mfma_f32_16x16x32_bf16 v[124:127], a[36:39], a[236:239], v[124:127]\n"
    "s_waitcnt lgkmcnt(4)\n"
    "v_fma_f32 v96, v96, v47, s74\n"
    "v_fma_f32 v97, v97, v47, s74\n"
    "v_mfma_f32_16x16x32_bf16 v[128:131], a[0:3], v[236:239], v[128:131]\n"
    "v_fma_f32 v98, v98, v47, s74\n"
    "v_fma_f32 v99, v99, v47, s74\n"
    "v_mfma_f32_16x16x32_bf16 v[132:135], a[0:3], a[92:95], v[132:135]\n"
    "v_max_f32 v96, s74, v96\n"
    "v_max_f32 v97, s74, v97\n"
    "v_mfma_f32_16x16x32_bf16 v[136:139], a[0:3], a[164:167], v[136:139]\n"
    "v_max_f32 v98, s74, v98\n"
    "v_max_f32 v99, s74, v99\n"
    "v_mfma_f32_16x16x32_bf16 v[140:143], a[0:3], a[236:239], v[140:143]\n"
    "s_waitcnt lgkmcnt(2)\n"
    "v_add_f32 v96, v96, v28\n"
    "v_add_f32 v97, v97, v29\n"
    "v_mfma_f32_16x16x32_bf16 v[144:147], a[4:7], v[236:239], v[144:147]\n"
    "v_add_f32 v98, v98, v30\n"
    "v_add_f32 v99, v99, v31\n"
    "v_mfma_f32_16x16x32_bf16 v[148:151], a[4:7], a[92:95], v[148:151]\n"
    "v_exp_f32_e64 v28, -v100 clamp\n"
    "v_mfma_f32_16x16x32_bf16 v[152:155], a[4:7], a[164:167], v[152:155]\n"
    "v_exp_f32_e64 v29, -v101 clamp\n"
    "v_mfma_f32_16x16x32_bf16 v[156:159], a[4:7], a[236:239], v[156:159]\n"
    "s_waitcnt vmcnt(23) lgkmcnt(0)\n"
    "s_barrier\n"
    "ds_read_b64_tr_b16 a[12:13], v6 offset:16384\n"
    "ds_read_b64_tr_b16 a[14:15], v6 offset:16896\n"
    "v_mfma_f32_16x16x32_bf16 v[160:163], a[8:11], v[236:239], v[160:163]\n"
    "s_add_u32 m0, s56, 65536\n"
    "ds_read_b64_tr_b16 a[36:37], v7 offset:16384\n"
    "ds_read_b64_tr_b16 a[38:39], v7 offset:16896\n"
    "v_mfma_f32_16x16x32_bf16 v[164:167], a[8:11], a[92:95], v[164:167]\n"
    "s_add_u32 s58, s61, 2359424\n"
    "ds_read_b64_tr_b16 a[0:1], v6 offset:24576\n"
    "v_mfma_f32_16x16x32_bf16 v[168:171], a[8:11], a[164:167], v[168:171]\n"
    "ds_read_b64_tr_b16 a[2:3], v6 offset:25088\n"
    "buffer_load_dwordx4 v2, s[36:39], s58 offen offset:0 lds\n"
    "v_mfma_f32_16x16x32_bf16 v[172:175], a[8:11], a[236:239], v[172:175]\n"
    "s_waitcnt lgkmcnt(4)\n"
    "ds_read_b64_tr_b16 a[4:5], v7 offset:24576\n"
    "v_mfma_f32_16x16x32_bf16 v[112:115], a[12:15], v[240:243], v[112:115]\n"
    "ds_read_b64_tr_b16 a[6:7], v7 offset:25088\n"
    "v_mfma_f32_16x16x32_bf16 v[116:119], a[12:15], a[96:99], v[116:119]\n"
    "ds_read_b64_tr_b16 a[8:9], v6 offset:20480\n"
    "v_mfma_f32_16x16x32_bf16 v[120:123], a[12:15], a[168:171], v[120:123]\n"
    "ds_read_b64_tr_b16 a[10:11], v6 offset:20992\n"
    "v_mfma_f32_16x16x32_bf16 v[124:127], a[12:15], a[240:243], v[124:127]\n"
    "s_waitcnt lgkmcnt(6)\n"
    "ds_read_b64_tr_b16 a[12:13], v7 offset:20480\n"
    "buffer_load_dwordx4 v3, s[36:39], s58 offen offset:1024 lds\n"
    "v_mfma_f32_16x16x32_bf16 v[128:131], a[36:39], v[240:243], v[128:131]\n"
    "ds_read_b64_tr_b16 a[14:15], v7 offset:20992\n"
    "v_mfma_f32_16x16x32_bf16 v[132:135], a[36:39], a[96:99], v[132:135]\n"
    "v_exp_f32_e64 v30, -v102 clamp\n"
    "v_mfma_f32_16x16x32_bf16 v[136:139], a[36:39], a[168:171], v[136:139]\n"
    "v_exp_f32_e64 v31, -v103 clamp\n"
    "v_mfma_f32_16x16x32_bf16 v[140:143], a[36:39], a[240:243], v[140:143]\n"
    "s_waitcnt lgkmcnt(6)\n"
    "ds_read_b64_tr_b16 a[36:37], v6 offset:28672\n"
    "v_fma_f32 v100, v100, v47, s74\n"
    "v_mfma_f32_16x16x32_bf16 v[144:147], a[0:3], v[240:243], v[144:147]\n"
    "ds_read_b64_tr_b16 a[38:39], v6 offset:29184\n"
    "buffer_load_dwordx4 v4, s[36:39], s58 offen offset:2048 lds\n"
    "v_mfma_f32_16x16x32_bf16 v[148:151], a[0:3], a[96:99], v[148:151]\n"
    "v_fma_f32 v101, v101, v47, s74\n"
    "v_fma_f32 v102, v102, v47, s74\n"
    "v_fma_f32 v103, v103, v47, s74\n"
    "v_mfma_f32_16x16x32_bf16 v[152:155], a[0:3], a[168:171], v[152:155]\n"
    "v_max_f32 v100, s74, v100\n"
    "v_max_f32 v101, s74, v101\n"
    "v_mfma_f32_16x16x32_bf16 v[156:159], a[0:3], a[240:243], v[156:159]\n"
    "s_waitcnt lgkmcnt(6)\n"
    "ds_read_b64_tr_b16 a[0:1], v7 offset:28672\n"
    "v_max_f32 v102, s74, v102\n"
    "v_mfma_f32_16x16x32_bf16 v[160:163], a[4:7], v[240:243], v[160:163]\n"
    "ds_read_b64_tr_b16 a[2:3], v7 offset:29184\n"
    "v_max_f32 v103, s74, v103\n"
    "v_mfma_f32_16x16x32_bf16 v[164:167], a[4:7], a[96:99], v[164:167]\n"
    "s_and_b32 s59, s23, 15\n"
    "buffer_load_dwordx4 v5, s[36:39], s58 offen offset:3072 lds\n"
    "s_lshl_b32 s59, s59, 1\n"
    "v_mfma_f32_16x16x32_bf16 v[168:171], a[4:7], a[168:171], v[168:171]\n"
    "s_add_u32 s59, s59, s25\n"
    "s_mul_i32 s59, s59, 165888\n"
    "s_mul_i32 s58, s25, 294912\n"
    "v_mfma_f32_16x16x32_bf16 v[172:175], a[4:7], a[240:243], v[172:175]\n"
    "s_waitcnt lgkmcnt(6)\n"
    "s_sub_u32 s59, s59, s58\n"
    "s_add_u32 s59, s59, s61\n"
    "v_mfma_f32_16x16x32_bf16 v[112:115], a[8:11], v[244:247], v[112:115]\n"
    "s_add_u32 s59, s59, 256\n"
    "v_min_u32 v1, 17, v0\n"
    "v_mul_u32_u24 v1, 9216, v1\n"
    "v_mfma_f32_16x16x32_bf16 v[116:119], a[8:11], a[100:103], v[116:119]\n"
    "buffer_load_dword a34, v1, s[36:39], s59 offen offset:0\n"
    "v_add_f32 v100, v100, v28\n"
    "v_mfma_f32_16x16x32_bf16 v[120:123], a[8:11], a[172:175], v[120:123]\n"
    "v_add_f32 v101, v101, v29\n"
    "v_add_f32 v102, v102, v30\n"
    "v_add_f32 v103, v103, v31\n"
    "v_mfma_f32_16x16x32_bf16 v[124:127], a[8:11], a[244:247], v[124:127]\n"
    "s_waitcnt lgkmcnt(4)\n"
    "v_exp_f32_e64 v28, -v104 clamp\n"
    "v_mfma_f32_16x16x32_bf16 v[128:131], a[12:15], v[244:247], v[128:131]\n"
    "v_exp_f32_e64 v29, -v105 clamp\n"
    "v_mfma_f32_16x16x32_bf16 v[132:135], a[12:15], a[100:103], v[132:135]\n"
    "v_exp_f32_e64 v30, -v106 clamp\n"
    "v_mfma_f32_16x16x32_bf16 v[136:139], a[12:15], a[172:175], v[136:139]\n"
    "v_exp_f32_e64 v31, -v107 clamp\n"
    "v_mfma_f32_16x16x32_bf16 v[140:143], a[12:15], a[244:247], v[140:143]\n"
    "s_waitcnt lgkmcnt(2)\n"
    "v_fma_f32 v104, v104, v47, s74\n"
    "v_fma_f32 v105, v105, v47, s74\n"
    "v_mfma_f32_16x16x32_bf16 v[144:147], a[36:39], v[244:247], v[144:147]\n"
    "v_fma_f32 v106, v106, v47, s74\n"
    "L_hop4:\n"
    "s_cbranch_execz L_hop8\n"
    "v_fma_f32 v107, v107, v47, s74\n"
    "v_mfma_f32_16x16x32_bf16 v[148:151], a[36:39], a[100:103], v[148:151]\n"
    "v_max_f32 v104, s74, v104\n"
    "v_max_f32 v105, s74, v105\n"
    "v_max_f32 v106, s74, v106\n"
    "v_mfma_f32_16x16x32_bf16 v[152:155], a[36:39], a[172:175], v[152:155]\n"
    "v_max_f32 v107, s74, v107\n"
    "v_add_f32 v104, v104, v28\n"
    "v_mfma_f32_16x16x32_bf16 v[156:159], a[36:39], a[244:247], v[156:159]\n"
    "s_waitcnt vmcnt(24) lgkmcnt(0)\n"
    "s_barrier\n"
    "ds_read_b64_tr_b16 a[4:5], v6 offset:40960\n"
    "ds_read_b64_tr_b16 a[6:7], v6 offset:41472\n"
    "v_mfma_f32_16x16x32_bf16 v[160:163], a[0:3], v[244:247], v[160:163]\n"
    "s_add_u32 m0, s56, 4294942720\n"
    "ds_read_b64_tr_b16 a[8:9], v7 offset:40960\n"
    "ds_read_b64_tr_b16 a[10:11], v7 offset:41472\n"
    "v_mfma_f32_16x16x32_bf16 v[164:167], a[0:3], a[100:103], v[164:167]\n"
    "s_add_u32 s58, s61, 2949248\n"
    "ds_read_b64_tr_b16 a[12:13], v6 offset:49152\n"
    "buffer_load_dwordx3 v[42:44], v46, s[48:51], 0 offen offset:0 sc1\n"
    "v_mfma_f32_16x16x32_bf16 v[168:171], a[0:3], a[172:175], v[168:171]\n"
    "ds_read_b64_tr_b16 a[14:15], v6 offset:49664\n"
    "buffer_load_dwordx4 v2, s[36:39], s58 offen offset:0 lds\n"
    "v_mfma_f32_16x16x32_bf16 v[172:175], a[0:3], a[244:247], v[172:175]\n"
    "s_waitcnt lgkmcnt(4)\n"
    "ds_read_b64_tr_b16 a[36:37], v7 offset:49152\n"
    "v_add_f32 v105, v105, v29\n"
    "v_mfma_f32_16x16x32_bf16 v[112:115], a[4:7], v[248:251], v[112:115]\n"
    "ds_read_b64_tr_b16 a[38:39], v7 offset:49664\n"
    "v_add_f32 v106, v106, v30\n"
    "v_mfma_f32_16x16x32_bf16 v[116:119], a[4:7], a[104:107], v[116:119]\n"
    "ds_read_b64_tr_b16 a[0:1], v6 offset:45056\n"
    "v_add_f32 v107, v107, v31\n"
    "v_mfma_f32_16x16x32_bf16 v[120:123], a[4:7], a[176:179], v[120:123]\n"
    "ds_read_b64_tr_b16 a[2:3], v6 offset:45568\n"
    "v_mfma_f32_16x16x32_bf16 v[124:127], a[4:7], a[248:251], v[124:127]\n"
    "s_waitcnt lgkmcnt(6)\n"
    "ds_read_b64_tr_b16 a[4:5], v7 offset:45056\n"
    "buffer_load_dwordx4 v3, s[36:39], s58 offen offset:1024 lds\n"
    "v_mfma_f32_16x16x32_bf16 v[128:131], a[8:11], v[248:251], v[128:131]\n"
    "ds_read_b64_tr_b16 a[6:7], v7 offset:45568\n"
    "v_mfma_f32_16x16x32_bf16 v[132:135], a[8:11], a[104:107], v[132:135]\n"
    "v_exp_f32_e64 v28, -v108 clamp\n"
    "v_mfma_f32_16x16x32_bf16 v[136:139], a[8:11], a[176:179], v[136:139]\n"
    "v_exp_f32_e64 v29, -v109 clamp\n"
    "v_mfma_f32_16x16x32_bf16 v[140:143], a[8:11], a[248:251], v[140:143]\n"
    "s_waitcnt lgkmcnt(6)\n"
    "ds_read_b64_tr_b16 a[8:9], v6 offset:53248\n"
    "v_mfma_f32_16x16x32_bf16 v[144:147], a[12:15], v[248:251], v[144:147]\n"
    "ds_read_b64_tr_b16 a[10:11], v6 offset:53760\n"
    "buffer_load_dwordx4 v4, s[36:39], s58 offen offset:2048 lds\n"
    "v_mfma_f32_16x16x32_bf16 v[148:151], a[12:15], a[104:107], v[148:151]\n"
    "v_exp_f32_e64 v30, -v110 clamp\n"
    "v_mfma_f32_16x16x32_bf16 v[152:155], a[12:15], a[176:179], v[152:155]\n"
    "v_exp_f32_e64 v31, -v111 clamp\n"
    "v_mfma_f32_16x16x32_bf16 v[156:159], a[12:15], a[248:251], v[156:159]\n"
    "s_waitcnt lgkmcnt(6)\n"
    "ds_read_b64_tr_b16 a[12:13], v7 offset:53248\n"
    "v_fma_f32 v108, v108, v47, s74\n"
    "v_fma_f32 v109, v109, v47, s74\n"
    "v_mfma_f32_16x16x32_bf16 v[160:163], a[36:39], v[248:251], v[160:163]\n"
    "ds_read_b64_tr_b16 a[14:15], v7 offset:53760\n"
    "v_fma_f32 v110, v110, v47, s74\n"
    "v_mfma_f32_16x16x32_bf16 v[164:167], a[36:39], a[104:107], v[164:167]\n"
    "buffer_load_dwordx4 v5, s[36:39], s58 offen offset:3072 lds\n"
    "v_fma_f32 v111, v111, v47, s74\n"
    "v_mfma_f32_16x16x32_bf16 v[168:171], a[36:39], a[176:179], v[168:171]\n"
    "v_max_f32 v108, s74, v108\n"
    "v_max_f32 v109, s74, v109\n"
    "v_mfma_f32_16x16x32_bf16 v[172:175], a[36:39], a[248:251], v[172:175]\n"
    "s_waitcnt lgkmcnt(6)\n"
    "v_max_f32 v110, s74, v110\n"
    "v_max_f32 v111, s74, v111\n"
    "v_add_f32 v108, v108, v28\n"
    "v_mfma_f32_16x16x32_bf16 v[112:115], a[0:3], v[252:255], v[112:115]\n"
    "v_add_f32 v109, v109, v29\n"
    "v_add_f32 v110, v110, v30\n"
    "v_mfma_f32_16x16x32_bf16 v[116:119], a[0:3], a[108:111], v[116:119]\n"
    "v_add_f32 v111, v111, v31\n"
    "v_cvt_pk_bf16_f32 v28, v96, v97\n"
    "v_mfma_f32_16x16x32_bf16 v[120:123], a[0:3], a[180:183], v[120:123]\n"
    "v_cvt_pk_bf16_f32 v29, v98, v99\n"
    "v_accvgpr_write_b32 a16, v28\n"
    "v_accvgpr_write_b32 a17, v29\n"
    "v_mfma_f32_16x16x32_bf16 v[124:127], a[0:3], a[252:255], v[124:127]\n"
    "s_waitcnt lgkmcnt(4)\n"
    "v_accvgpr_write_b32 a19, 0\n"
    "ds_read_b32 a18, v33 offset:0\n"
    "v_mfma_f32_16x16x32_bf16 v[128:131], a[4:7], v[252:255], v[128:131]\n"
    "v_cvt_pk_bf16_f32 v30, v100, v101\n"
    "v_cvt_pk_bf16_f32 v31, v102, v103\n"
    "v_mfma_f32_16x16x32_bf16 v[132:135], a[4:7], a[108:111], v[132:135]\n"
    "v_accvgpr_write_b32 a20, v30\n"
    "v_accvgpr_write_b32 a21, v31\n"
    "v_accvgpr_write_b32 a23, 0\n"
    "v_mfma_f32_16x16x32_bf16 v[136:139], a[4:7], a[180:183], v[136:139]\n"
    "ds_read_b32 a22, v33 offset:128\n"
    "v_cvt_pk_bf16_f32 v28, v104, v105\n"
    "v_mfma_f32_16x16x32_bf16 v[140:143], a[4:7], a[252:255], v[140:143]\n"
    "s_waitcnt lgkmcnt(4)\n"
    "v_cvt_pk_bf16_f32 v29, v106, v107\n"
    "v_accvgpr_write_b32 a24, v28\n"
    "v_mfma_f32_16x16x32_bf16 v[144:147], a[8:11], v[252:255], v[144:147]\n"
    "v_accvgpr_write_b32 a25, v29\n"
    "v_accvgpr_write_b32 a27, 0\n"
    "v_mfma_f32_16x16x32_bf16 v[148:151], a[8:11], a[108:111], v[148:151]\n"
    "v_mfma_f32_16x16x32_bf16 v[152:155], a[8:11], a[180:183], v[152:155]\n"
    "v_mfma_f32_16x16x32_bf16 v[156:159], a[8:11], a[252:255], v[156:159]\n"
    "s_waitcnt vmcnt(25) lgkmcnt(0)\n"
    "s_barrier\n"
    "ds_read_b64_tr_b16 a[36:37], v8 offset:0\n"
    "ds_read_b64_tr_b16 a[38:39], v8 offset:512\n"
    "s_add_u32 m0, s56, 0\n"
    "v_mfma_f32_16x16x32_bf16 v[160:163], a[12:15], v[252:255], v[160:163]\n"
    "s_add_u32 s58, s61, 3539072\n"
    "ds_read_b64_tr_b16 a[0:1], v9 offset:0\n"
    "ds_read_b64_tr_b16 a[2:3], v9 offset:512\n"
    "ds_read_b32 a26, v33 offset:256\n"
    "v_cvt_pk_bf16_f32 v30, v108, v109\n"
    "v_cvt_pk_bf16_f32 v31, v110, v111\n"
    "v_mfma_f32_16x16x32_bf16 v[164:167], a[12:15], a[108:111], v[164:167]\n"
    "s_add_u32 s89, s27, 1\n"
    "ds_read_b64_tr_b16 a[4:5], v8 offset:8192\n"
    "v_accvgpr_write_b32 a28, v30\n"
    "v_accvgpr_write_b32 a29, v31\n"
    "v_accvgpr_write_b32 a31, 0\n"
    "ds_read_b32 a30, v33 offset:384\n"
    "v_mfma_f32_16x16x32_bf16 v[168:171], a[12:15], a[180:183], v[168:171]\n"
    "ds_read_b64_tr_b16 a[6:7], v8 offset:8704\n"
    "buffer_load_dwordx4 v2, s[36:39], s58 offen offset:0 lds\n"
    "v_mfma_f32_16x16x32_bf16 v[172:175], a[12:15], a[252:255], v[172:175]\n"
    "s_waitcnt lgkmcnt(6)\n"
    "ds_read_b64_tr_b16 a[8:9], v9 offset:8192\n"
    "v_mov_b32 v20, 0x80000000\n"
    "v_mfma_f32_16x16x32_bf16 v[48:51], a[36:39], v[184:187], 0\n"
    "ds_read_b64_tr_b16 a[10:11], v9 offset:8704\n"
    "v_writelane_b32 v20, s26, 0\n"
    "v_mfma_f32_16x16x32_bf16 v[52:55], a[36:39], a[40:43], 0\n"
    "ds_read_b64_tr_b16 a[12:13], v8 offset:4096\n"
    "v_mov_b32 v21, s89\n"
    "v_mfma_f32_16x16x32_bf16 v[56:59], a[36:39], a[112:115], 0\n"
    "s_add_u32 s60, s92, 0\n"
    "ds_read_b64_tr_b16 a[14:15], v8 offset:4608\n"
    "v_mfma_f32_16x16x32_bf16 v[60:63], a[36:39], a[184:187], 0\n"
    "s_waitcnt lgkmcnt(8)\n"
    "ds_read_b64_tr_b16 a[36:37], v9 offset:4096\n"
    "buffer_load_dwordx4 v3, s[36:39], s58 offen offset:1024 lds\n"
    "v_mfma_f32_16x16x32_bf16 v[64:67], a[0:3], v[184:187], 0\n"
    "ds_read_b64_tr_b16 a[38:39], v9 offset:4608\n"
    "v_mfma_f32_16x16x32_bf16 v[68:71], a[0:3], a[40:43], 0\n"
    "v_exp_f32 v12, v112\n"
    "v_mfma_f32_16x16x32_bf16 v[72:75], a[0:3], a[112:115], 0\n"
    "v_exp_f32 v13, v113\n"
    "v_mfma_f32_16x16x32_bf16 v[76:79], a[0:3], a[184:187], 0\n"
    "s_waitcnt lgkmcnt(6)\n"
    "ds_read_b64_tr_b16 a[0:1], v8 offset:12288\n"
    "v_mfma_f32_16x16x32_bf16 v[80:83], a[4:7], v[184:187], 0\n"
    "ds_read_b64_tr_b16 a[2:3], v8 offset:12800\n"
    "buffer_load_dwordx4 v4, s[36:39], s58 offen offset:2048 lds\n"
    "v_mfma_f32_16x16x32_bf16 v[84:87], a[4:7], a[40:43], 0\n"
    "v_exp_f32 v14, v114\n"
    "v_mfma_f32_16x16x32_bf16 v[88:91], a[4:7], a[112:115], 0\n"
    "v_exp_f32 v15, v115\n"
    "v_mfma_f32_16x16x32_bf16 v[92:95], a[4:7], a[184:187], 0\n"
    "s_waitcnt lgkmcnt(6)\n"
    "ds_read_b64_tr_b16 a[4:5], v9 offset:12288\n"
    "v_mul_f32 v144, v112, v144\n"
    "v_mfma_f32_16x16x32_bf16 v[96:99], a[8:11], v[184:187], 0\n"
    "ds_read_b64_tr_b16 a[6:7], v9 offset:12800\n"
    "v_mul_f32 v145, v113, v145\n"
    "v_mfma_f32_16x16x32_bf16 v[100:103], a[8:11], a[40:43], 0\n"
    "buffer_load_dwordx4 v5, s[36:39], s58 offen offset:3072 lds\n"
    "v_mul_f32 v146, v114, v146\n"
    "v_mfma_f32_16x16x32_bf16 v[104:107], a[8:11], a[112:115], 0\n"
    "v_mul_f32 v147, v115, v147\n"
    "v_add_f32 v12, 1.0, v12\n"
    "v_mfma_f32_16x16x32_bf16 v[108:111], a[8:11], a[184:187], 0\n"
    "s_waitcnt lgkmcnt(6)\n"
    "v_add_f32 v13, 1.0, v13\n"
    "v_add_f32 v14, 1.0, v14\n"
    "v_mfma_f32_16x16x32_bf16 v[48:51], a[12:15], v[188:191], v[48:51]\n"
    "v_add_f32 v15, 1.0, v15\n"
    "v_mfma_f32_16x16x32_bf16 v[52:55], a[12:15], a[44:47], v[52:55]\n"
    "v_rcp_f32 v12, v12\n"
    "v_mfma_f32_16x16x32_bf16 v[56:59], a[12:15], a[116:119], v[56:59]\n"
    "v_rcp_f32 v13, v13\n"
    "v_mfma_f32_16x16x32_bf16 v[60:63], a[12:15], a[188:191], v[60:63]\n"
    "s_waitcnt lgkmcnt(4)\n"
    "v_rcp_f32 v14, v14\n"
    "v_mfma_f32_16x16x32_bf16 v[64:67], a[36:39], v[188:191], v[64:67]\n"
    "v_rcp_f32 v15, v15\n"
    "v_mfma_f32_16x16x32_bf16 v[68:71], a[36:39], a[44:47], v[68:71]\n"
    "v_mul_f32 v144, v144, v12\n"
    "v_mul_f32 v145, v145, v13\n"
    "v_mfma_f32_16x16x32_bf16 v[72:75], a[36:39], a[116:119], v[72:75]\n"
    "v_mul_f32 v146, v146, v14\n"
    "v_mul_f32 v147, v147, v15\n"
    "v_mfma_f32_16x16x32_bf16 v[76:79], a[36:39], a[188:191], v[76:79]\n"
    "s_waitcnt lgkmcnt(2)\n"
    "v_cvt_pk_bf16_f32 v112, v144, v145\n"
    "v_cvt_pk_bf16_f32 v113, v146, v147\n"
    "v_mfma_f32_16x16x32_bf16 v[80:83], a[0:3], v[188:191], v[80:83]\n"
    "v_exp_f32 v12, v128\n"
    "v_mfma_f32_16x16x32_bf16 v[84:87], a[0:3], a[44:47], v[84:87]\n"
    "v_exp_f32 v13, v129\n"
    "v_mfma_f32_16x16x32_bf16 v[88:91], a[0:3], a[116:119], v[88:91]\n"
    "v_exp_f32 v14, v130\n"
    "v_mfma_f32_16x16x32_bf16 v[92:95], a[0:3], a[188:191], v[92:95]\n"
    "s_waitcnt vmcnt(25) lgkmcnt(0)\n"
    "s_barrier\n"
    "ds_read_b64_tr_b16 a[8:9], v8 offset:16384\n"
    "ds_read_b64_tr_b16 a[10:11], v8 offset:16896\n"
    "v_mfma_f32_16x16x32_bf16 v[96:99], a[4:7], v[188:191], v[96:99]\n"
    "s_add_u32 m0, s56, 16384\n"
    "ds_read_b64_tr_b16 a[12:13], v9 offset:16384\n"
    "ds_read_b64_tr_b16 a[14:15], v9 offset:16896\n"
    "v_mfma_f32_16x16x32_bf16 v[100:103], a[4:7], a[44:47], v[100:103]\n"
    "s_add_u32 s58, s61, 4128896\n"
    "ds_read_b64_tr_b16 a[36:37], v8 offset:24576\n"
    "s_waitcnt vmcnt(14)\n"
    "buffer_store_dword v21, v20, s[48:51], 0 offen offset:0 sc1\n"
    "v_mfma_f32_16x16x32_bf16 v[104:107], a[4:7], a[116:119], v[104:107]\n"
    "ds_read_b64_tr_b16 a[38:39], v8 offset:25088\n"
    "buffer_load_dwordx4 v2, s[36:39], s58 offen offset:0 lds\n"
    "v_mfma_f32_16x16x32_bf16 v[108:111], a[4:7], a[188:191], v[108:111]\n"
    "s_waitcnt lgkmcnt(4)\n"
    "ds_read_b64_tr_b16 a[0:1], v9 offset:24576\n"
    "v_mov_b32 v24, 0x80000000\n"
    "v_mfma_f32_16x16x32_bf16 v[48:51], a[8:11], v[192:195], v[48:51]\n"
    "ds_read_b64_tr_b16 a[2:3], v9 offset:25088\n"
    "v_cndmask_b32_e64 v24, v17, v24, s[80:81]\n"
    "v_mfma_f32_16x16x32_bf16 v[52:55], a[8:11], a[48:51], v[52:55]\n"
    "ds_read_b64_tr_b16 a[4:5], v8 offset:20480\n"
    "v_mfma_f32_16x16x32_bf16 v[56:59], a[8:11], a[120:123], v[56:59]\n"
    "ds_read_b64_tr_b16 a[6:7], v8 offset:20992\n"
    "v_mfma_f32_16x16x32_bf16 v[60:63], a[8:11], a[192:195], v[60:63]\n"
    "s_waitcnt lgkmcnt(6)\n"
    "ds_read_b64_tr_b16 a[8:9], v9 offset:20480\n"
    "buffer_load_dwordx4 v3, s[36:39], s58 offen offset:1024 lds\n"
    "v_mfma_f32_16x16x32_bf16 v[64:67], a[12:15], v[192:195], v[64:67]\n"
    "ds_read_b64_tr_b16 a[10:11], v9 offset:20992\n"
    "v_mfma_f32_16x16x32_bf16 v[68:71], a[12:15], a[48:51], v[68:71]\n"
    "v_exp_f32 v15, v131\n"
    "v_mfma_f32_16x16x32_bf16 v[72:75], a[12:15], a[120:123], v[72:75]\n"
    "v_mul_f32 v160, v128, v160\n"
    "v_mul_f32 v161, v129, v161\n"
    "v_mfma_f32_16x16x32_bf16 v[76:79], a[12:15], a[192:195], v[76:79]\n"
    "s_waitcnt lgkmcnt(6)\n"
    "ds_read_b64_tr_b16 a[12:13], v8 offset:28672\n"
    "v_mul_f32 v162, v130, v162\n"
    "v_mfma_f32_16x16x32_bf16 v[80:83], a[36:39], v[192:195], v[80:83]\n"
    "ds_read_b64_tr_b16 a[14:15], v8 offset:29184\n"
    "buffer_load_dwordx4 v4, s[36:39], s58 offen offset:2048 lds\n"
    "v_mfma_f32_16x16x32_bf16 v[84:87], a[36:39], a[48:51], v[84:87]\n"
    "v_mul_f32 v163, v131, v163\n"
    "v_add_f32 v12, 1.0, v12\n"
    "v_mfma_f32_16x16x32_bf16 v[88:91], a[36:39], a[120:123], v[88:91]\n"
    "v_add_f32 v13, 1.0, v13\n"
    "v_add_f32 v14, 1.0, v14\n"
    "v_mfma_f32_16x16x32_bf16 v[92:95], a[36:39], a[192:195], v[92:95]\n"
    "s_waitcnt lgkmcnt(6)\n"
    "ds_read_b64_tr_b16 a[36:37], v9 offset:28672\n"
    "v_add_f32 v15, 1.0, v15\n"
    "v_mfma_f32_16x16x32_bf16 v[96:99], a[0:3], v[192:195], v[96:99]\n"
    "ds_read_b64_tr_b16 a[38:39], v9 offset:29184\n"
    "v_mfma_f32_16x16x32_bf16 v[100:103], a[0:3], a[48:51], v[100:103]\n"
    "buffer_load_dwordx4 v5, s[36:39], s58 offen offset:3072 lds\n"
    "v_mfma_f32_16x16x32_bf16 v[104:107], a[0:3], a[120:123], v[104:107]\n"
    "v_rcp_f32 v12, v12\n"
    "v_mfma_f32_16x16x32_bf16 v[108:111], a[0:3], a[192:195], v[108:111]\n"
    "s_waitcnt lgkmcnt(6)\n"
    "v_rcp_f32 v13, v13\n"
    "v_mfma_f32_16x16x32_bf16 v[48:51], a[4:7], v[196:199], v[48:51]\n"
    "v_rcp_f32 v14, v14\n"
    "v_mfma_f32_16x16x32_bf16 v[52:55], a[4:7], a[52:55], v[52:55]\n"
    "v_rcp_f32 v15, v15\n"
    "v_mfma_f32_16x16x32_bf16 v[56:59], a[4:7], a[124:127], v[56:59]\n"
    "v_mul_f32 v160, v160, v12\n"
    "v_mul_f32 v161, v161, v13\n"
    "v_mfma_f32_16x16x32_bf16 v[60:63], a[4:7], a[196:199], v[60:63]\n"
    "s_waitcnt lgkmcnt(4)\n"
    "v_mul_f32 v162, v162, v14\n"
    "v_mul_f32 v163, v163, v15\n"
    "v_mfma_f32_16x16x32_bf16 v[64:67], a[8:11], v[196:199], v[64:67]\n"
    "v_cvt_pk_bf16_f32 v114, v160, v161\n"
    "v_cvt_pk_bf16_f32 v115, v162, v163\n"
    "v_mfma_f32_16x16x32_bf16 v[68:71], a[8:11], a[52:55], v[68:71]\n"
    "v_exp_f32 v12, v116\n"
    "v_mfma_f32_16x16x32_bf16 v[72:75], a[8:11], a[124:127], v[72:75]\n"
    "v_exp_f32 v13, v117\n"
    "v_mfma_f32_16x16x32_bf16 v[76:79], a[8:11], a[196:199], v[76:79]\n"
    "s_waitcnt lgkmcnt(2)\n"
    "v_exp_f32 v14, v118\n"
    "v_mfma_f32_16x16x32_bf16 v[80:83], a[12:15], v[196:199], v[80:83]\n"
    "v_exp_f32 v15, v119\n"
    "v_mfma_f32_16x16x32_bf16 v[84:87], a[12:15], a[52:55], v[84:87]\n"
    "v_mul_f32 v148, v116, v148\n"
    "v_mul_f32 v149, v117, v149\n"
    "v_mfma_f32_16x16x32_bf16 v[88:91], a[12:15], a[124:127], v[88:91]\n"
    "v_mul_f32 v150, v118, v150\n"
    "v_mul_f32 v151, v119, v151\n"
    "v_mfma_f32_16x16x32_bf16 v[92:95], a[12:15], a[196:199], v[92:95]\n"
    "s_waitcnt lgkmcnt(0)\n"
    "s_barrier\n"
    "ds_read_b64_tr_b16 a[0:1], v8 offset:32768\n"
    "ds_read_b64_tr_b16 a[2:3], v8 offset:33280\n"
    "v_mfma_f32_16x16x32_bf16 v[96:99], a[36:39], v[196:199], v[96:99]\n"
    "s_add_u32 m0, s56, 32768\n"
    "ds_read_b64_tr_b16 a[4:5], v9 offset:32768\n"
    "ds_read_b64_tr_b16 a[6:7], v9 offset:33280\n"
    "v_mfma_f32_16x16x32_bf16 v[100:103], a[36:39], a[52:55], v[100:103]\n"
    "s_add_u32 s58, s61, 4718720\n"
    "ds_read_b64_tr_b16 a[8:9], v8 offset:40960\n"
    "v_add_f32 v12, 1.0, v12\n"
    "v_mfma_f32_16x16x32_bf16 v[104:107], a[36:39], a[124:127], v[104:107]\n"
    "ds_read_b64_tr_b16 a[10:11], v8 offset:41472\n"
    "buffer_load_dwordx4 v2, s[36:39], s58 offen offset:0 lds\n"
    "v_mfma_f32_16x16x32_bf16 v[108:111], a[36:39], a[196:199], v[108:111]\n"
    "s_waitcnt lgkmcnt(4)\n"
    "ds_read_b64_tr_b16 a[12:13], v9 offset:40960\n"
    "v_add_f32 v13, 1.0, v13\n"
    "v_mfma_f32_16x16x32_bf16 v[48:51], a[0:3], v[200:203], v[48:51]\n"
    "ds_read_b64_tr_b16 a[14:15], v9 offset:41472\n"
    "v_add_f32 v14, 1.0, v14\n"
    "v_mfma_f32_16x16x32_bf16 v[52:55], a[0:3], a[56:59], v[52:55]\n"
    "ds_read_b64_tr_b16 a[36:37], v8 offset:36864\n"
    "v_add_f32 v15, 1.0, v15\n"
    "v_mfma_f32_16x16x32_bf16 v[56:59], a[0:3], a[128:131], v[56:59]\n"
    "ds_read_b64_tr_b16 a[38:39], v8 offset:37376\n"
    "v_mfma_f32_16x16x32_bf16 v[60:63], a[0:3], a[200:203], v[60:63]\n"
    "s_waitcnt lgkmcnt(6)\n"
    "ds_read_b64_tr_b16 a[0:1], v9 offset:36864\n"
    "buffer_load_dwordx4 v3, s[36:39], s58 offen offset:1024 lds\n"
    "v_mfma_f32_16x16x32_bf16 v[64:67], a[4:7], v[200:203], v[64:67]\n"
    "ds_read_b64_tr_b16 a[2:3], v9 offset:37376\n"
    "v_mfma_f32_16x16x32_bf16 v[68:71], a[4:7], a[56:59], v[68:71]\n"
    "ds_write_b128 v10, v[112:115] offset:0\n"
    "v_mfma_f32_16x16x32_bf16 v[72:75], a[4:7], a[128:131], v[72:75]\n"
    "buffer_store_dwordx4 v[112:115], v10, s[48:51], s60 offen offset:0 sc1\n"
    "v_mfma_f32_16x16x32_bf16 v[76:79], a[4:7], a[200:203], v[76:79]\n"
    "s_waitcnt lgkmcnt(7)\n"
    "ds_read_b64_tr_b16 a[4:5], v8 offset:45056\n"
    "v_mfma_f32_16x16x32_bf16 v[80:83], a[8:11], v[200:203], v[80:83]\n"
    "ds_read_b64_tr_b16 a[6:7], v8 offset:45568\n"
    "buffer_load_dwordx4 v4, s[36:39], s58 offen offset:2048 lds\n"
    "v_mfma_f32_16x16x32_bf16 v[84:87], a[8:11], a[56:59], v[84:87]\n"
    "v_rcp_f32 v12, v12\n"
    "v_mfma_f32_16x16x32_bf16 v[88:91], a[8:11], a[128:131], v[88:91]\n"
    "v_rcp_f32 v13, v13\n"
    "v_mfma_f32_16x16x32_bf16 v[92:95], a[8:11], a[200:203], v[92:95]\n"
    "s_waitcnt lgkmcnt(7)\n"
    "ds_read_b64_tr_b16 a[8:9], v9 offset:45056\n"
    "v_mfma_f32_16x16x32_bf16 v[96:99], a[12:15], v[200:203], v[96:99]\n"
    "ds_read_b64_tr_b16 a[10:11], v9 offset:45568\n"
    "v_mfma_f32_16x16x32_bf16 v[100:103], a[12:15], a[56:59], v[100:103]\n"
    "buffer_load_dwordx4 v5, s[36:39], s58 offen offset:3072 lds\n"
    "v_mfma_f32_16x16x32_bf16 v[104:107], a[12:15], a[128:131], v[104:107]\n"
    "v_rcp_f32 v14, v14\n"
    "v_mfma_f32_16x16x32_bf16 v[108:111], a[12:15], a[200:203], v[108:111]\n"
    "s_waitcnt lgkmcnt(7)\n"
    "v_rcp_f32 v15, v15\n"
    "v_mfma_f32_16x16x32_bf16 v[48:51], a[36:39], v[204:207], v[48:51]\n"
    "v_mul_f32 v148, v148, v12\n"
    "v_mul_f32 v149, v149, v13\n"
    "v_mfma_f32_16x16x32_bf16 v[52:55], a[36:39], a[60:63], v[52:55]\n"
    "v_mul_f32 v150, v150, v14\n"
    "v_mul_f32 v151, v151, v15\n"
    "v_mfma_f32_16x16x32_bf16 v[56:59], a[36:39], a[132:135], v[56:59]\n"
    "v_cvt_pk_bf16_f32 v116, v148, v149\n"
    "v_cvt_pk_bf16_f32 v117, v150, v151\n"
    "v_mfma_f32_16x16x32_bf16 v[60:63], a[36:39], a[204:207], v[60:63]\n"
    "s_waitcnt lgkmcnt(5)\n"
    "v_exp_f32 v12, v132\n"
    "v_mfma_f32_16x16x32_bf16 v[64:67], a[0:3], v[204:207], v[64:67]\n"
    "v_exp_f32 v13, v133\n"
    "v_mfma_f32_16x16x32_bf16 v[68:71], a[0:3], a[60:63], v[68:71]\n"
    "v_exp_f32 v14, v134\n"
    "v_mfma_f32_16x16x32_bf16 v[72:75], a[0:3], a[132:135], v[72:75]\n"
    "v_exp_f32 v15, v135\n"
    "v_mfma_f32_16x16x32_bf16 v[76:79], a[0:3], a[204:207], v[76:79]\n"
    "s_waitcnt lgkmcnt(2)\n"
    "v_mul_f32 v164, v132, v164\n"
    "v_mul_f32 v165, v133, v165\n"
    "v_mfma_f32_16x16x32_bf16 v[80:83], a[4:7], v[204:207], v[80:83]\n"
    "v_mul_f32 v166, v134, v166\n"
    "v_mul_f32 v167, v135, v167\n"
    "v_mfma_f32_16x16x32_bf16 v[84:87], a[4:7], a[60:63], v[84:87]\n"
    "v_add_f32 v12, 1.0, v12\n"
    "v_add_f32 v13, 1.0, v13\n"
    "v_mfma_f32_16x16x32_bf16 v[88:91], a[4:7], a[132:135], v[88:91]\n"
    "v_add_f32 v14, 1.0, v14\n"
    "v_add_f32 v15, 1.0, v15\n"
    "v_mfma_f32_16x16x32_bf16 v[92:95], a[4:7], a[204:207], v[92:95]\n"
    "s_waitcnt lgkmcnt(0)\n"
    "s_barrier\n"
    "ds_read_b64_tr_b16 a[12:13], v6 offset:0\n"
    "ds_read_b64_tr_b16 a[14:15], v6 offset:512\n"
    "v_mfma_f32_16x16x32_bf16 v[96:99], a[8:11], v[204:207], v[96:99]\n"
    "s_add_u32 m0, s56, 49152\n"
    "ds_read_b64_tr_b16 a[36:37], v7 offset:0\n"
    "ds_read_b64_tr_b16 a[38:39], v7 offset:512\n"
    "v_mfma_f32_16x16x32_bf16 v[100:103], a[8:11], a[60:63], v[100:103]\n"
    "s_add_u32 s58, s61, 256\n"
    "ds_read_b64_tr_b16 a[0:1], v6 offset:8192\n"
    "buffer_load_dword v22, v24, s[48:51], 0 offen offset:256 sc1\n"
    "v_mfma_f32_16x16x32_bf16 v[104:107], a[8:11], a[132:135], v[104:107]\n"
    "ds_read_b64_tr_b16 a[2:3], v6 offset:8704\n"
    "buffer_load_dwordx4 v2, s[36:39], s58 offen offset:0 lds\n"
    "v_mfma_f32_16x16x32_bf16 v[108:111], a[8:11], a[204:207], v[108:111]\n"
    "s_waitcnt lgkmcnt(4)\n"
    "ds_read_b64_tr_b16 a[4:5], v7 offset:8192\n"
    "v_mfma_f32_16x16x32_bf16 v[48:51], a[12:15], v[208:211], v[48:51]\n"
    "ds_read_b64_tr_b16 a[6:7], v7 offset:8704\n"
    "v_mfma_f32_16x16x32_bf16 v[52:55], a[12:15], a[64:67], v[52:55]\n"
    "ds_read_b64_tr_b16 a[8:9], v6 offset:4096\n"
    "v_mfma_f32_16x16x32_bf16 v[56:59], a[12:15], a[136:139], v[56:59]\n"
    "ds_read_b64_tr_b16 a[10:11], v6 offset:4608\n"
    "v_mfma_f32_16x16x32_bf16 v[60:63], a[12:15], a[208:211], v[60:63]\n"
    "s_waitcnt lgkmcnt(6)\n"
    "ds_read_b64_tr_b16 a[12:13], v7 offset:4096\n"
    "buffer_load_dwordx4 v3, s[36:39], s58 offen offset:1024 lds\n"
    "v_mfma_f32_16x16x32_bf16 v[64:67], a[36:39], v[208:211], v[64:67]\n"
    "ds_read_b64_tr_b16 a[14:15], v7 offset:4608\n"
    "v_mfma_f32_16x16x32_bf16 v[68:71], a[36:39], a[64:67], v[68:71]\n"
    "s_waitcnt vmcnt(21)\n"
    "v_xor_b32 v45, v42, v43\n"
    "v_xor_b32 v45, 0x5bd1e995, v45\n"
    "v_cmp_eq_u32 vcc, v45, v44\n"
    "v_not_b32 v45, s27\n"
    "s_nop 0\n"
    "v_cndmask_b32 v45, v45, v42, vcc\n"
    "v_cmp_eq_u32 vcc, s27, v45\n"
    "s_nop 1\n"
    "s_cmp_eq_u64 vcc, -1\n"
    "s_cbranch_scc1 L_ok0\n"
    "s_mov_b32 s79, 100000\n"
    "L_sp0:\n"
    "s_sleep 2\n"
    "buffer_load_dword v42, v18, s[48:51], 0 offen offset:128 sc1\n"
    "s_waitcnt vmcnt(0)\n"
    "v_mov_b32 v43, s27\n"
    "v_xor_b32 v44, v42, v43\n"
    "v_xor_b32 v44, 0x5bd1e995, v44\n"
    "buffer_store_dwordx3 v[42:44], v41, s[48:51], 0 offen sc1\n"
    "s_waitcnt vmcnt(0)\n"
    "buffer_load_dwordx3 v[42:44], v46, s[48:51], 0 offen sc1\n"
    "s_waitcnt vmcnt(0)\n"
    "v_xor_b32 v45, v42, v43\n"
    "v_xor_b32 v45, 0x5bd1e995, v45\n"
    "v_cmp_eq_u32 vcc, v45, v44\n"
    "v_not_b32 v45, s27\n"
    "s_nop 0\n"
    "v_cndmask_b32 v45, v45, v42, vcc\n"
    "v_cmp_eq_u32 vcc, s27, v45\n"
    "s_nop 1\n"
    "s_cmp_eq_u64 vcc, -1\n"
    "s_cbranch_scc1 L_ok0\n"
    "s_sub_u32 s79, s79, 1\n"
    "s_cmp_lg_u32 s79, 0\n"
    "s_cbranch_scc1 L_sp0\n"
    "L_ok0:\n"
    "v_mfma_f32_16x16x32_bf16 v[72:75], a[36:39], a[136:139], v[72:75]\n"
    "v_mov_b32 v16, v43\n"
    "v_mov_b32 v42, v43\n"
    "v_mfma_f32_16x16x32_bf16 v[76:79], a[36:39], a[208:211], v[76:79]\n"
    "s_waitcnt lgkmcnt(6)\n"
    "ds_read_b64_tr_b16 a[36:37], v6 offset:12288\n"
    "v_mov_b32 v43, s27\n"
    "v_mfma_f32_16x16x32_bf16 v[80:83], a[0:3], v[208:211], v[80:83]\n"
    "ds_read_b64_tr_b16 a[38:39], v6 offset:12800\n"
    "buffer_load_dwordx4 v4, s[36:39], s58 offen offset:2048 lds\n"
    "v_mfma_f32_16x16x32_bf16 v[84:87], a[0:3], a[64:67], v[84:87]\n"
    "v_xor_b32 v44, v42, v43\n"
    "v_xor_b32 v44, 0x5bd1e995, v44\n"
    "v_mfma_f32_16x16x32_bf16 v[88:91], a[0:3], a[136:139], v[88:91]\n"
    "buffer_store_dwordx3 v[42:44], v41, s[48:51], 0 offen offset:0 sc1\n"
    "v_lshlrev_b32 v18, 4, v0\n"
    "v_mfma_f32_16x16x32_bf16 v[92:95], a[0:3], a[208:211], v[92:95]\n"
    "s_waitcnt lgkmcnt(6)\n"
    "ds_read_b64_tr_b16 a[0:1], v7 offset:12288\n"
    "v_lshrrev_b32 v19, 4, v0\n"
    "v_mfma_f32_16x16x32_bf16 v[96:99], a[4:7], v[208:211], v[96:99]\n"
    "ds_read_b64_tr_b16 a[2:3], v7 offset:12800\n"
    "v_cmp_eq_u32_e64 vcc, 1, v19\n"
    "v_mfma_f32_16x16x32_bf16 v[100:103], a[4:7], a[64:67], v[100:103]\n"
    "buffer_load_dwordx4 v5, s[36:39], s58 offen offset:3072 lds\n"
    "v_mov_b32 v32, 0x80000000\n"
    "v_mfma_f32_16x16x32_bf16 v[104:107], a[4:7], a[136:139], v[104:107]\n"
    "v_add_u32 v19, 1792, v18\n"
    "v_cndmask_b32_e64 v19, v32, v19, vcc\n"
    "v_mfma_f32_16x16x32_bf16 v[108:111], a[4:7], a[208:211], v[108:111]\n"
    "s_waitcnt lgkmcnt(6)\n"
    "v_rcp_f32 v12, v12\n"
    "v_mfma_f32_16x16x32_bf16 v[48:51], a[8:11], v[212:215], v[48:51]\n"
    "v_rcp_f32 v13, v13\n"
    "v_mfma_f32_16x16x32_bf16 v[52:55], a[8:11], a[68:71], v[52:55]\n"
    "v_rcp_f32 v14, v14\n"
    "v_mfma_f32_16x16x32_bf16 v[56:59], a[8:11], a[140:143], v[56:59]\n"
    "v_rcp_f32 v15, v15\n"
    "v_mfma_f32_16x16x32_bf16 v[60:63], a[8:11], a[212:215], v[60:63]\n"
    "s_waitcnt lgkmcnt(4)\n"
    "v_mul_f32 v164, v164, v12\n"
    "v_mul_f32 v165, v165, v13\n"
    "v_mfma_f32_16x16x32_bf16 v[64:67], a[12:15], v[212:215], v[64:67]\n"
    "v_mul_f32 v166, v166, v14\n"
    "v_mul_f32 v167, v167, v15\n"
    "v_mfma_f32_16x16x32_bf16 v[68:71], a[12:15], a[68:71], v[68:71]\n"
    "v_cvt_pk_bf16_f32 v118, v164, v165\n"
    "v_cvt_pk_bf16_f32 v119, v166, v167\n"
    "v_mfma_f32_16x16x32_bf16 v[72:75], a[12:15], a[140:143], v[72:75]\n"
    "v_exp_f32 v12, v120\n"
    "v_mfma_f32_16x16x32_bf16 v[76:79], a[12:15], a[212:215], v[76:79]\n"
    "s_waitcnt lgkmcnt(2)\n"
    "v_exp_f32 v13, v121\n"
    "v_mfma_f32_16x16x32_bf16 v[80:83], a[36:39], v[212:215], v[80:83]\n"
    "v_exp_f32 v14, v122\n"
    "v_mfma_f32_16x16x32_bf16 v[84:87], a[36:39], a[68:71], v[84:87]\n"
    "v_exp_f32 v15, v123\n"
    "v_mfma_f32_16x16x32_bf16 v[88:91], a[36:39], a[140:143], v[88:91]\n"
    "v_mul_f32 v152, v120, v152\n"
    "v_mul_f32 v153, v121, v153\n"
    "v_mfma_f32_16x16x32_bf16 v[92:95], a[36:39], a[212:215], v[92:95]\n"
    "s_waitcnt lgkmcnt(0)\n"
    "s_barrier\n"
    "ds_read_b64_tr_b16 a[4:5], v8 offset:49152\n"
    "ds_read_b64_tr_b16 a[6:7], v8 offset:49664\n"
    "v_mfma_f32_16x16x32_bf16 v[96:99], a[0:3], v[212:215], v[96:99]\n"
    "s_add_u32 m0, s56, 4294926336\n"
    "ds_read_b64_tr_b16 a[8:9], v9 offset:49152\n"
    "ds_read_b64_tr_b16 a[10:11], v9 offset:49664\n"
    "v_mfma_f32_16x16x32_bf16 v[100:103], a[0:3], a[68:71], v[100:103]\n"
    "s_add_u32 s58, s61, 590080\n"
    "ds_read_b64_tr_b16 a[12:13], v8 offset:57344\n"
    "v_mul_f32 v154, v122, v154\n"
    "v_mfma_f32_16x16x32_bf16 v[104:107], a[0:3], a[140:143], v[104:107]\n"
    "ds_read_b64_tr_b16 a[14:15], v8 offset:57856\n"
    "buffer_load_dwordx4 v2, s[36:39], s58 offen offset:0 lds\n"
    "v_mfma_f32_16x16x32_bf16 v[108:111], a[0:3], a[212:215], v[108:111]\n"
    "s_waitcnt lgkmcnt(4)\n"
    "ds_read_b64_tr_b16 a[36:37], v9 offset:57344\n"
    "v_mul_f32 v155, v123, v155\n"
    "v_mfma_f32_16x16x32_bf16 v[48:51], a[4:7], v[216:219], v[48:51]\n"
    "ds_read_b64_tr_b16 a[38:39], v9 offset:57856\n"
    "v_add_f32 v12, 1.0, v12\n"
    "v_mfma_f32_16x16x32_bf16 v[52:55], a[4:7], a[72:75], v[52:55]\n"
    "ds_read_b64_tr_b16 a[0:1], v8 offset:53248\n"
    "v_add_f32 v13, 1.0, v13\n"
    "v_mfma_f32_16x16x32_bf16 v[56:59], a[4:7], a[144:147], v[56:59]\n"
    "ds_read_b64_tr_b16 a[2:3], v8 offset:53760\n"
    "v_add_f32 v14, 1.0, v14\n"
    "v_mfma_f32_16x16x32_bf16 v[60:63], a[4:7], a[216:219], v[60:63]\n"
    "s_waitcnt lgkmcnt(6)\n"
    "ds_read_b64_tr_b16 a[4:5], v9 offset:53248\n"
    "buffer_load_dwordx4 v3, s[36:39], s58 offen offset:1024 lds\n"
    "v_mfma_f32_16x16x32_bf16 v[64:67], a[8:11], v[216:219], v[64:67]\n"
    "ds_read_b64_tr_b16 a[6:7], v9 offset:53760\n"
    "v_add_f32 v15, 1.0, v15\n"
    "v_mfma_f32_16x16x32_bf16 v[68:71], a[8:11], a[72:75], v[68:71]\n"
    "ds_write_b128 v10, v[116:119] offset:1024\n"
    "v_mfma_f32_16x16x32_bf16 v[72:75], a[8:11], a[144:147], v[72:75]\n"
    "buffer_store_dwordx4 v[116:119], v10, s[48:51], s60 offen offset:1024 sc1\n"
    "v_mfma_f32_16x16x32_bf16 v[76:79], a[8:11], a[216:219], v[76:79]\n"
    "s_waitcnt lgkmcnt(7)\n"
    "ds_read_b64_tr_b16 a[8:9], v8 offset:61440\n"
    "v_mfma_f32_16x16x32_bf16 v[80:83], a[12:15], v[216:219], v[80:83]\n"
    "ds_read_b64_tr_b16 a[10:11], v8 offset:61952\n"
    "buffer_load_dwordx4 v4, s[36:39], s58 offen offset:2048 lds\n"
    "v_mfma_f32_16x16x32_bf16 v[84:87], a[12:15], a[72:75], v[84:87]\n"
    "v_rcp_f32 v12, v12\n"
    "v_mfma_f32_16x16x32_bf16 v[88:91], a[12:15], a[144:147], v[88:91]\n"
    "v_rcp_f32 v13, v13\n"
    "v_mfma_f32_16x16x32_bf16 v[92:95], a[12:15], a[216:219], v[92:95]\n"
    "s_waitcnt lgkmcnt(7)\n"
    "ds_read_b64_tr_b16 a[12:13], v9 offset:61440\n"
    "v_mfma_f32_16x16x32_bf16 v[96:99], a[36:39], v[216:219], v[96:99]\n"
    "ds_read_b64_tr_b16 a[14:15], v9 offset:61952\n"
    "v_mfma_f32_16x16x32_bf16 v[100:103], a[36:39], a[72:75], v[100:103]\n"
    "buffer_load_dwordx4 v5, s[36:39], s58 offen offset:3072 lds\n"
    "v_mfma_f32_16x16x32_bf16 v[104:107], a[36:39], a[144:147], v[104:107]\n"
    "v_rcp_f32 v14, v14\n"
    "v_mfma_f32_16x16x32_bf16 v[108:111], a[36:39], a[216:219], v[108:111]\n"
    "s_waitcnt lgkmcnt(7)\n"
    "v_rcp_f32 v15, v15\n"
    "v_mfma_f32_16x16x32_bf16 v[48:51], a[0:3], v[220:223], v[48:51]\n"
    "v_mul_f32 v152, v152, v12\n"
    "v_mul_f32 v153, v153, v13\n"
    "v_mfma_f32_16x16x32_bf16 v[52:55], a[0:3], a[76:79], v[52:55]\n"
    "v_mul_f32 v154, v154, v14\n"
    "v_mul_f32 v155, v155, v15\n"
    "v_mfma_f32_16x16x32_bf16 v[56:59], a[0:3], a[148:151], v[56:59]\n"
    "v_cvt_pk_bf16_f32 v120, v152, v153\n"
    "v_cvt_pk_bf16_f32 v121, v154, v155\n"
    "v_mfma_f32_16x16x32_bf16 v[60:63], a[0:3], a[220:223], v[60:63]\n"
    "s_waitcnt lgkmcnt(5)\n"
    "v_exp_f32 v12, v136\n"
    "v_mfma_f32_16x16x32_bf16 v[64:67], a[4:7], v[220:223], v[64:67]\n"
    "v_exp_f32 v13, v137\n"
    "v_mfma_f32_16x16x32_bf16 v[68:71], a[4:7], a[76:79], v[68:71]\n"
    "v_exp_f32 v14, v138\n"
    "v_mfma_f32_16x16x32_bf16 v[72:75], a[4:7], a[148:151], v[72:75]\n"
    "v_exp_f32 v15, v139\n"
    "v_mfma_f32_16x16x32_bf16 v[76:79], a[4:7], a[220:223], v[76:79]\n"
    "s_waitcnt lgkmcnt(2)\n"
    "v_mul_f32 v168, v136, v168\n"
    "v_mul_f32 v169, v137, v169\n"
    "v_mfma_f32_16x16x32_bf16 v[80:83], a[8:11], v[220:223], v[80:83]\n"
    "v_mul_f32 v170, v138, v170\n"
    "v_mul_f32 v171, v139, v171\n"
    "v_mfma_f32_16x16x32_bf16 v[84:87], a[8:11], a[76:79], v[84:87]\n"
    "v_add_f32 v12, 1.0, v12\n"
    "v_add_f32 v13, 1.0, v13\n"
    "v_mfma_f32_16x16x32_bf16 v[88:91], a[8:11], a[148:151], v[88:91]\n"
    "v_add_f32 v14, 1.0, v14\n"
    "v_add_f32 v15, 1.0, v15\n"
    "v_mfma_f32_16x16x32_bf16 v[92:95], a[8:11], a[220:223], v[92:95]\n"
    "s_waitcnt vmcnt(25) lgkmcnt(0)\n"
    "s_barrier\n"
    "ds_read_b64_tr_b16 a[36:37], v6 offset:16384\n"
    "ds_read_b64_tr_b16 a[38:39], v6 offset:16896\n"
    "v_mfma_f32_16x16x32_bf16 v[96:99], a[12:15], v[220:223], v[96:99]\n"
    "s_add_u32 m0, s56, 65536\n"
    "ds_read_b64_tr_b16 a[0:1], v7 offset:16384\n"
    "ds_read_b64_tr_b16 a[2:3], v7 offset:16896\n"
    "v_mfma_f32_16x16x32_bf16 v[100:103], a[12:15], a[76:79], v[100:103]\n"
    "L_hop5:\n"
    "s_cbranch_execz L_hop9\n"
    "s_add_u32 s58, s61, 1179904\n"
    "ds_read_b64_tr_b16 a[4:5], v6 offset:24576\n"
    "v_mfma_f32_16x16x32_bf16 v[104:107], a[12:15], a[148:151], v[104:107]\n"
    "ds_read_b64_tr_b16 a[6:7], v6 offset:25088\n"
    "buffer_load_dwordx4 v2, s[36:39], s58 offen offset:0 lds\n"
    "v_mfma_f32_16x16x32_bf16 v[108:111], a[12:15], a[220:223], v[108:111]\n"
    "s_waitcnt lgkmcnt(4)\n"
    "ds_read_b64_tr_b16 a[8:9], v7 offset:24576\n"
    "v_mfma_f32_16x16x32_bf16 v[48:51], a[36:39], v[224:227], v[48:51]\n"
    "ds_read_b64_tr_b16 a[10:11], v7 offset:25088\n"
    "v_mfma_f32_16x16x32_bf16 v[52:55], a[36:39], a[80:83], v[52:55]\n"
    "ds_read_b64_tr_b16 a[12:13], v6 offset:20480\n"
    "v_mfma_f32_16x16x32_bf16 v[56:59], a[36:39], a[152:155], v[56:59]\n"
    "ds_read_b64_tr_b16 a[14:15], v6 offset:20992\n"
    "v_mfma_f32_16x16x32_bf16 v[60:63], a[36:39], a[224:227], v[60:63]\n"
    "s_waitcnt lgkmcnt(6)\n"
    "ds_read_b64_tr_b16 a[36:37], v7 offset:20480\n"
    "buffer_load_dwordx4 v3, s[36:39], s58 offen offset:1024 lds\n"
    "v_mfma_f32_16x16x32_bf16 v[64:67], a[0:3], v[224:227], v[64:67]\n"
    "ds_read_b64_tr_b16 a[38:39], v7 offset:20992\n"
    "v_mfma_f32_16x16x32_bf16 v[68:71], a[0:3], a[80:83], v[68:71]\n"
    "v_rcp_f32 v12, v12\n"
    "v_mfma_f32_16x16x32_bf16 v[72:75], a[0:3], a[152:155], v[72:75]\n"
    "v_rcp_f32 v13, v13\n"
    "v_mfma_f32_16x16x32_bf16 v[76:79], a[0:3], a[224:227], v[76:79]\n"
    "s_waitcnt lgkmcnt(6)\n"
    "ds_read_b64_tr_b16 a[0:1], v6 offset:28672\n"
    "v_mfma_f32_16x16x32_bf16 v[80:83], a[4:7], v[224:227], v[80:83]\n"
    "ds_read_b64_tr_b16 a[2:3], v6 offset:29184\n"
    "buffer_load_dwordx4 v4, s[36:39], s58 offen offset:2048 lds\n"
    "v_mfma_f32_16x16x32_bf16 v[84:87], a[4:7], a[80:83], v[84:87]\n"
    "v_rcp_f32 v14, v14\n"
    "v_mfma_f32_16x16x32_bf16 v[88:91], a[4:7], a[152:155], v[88:91]\n"
    "v_rcp_f32 v15, v15\n"
    "v_mfma_f32_16x16x32_bf16 v[92:95], a[4:7], a[224:227], v[92:95]\n"
    "s_waitcnt lgkmcnt(6)\n"
    "ds_read_b64_tr_b16 a[4:5], v7 offset:28672\n"
    "v_mul_f32 v168, v168, v12\n"
    "v_mfma_f32_16x16x32_bf16 v[96:99], a[8:11], v[224:227], v[96:99]\n"
    "ds_read_b64_tr_b16 a[6:7], v7 offset:29184\n"
    "v_mul_f32 v169, v169, v13\n"
    "v_mfma_f32_16x16x32_bf16 v[100:103], a[8:11], a[80:83], v[100:103]\n"
    "buffer_load_dwordx4 v5, s[36:39], s58 offen offset:3072 lds\n"
    "v_mul_f32 v170, v170, v14\n"
    "v_mfma_f32_16x16x32_bf16 v[104:107], a[8:11], a[152:155], v[104:107]\n"
    "v_mul_f32 v171, v171, v15\n"
    "v_cvt_pk_bf16_f32 v122, v168, v169\n"
    "v_mfma_f32_16x16x32_bf16 v[108:111], a[8:11], a[224:227], v[108:111]\n"
    "s_waitcnt lgkmcnt(6)\n"
    "v_cvt_pk_bf16_f32 v123, v170, v171\n"
    "v_mfma_f32_16x16x32_bf16 v[48:51], a[12:15], v[228:231], v[48:51]\n"
    "ds_write_b128 v11, v[120:123] offset:0\n"
    "v_mfma_f32_16x16x32_bf16 v[52:55], a[12:15], a[84:87], v[52:55]\n"
    "buffer_store_dwordx4 v[120:123], v11, s[48:51], s60 offen offset:0 sc1\n"
    "v_mfma_f32_16x16x32_bf16 v[56:59], a[12:15], a[156:159], v[56:59]\n"
    "v_exp_f32 v12, v124\n"
    "v_mfma_f32_16x16x32_bf16 v[60:63], a[12:15], a[228:231], v[60:63]\n"
    "s_waitcnt lgkmcnt(5)\n"
    "v_exp_f32 v13, v125\n"
    "v_mfma_f32_16x16x32_bf16 v[64:67], a[36:39], v[228:231], v[64:67]\n"
    "v_exp_f32 v14, v126\n"
    "v_mfma_f32_16x16x32_bf16 v[68:71], a[36:39], a[84:87], v[68:71]\n"
    "v_exp_f32 v15, v127\n"
    "v_mfma_f32_16x16x32_bf16 v[72:75], a[36:39], a[156:159], v[72:75]\n"
    "v_mul_f32 v156, v124, v156\n"
    "v_mul_f32 v157, v125, v157\n"
    "v_mfma_f32_16x16x32_bf16 v[76:79], a[36:39], a[228:231], v[76:79]\n"
    "s_waitcnt lgkmcnt(3)\n"
    "v_mul_f32 v158, v126, v158\n"
    "v_mul_f32 v159, v127, v159\n"
    "v_mfma_f32_16x16x32_bf16 v[80:83], a[0:3], v[228:231], v[80:83]\n"
    "v_add_f32 v12, 1.0, v12\n"
    "v_add_f32 v13, 1.0, v13\n"
    "v_mfma_f32_16x16x32_bf16 v[84:87], a[0:3], a[84:87], v[84:87]\n"
    "v_add_f32 v14, 1.0, v14\n"
    "v_add_f32 v15, 1.0, v15\n"
    "v_mfma_f32_16x16x32_bf16 v[88:91], a[0:3], a[156:159], v[88:91]\n"
    "v_rcp_f32 v12, v12\n"
    "v_mfma_f32_16x16x32_bf16 v[92:95], a[0:3], a[228:231], v[92:95]\n"
    "s_waitcnt vmcnt(26) lgkmcnt(0)\n"
    "s_barrier\n"
    "ds_read_b64_tr_b16 a[8:9], v6 offset:40960\n"
    "ds_read_b64_tr_b16 a[10:11], v6 offset:41472\n"
    "v_mfma_f32_16x16x32_bf16 v[96:99], a[4:7], v[228:231], v[96:99]\n"
    "s_add_u32 m0, s56, 4294942720\n"
    "ds_read_b64_tr_b16 a[12:13], v7 offset:40960\n"
    "ds_read_b64_tr_b16 a[14:15], v7 offset:41472\n"
    "v_mfma_f32_16x16x32_bf16 v[100:103], a[4:7], a[84:87], v[100:103]\n"
    "s_add_u32 s58, s61, 1769728\n"
    "ds_read_b64_tr_b16 a[36:37], v6 offset:49152\n"
    "v_mfma_f32_16x16x32_bf16 v[104:107], a[4:7], a[156:159], v[104:107]\n"
    "ds_read_b64_tr_b16 a[38:39], v6 offset:49664\n"
    "buffer_load_dwordx4 v2, s[36:39], s58 offen offset:0 lds\n"
    "v_mfma_f32_16x16x32_bf16 v[108:111], a[4:7], a[228:231], v[108:111]\n"
    "s_waitcnt lgkmcnt(4)\n"
    "ds_read_b64_tr_b16 a[0:1], v7 offset:49152\n"
    "v_mfma_f32_16x16x32_bf16 v[48:51], a[8:11], v[232:235], v[48:51]\n"
    "ds_read_b64_tr_b16 a[2:3], v7 offset:49664\n"
    "v_mfma_f32_16x16x32_bf16 v[52:55], a[8:11], a[88:91], v[52:55]\n"
    "ds_read_b64_tr_b16 a[4:5], v6 offset:45056\n"
    "v_mfma_f32_16x16x32_bf16 v[56:59], a[8:11], a[160:163], v[56:59]\n"
    "ds_read_b64_tr_b16 a[6:7], v6 offset:45568\n"
    "v_mfma_f32_16x16x32_bf16 v[60:63], a[8:11], a[232:235], v[60:63]\n"
    "s_waitcnt lgkmcnt(6)\n"
    "ds_read_b64_tr_b16 a[8:9], v7 offset:45056\n"
    "buffer_load_dwordx4 v3, s[36:39], s58 offen offset:1024 lds\n"
    "v_mfma_f32_16x16x32_bf16 v[64:67], a[12:15], v[232:235], v[64:67]\n"
    "ds_read_b64_tr_b16 a[10:11], v7 offset:45568\n"
    "v_mfma_f32_16x16x32_bf16 v[68:71], a[12:15], a[88:91], v[68:71]\n"
    "s_waitcnt vmcnt(17)\n"
    "v_sub_u32 v23, v22, v16\n"
    "v_subrev_u32 v23, 1, v23\n"
    "v_cmp_gt_u32 vcc, 32, v23\n"
    "s_nop 1\n"
    "s_or_b64 vcc, vcc, s[80:81]\n"
    "s_cmp_eq_u64 vcc, -1\n"
    "s_cbranch_scc1 L_ok1\n"
    "s_mov_b32 s79, 100000\n"
    "L_sp1:\n"
    "s_sleep 2\n"
    "buffer_load_dword v22, v24, s[48:51], 0 offen offset:256 sc1\n"
    "s_waitcnt vmcnt(0)\n"
    "v_sub_u32 v23, v22, v16\n"
    "v_subrev_u32 v23, 1, v23\n"
    "v_cmp_gt_u32 vcc, 32, v23\n"
    "s_nop 1\n"
    "s_or_b64 vcc, vcc, s[80:81]\n"
    "s_cmp_eq_u64 vcc, -1\n"
    "s_cbranch_scc1 L_ok1\n"
    "s_sub_u32 s79, s79, 1\n"
    "s_cmp_lg_u32 s79, 0\n"
    "s_cbranch_scc1 L_sp1\n"
    "L_ok1:\n"
    "v_mfma_f32_16x16x32_bf16 v[72:75], a[12:15], a[160:163], v[72:75]\n"
    "s_add_u32 s86, s71, 0\n"
    "buffer_load_dwordx4 v[20:23], v18, s[48:51], s86 offen offset:0 sc1\n"
    "v_mfma_f32_16x16x32_bf16 v[76:79], a[12:15], a[232:235], v[76:79]\n"
    "s_waitcnt lgkmcnt(6)\n"
    "ds_read_b64_tr_b16 a[12:13], v6 offset:53248\n"
    "buffer_load_dwordx4 v[24:27], v18, s[48:51], s86 offen offset:1024 sc1\n"
    "v_mfma_f32_16x16x32_bf16 v[80:83], a[36:39], v[232:235], v[80:83]\n"
    "ds_read_b64_tr_b16 a[14:15], v6 offset:53760\n"
    "buffer_load_dwordx4 v4, s[36:39], s58 offen offset:2048 lds\n"
    "v_mfma_f32_16x16x32_bf16 v[84:87], a[36:39], a[88:91], v[84:87]\n"
    "v_rcp_f32 v13, v13\n"
    "v_mfma_f32_16x16x32_bf16 v[88:91], a[36:39], a[160:163], v[88:91]\n"
    "v_rcp_f32 v14, v14\n"
    "v_mfma_f32_16x16x32_bf16 v[92:95], a[36:39], a[232:235], v[92:95]\n"
    "s_waitcnt lgkmcnt(6)\n"
    "ds_read_b64_tr_b16 a[36:37], v7 offset:53248\n"
    "v_mfma_f32_16x16x32_bf16 v[96:99], a[0:3], v[232:235], v[96:99]\n"
    "ds_read_b64_tr_b16 a[38:39], v7 offset:53760\n"
    "v_mfma_f32_16x16x32_bf16 v[100:103], a[0:3], a[88:91], v[100:103]\n"
    "buffer_load_dwordx4 v5, s[36:39], s58 offen offset:3072 lds\n"
    "v_mfma_f32_16x16x32_bf16 v[104:107], a[0:3], a[160:163], v[104:107]\n"
    "v_rcp_f32 v15, v15\n"
    "v_mfma_f32_16x16x32_bf16 v[108:111], a[0:3], a[232:235], v[108:111]\n"
    "s_waitcnt lgkmcnt(6)\n"
    "v_mul_f32 v156, v156, v12\n"
    "v_mul_f32 v157, v157, v13\n"
    "v_mfma_f32_16x16x32_bf16 v[48:51], a[4:7], v[236:239], v[48:51]\n"
    "v_mul_f32 v158, v158, v14\n"
    "v_mul_f32 v159, v159, v15\n"
    "v_mfma_f32_16x16x32_bf16 v[52:55], a[4:7], a[92:95], v[52:55]\n"
    "buffer_load_dwordx4 v[28:31], v19, s[48:51], s86 offen offset:0 sc1\n"
    "s_add_u32 s86, s71, 2304\n"
    "v_mfma_f32_16x16x32_bf16 v[56:59], a[4:7], a[164:167], v[56:59]\n"
    "v_cvt_pk_bf16_f32 v124, v156, v157\n"
    "v_cvt_pk_bf16_f32 v125, v158, v159\n"
    "v_mfma_f32_16x16x32_bf16 v[60:63], a[4:7], a[236:239], v[60:63]\n"
    "s_waitcnt lgkmcnt(4)\n"
    "v_exp_f32 v12, v140\n"
    "v_mfma_f32_16x16x32_bf16 v[64:67], a[8:11], v[236:239], v[64:67]\n"
    "buffer_load_dwordx4 v[44:47], v18, s[48:51], s86 offen offset:0 sc1\n"
    "v_mfma_f32_16x16x32_bf16 v[68:71], a[8:11], a[92:95], v[68:71]\n"
    "v_exp_f32 v13, v141\n"
    "v_mfma_f32_16x16x32_bf16 v[72:75], a[8:11], a[164:167], v[72:75]\n"
    "v_exp_f32 v14, v142\n"
    "v_mfma_f32_16x16x32_bf16 v[76:79], a[8:11], a[236:239], v[76:79]\n"
    "s_waitcnt lgkmcnt(2)\n"
    "buffer_load_dwordx4 v[176:179], v18, s[48:51], s86 offen offset:1024 sc1\n"
    "v_mfma_f32_16x16x32_bf16 v[80:83], a[12:15], v[236:239], v[80:83]\n"
    "v_exp_f32 v15, v143\n"
    "v_mfma_f32_16x16x32_bf16 v[84:87], a[12:15], a[92:95], v[84:87]\n"
    "v_mul_f32 v172, v140, v172\n"
    "v_mul_f32 v173, v141, v173\n"
    "v_mfma_f32_16x16x32_bf16 v[88:91], a[12:15], a[164:167], v[88:91]\n"
    "v_mul_f32 v174, v142, v174\n"
    "v_mul_f32 v175, v143, v175\n"
    "v_mfma_f32_16x16x32_bf16 v[92:95], a[12:15], a[236:239], v[92:95]\n"
    "s_waitcnt lgkmcnt(0)\n"
    "s_barrier\n"
    "ds_read_b64_tr_b16 a[0:1], v8 offset:0\n"
    "ds_read_b64_tr_b16 a[2:3], v8 offset:512\n"
    "v_mfma_f32_16x16x32_bf16 v[96:99], a[36:39], v[236:239], v[96:99]\n"
    "s_add_u32 m0, s56, 0\n"
    "ds_read_b64_tr_b16 a[4:5], v9 offset:0\n"
    "ds_read_b64_tr_b16 a[6:7], v9 offset:512\n"
    "v_mfma_f32_16x16x32_bf16 v[100:103], a[36:39], a[92:95], v[100:103]\n"
    "s_add_u32 s58, s61, 2359552\n"
    "ds_read_b64_tr_b16 a[8:9], v8 offset:8192\n"
    "buffer_load_dwordx4 v[180:183], v19, s[48:51], s86 offen offset:0 sc1\n"
    "v_mfma_f32_16x16x32_bf16 v[104:107], a[36:39], a[164:167], v[104:107]\n"
    "ds_read_b64_tr_b16 a[10:11], v8 offset:8704\n"
    "buffer_load_dwordx4 v2, s[36:39], s58 offen offset:0 lds\n"
    "v_mfma_f32_16x16x32_bf16 v[108:111], a[36:39], a[236:239], v[108:111]\n"
    "s_waitcnt lgkmcnt(4)\n"
    "ds_read_b64_tr_b16 a[12:13], v9 offset:8192\n"
    "v_add_f32 v12, 1.0, v12\n"
    "v_mfma_f32_16x16x32_bf16 v[48:51], a[0:3], v[240:243], v[48:51]\n"
    "ds_read_b64_tr_b16 a[14:15], v9 offset:8704\n"
    "v_add_f32 v13, 1.0, v13\n"
    "v_mfma_f32_16x16x32_bf16 v[52:55], a[0:3], a[96:99], v[52:55]\n"
    "ds_read_b64_tr_b16 a[36:37], v8 offset:4096\n"
    "v_add_f32 v14, 1.0, v14\n"
    "v_mfma_f32_16x16x32_bf16 v[56:59], a[0:3], a[168:171], v[56:59]\n"
    "ds_read_b64_tr_b16 a[38:39], v8 offset:4608\n"
    "v_add_f32 v15, 1.0, v15\n"
    "v_mfma_f32_16x16x32_bf16 v[60:63], a[0:3], a[240:243], v[60:63]\n"
    "s_waitcnt lgkmcnt(6)\n"
    "ds_read_b64_tr_b16 a[0:1], v9 offset:4096\n"
    "buffer_load_dwordx4 v3, s[36:39], s58 offen offset:1024 lds\n"
    "v_mfma_f32_16x16x32_bf16 v[64:67], a[4:7], v[240:243], v[64:67]\n"
    "ds_read_b64_tr_b16 a[2:3], v9 offset:4608\n"
    "v_mfma_f32_16x16x32_bf16 v[68:71], a[4:7], a[96:99], v[68:71]\n"
    "v_rcp_f32 v12, v12\n"
    "v_mfma_f32_16x16x32_bf16 v[72:75], a[4:7], a[168:171], v[72:75]\n"
    "v_rcp_f32 v13, v13\n"
    "v_mfma_f32_16x16x32_bf16 v[76:79], a[4:7], a[240:243], v[76:79]\n"
    "s_waitcnt lgkmcnt(6)\n"
    "ds_read_b64_tr_b16 a[4:5], v8 offset:12288\n"
    "v_mfma_f32_16x16x32_bf16 v[80:83], a[8:11], v[240:243], v[80:83]\n"
    "ds_read_b64_tr_b16 a[6:7], v8 offset:12800\n"
    "buffer_load_dwordx4 v4, s[36:39], s58 offen offset:2048 lds\n"
    "v_mfma_f32_16x16x32_bf16 v[84:87], a[8:11], a[96:99], v[84:87]\n"
    "v_rcp_f32 v14, v14\n"
    "v_mfma_f32_16x16x32_bf16 v[88:91], a[8:11], a[168:171], v[88:91]\n"
    "v_rcp_f32 v15, v15\n"
    "v_mfma_f32_16x16x32_bf16 v[92:95], a[8:11], a[240:243], v[92:95]\n"
    "s_waitcnt lgkmcnt(6)\n"
    "ds_read_b64_tr_b16 a[8:9], v9 offset:12288\n"
    "v_mul_f32 v172, v172, v12\n"
    "v_mfma_f32_16x16x32_bf16 v[96:99], a[12:15], v[240:243], v[96:99]\n"
    "ds_read_b64_tr_b16 a[10:11], v9 offset:12800\n"
    "v_mul_f32 v173, v173, v13\n"
    "v_mfma_f32_16x16x32_bf16 v[100:103], a[12:15], a[96:99], v[100:103]\n"
    "buffer_load_dwordx4 v5, s[36:39], s58 offen offset:3072 lds\n"
    "v_mul_f32 v174, v174, v14\n"
    "v_mfma_f32_16x16x32_bf16 v[104:107], a[12:15], a[168:171], v[104:107]\n"
    "v_mul_f32 v175, v175, v15\n"
    "v_cvt_pk_bf16_f32 v126, v172, v173\n"
    "v_mfma_f32_16x16x32_bf16 v[108:111], a[12:15], a[240:243], v[108:111]\n"
    "s_waitcnt lgkmcnt(6)\n"
    "v_cvt_pk_bf16_f32 v127, v174, v175\n"
    "v_mfma_f32_16x16x32_bf16 v[48:51], a[36:39], v[244:247], v[48:51]\n"
    "ds_write_b128 v11, v[124:127] offset:1024\n"
    "v_mfma_f32_16x16x32_bf16 v[52:55], a[36:39], a[100:103], v[52:55]\n"
    "buffer_store_dwordx4 v[124:127], v11, s[48:51], s60 offen offset:1024 sc1\n"
    "v_mfma_f32_16x16x32_bf16 v[56:59], a[36:39], a[172:175], v[56:59]\n"
    "v_mfma_f32_16x16x32_bf16 v[60:63], a[36:39], a[244:247], v[60:63]\n"
    "s_waitcnt lgkmcnt(5)\n"
    "v_mfma_f32_16x16x32_bf16 v[64:67], a[0:3], v[244:247], v[64:67]\n"
    "v_mfma_f32_16x16x32_bf16 v[68:71], a[0:3], a[100:103], v[68:71]\n"
    "v_mfma_f32_16x16x32_bf16 v[72:75], a[0:3], a[172:175], v[72:75]\n"
    "v_mfma_f32_16x16x32_bf16 v[76:79], a[0:3], a[244:247], v[76:79]\n"
    "s_waitcnt lgkmcnt(3)\n"
    "v_mfma_f32_16x16x32_bf16 v[80:83], a[4:7], v[244:247], v[80:83]\n"
    "v_mfma_f32_16x16x32_bf16 v[84:87], a[4:7], a[100:103], v[84:87]\n"
    "v_mfma_f32_16x16x32_bf16 v[88:91], a[4:7], a[172:175], v[88:91]\n"
    "v_mfma_f32_16x16x32_bf16 v[92:95], a[4:7], a[244:247], v[92:95]\n"
    "s_waitcnt lgkmcnt(0)\n"
    "s_barrier\n"
    "ds_read_b64_tr_b16 a[12:13], v8 offset:16384\n"
    "ds_read_b64_tr_b16 a[14:15], v8 offset:16896\n"
    "v_mfma_f32_16x16x32_bf16 v[96:99], a[8:11], v[244:247], v[96:99]\n"
    "s_add_u32 m0, s56, 16384\n"
    "ds_read_b64_tr_b16 a[36:37], v9 offset:16384\n"
    "ds_read_b64_tr_b16 a[38:39], v9 offset:16896\n"
    "v_mfma_f32_16x16x32_bf16 v[100:103], a[8:11], a[100:103], v[100:103]\n"
    "s_add_u32 s58, s61, 2949376\n"
    "ds_read_b64_tr_b16 a[0:1], v8 offset:24576\n"
    "v_mfma_f32_16x16x32_bf16 v[104:107], a[8:11], a[172:175], v[104:107]\n"
    "ds_read_b64_tr_b16 a[2:3], v8 offset:25088\n"
    "buffer_load_dwordx4 v2, s[36:39], s58 offen offset:0 lds\n"
    "v_mfma_f32_16x16x32_bf16 v[108:111], a[8:11], a[244:247], v[108:111]\n"
    "s_waitcnt lgkmcnt(4)\n"
    "ds_read_b64_tr_b16 a[4:5], v9 offset:24576\n"
    "v_mfma_f32_16x16x32_bf16 v[48:51], a[12:15], v[248:251], v[48:51]\n"
    "ds_read_b64_tr_b16 a[6:7], v9 offset:25088\n"
    "v_mfma_f32_16x16x32_bf16 v[52:55], a[12:15], a[104:107], v[52:55]\n"
    "ds_read_b64_tr_b16 a[8:9], v8 offset:20480\n"
    "v_mfma_f32_16x16x32_bf16 v[56:59], a[12:15], a[176:179], v[56:59]\n"
    "ds_read_b64_tr_b16 a[10:11], v8 offset:20992\n"
    "v_mfma_f32_16x16x32_bf16 v[60:63], a[12:15], a[248:251], v[60:63]\n"
    "s_waitcnt lgkmcnt(6)\n"
    "ds_read_b64_tr_b16 a[12:13], v9 offset:20480\n"
    "buffer_load_dwordx4 v3, s[36:39], s58 offen offset:1024 lds\n"
    "v_mfma_f32_16x16x32_bf16 v[64:67], a[36:39], v[248:251], v[64:67]\n"
    "ds_read_b64_tr_b16 a[14:15], v9 offset:20992\n"
    "v_mfma_f32_16x16x32_bf16 v[68:71], a[36:39], a[104:107], v[68:71]\n"
    "v_mfma_f32_16x16x32_bf16 v[72:75], a[36:39], a[176:179], v[72:75]\n"
    "v_mfma_f32_16x16x32_bf16 v[76:79], a[36:39], a[248:251], v[76:79]\n"
    "s_waitcnt lgkmcnt(6)\n"
    "ds_read_b64_tr_b16 a[36:37], v8 offset:28672\n"
    "v_mfma_f32_16x16x32_bf16 v[80:83], a[0:3], v[248:251], v[80:83]\n"
    "ds_read_b64_tr_b16 a[38:39], v8 offset:29184\n"
    "buffer_load_dwordx4 v4, s[36:39], s58 offen offset:2048 lds\n"
    "v_mfma_f32_16x16x32_bf16 v[84:87], a[0:3], a[104:107], v[84:87]\n"
    "v_mfma_f32_16x16x32_bf16 v[88:91], a[0:3], a[176:179], v[88:91]\n"
    "v_mfma_f32_16x16x32_bf16 v[92:95], a[0:3], a[248:251], v[92:95]\n"
    "s_waitcnt lgkmcnt(6)\n"
    "ds_read_b64_tr_b16 a[0:1], v9 offset:28672\n"
    "v_mfma_f32_16x16x32_bf16 v[96:99], a[4:7], v[248:251], v[96:99]\n"
    "ds_read_b64_tr_b16 a[2:3], v9 offset:29184\n"
    "v_mfma_f32_16x16x32_bf16 v[100:103], a[4:7], a[104:107], v[100:103]\n"
    "s_and_b32 s59, s23, 15\n"
    "buffer_load_dwordx4 v5, s[36:39], s58 offen offset:3072 lds\n"
    "s_lshl_b32 s59, s59, 1\n"
    "v_mfma_f32_16x16x32_bf16 v[104:107], a[4:7], a[176:179], v[104:107]\n"
    "s_add_u32 s59, s59, s25\n"
    "s_mul_i32 s59, s59, 165888\n"
    "s_mul_i32 s58, s25, 294912\n"
    "v_mfma_f32_16x16x32_bf16 v[108:111], a[4:7], a[248:251], v[108:111]\n"
    "s_waitcnt lgkmcnt(6)\n"
    "s_sub_u32 s59, s59, s58\n"
    "s_add_u32 s59, s59, s61\n"
    "v_mfma_f32_16x16x32_bf16 v[48:51], a[8:11], v[252:255], v[48:51]\n"
    "s_add_u32 s59, s59, 384\n"
    "v_min_u32 v1, 17, v0\n"
    "v_mul_u32_u24 v1, 9216, v1\n"
    "v_mfma_f32_16x16x32_bf16 v[52:55], a[8:11], a[108:111], v[52:55]\n"
    "buffer_load_dword a34, v1, s[36:39], s59 offen offset:0\n"
    "v_mfma_f32_16x16x32_bf16 v[56:59], a[8:11], a[180:183], v[56:59]\n"
    "v_mfma_f32_16x16x32_bf16 v[60:63], a[8:11], a[252:255], v[60:63]\n"
    "s_waitcnt lgkmcnt(4)\n"
    "v_mfma_f32_16x16x32_bf16 v[64:67], a[12:15], v[252:255], v[64:67]\n"
    "v_mfma_f32_16x16x32_bf16 v[68:71], a[12:15], a[108:111], v[68:71]\n"
    "v_mfma_f32_16x16x32_bf16 v[72:75], a[12:15], a[180:183], v[72:75]\n"
    "v_mfma_f32_16x16x32_bf16 v[76:79], a[12:15], a[252:255], v[76:79]\n"
    "s_waitcnt lgkmcnt(2)\n"
    "v_mfma_f32_16x16x32_bf16 v[80:83], a[36:39], v[252:255], v[80:83]\n"
    "v_mfma_f32_16x16x32_bf16 v[84:87], a[36:39], a[108:111], v[84:87]\n"
    "v_mfma_f32_16x16x32_bf16 v[88:91], a[36:39], a[180:183], v[88:91]\n"
    "v_mfma_f32_16x16x32_bf16 v[92:95], a[36:39], a[252:255], v[92:95]\n"
    "s_waitcnt vmcnt(30) lgkmcnt(0)\n"
    "s_barrier\n"
    "ds_read_b64_tr_b16 a[4:5], v8 offset:32768\n"
    "ds_read_b64_tr_b16 a[6:7], v8 offset:33280\n"
    "v_mfma_f32_16x16x32_bf16 v[96:99], a[0:3], v[252:255], v[96:99]\n"
    "s_add_u32 m0, s56, 32768\n"
    "ds_read_b64_tr_b16 a[8:9], v9 offset:32768\n"
    "ds_read_b64_tr_b16 a[10:11], v9 offset:33280\n"
    "v_mfma_f32_16x16x32_bf16 v[100:103], a[0:3], a[108:111], v[100:103]\n"
    "s_add_u32 s58, s61, 3539200\n"
    "ds_read_b64_tr_b16 a[12:13], v8 offset:40960\n"
    "v_mfma_f32_16x16x32_bf16 v[104:107], a[0:3], a[180:183], v[104:107]\n"
    "ds_read_b64_tr_b16 a[14:15], v8 offset:41472\n"
    "buffer_load_dwordx4 v2, s[36:39], s58 offen offset:0 lds\n"
    "v_mfma_f32_16x16x32_bf16 v[108:111], a[0:3], a[252:255], v[108:111]\n"
    "s_waitcnt lgkmcnt(4)\n"
    "ds_read_b64_tr_b16 a[36:37], v9 offset:40960\n"
    "v_mfma_f32_16x16x32_bf16 v[112:115], a[4:7], v[184:187], 0\n"
    "ds_read_b64_tr_b16 a[38:39], v9 offset:41472\n"
    "v_mfma_f32_16x16x32_bf16 v[116:119], a[4:7], a[40:43], 0\n"
    "ds_read_b64_tr_b16 a[0:1], v8 offset:36864\n"
    "v_mfma_f32_16x16x32_bf16 v[120:123], a[4:7], a[112:115], 0\n"
    "s_add_u32 s60, s92, 16384\n"
    "ds_read_b64_tr_b16 a[2:3], v8 offset:37376\n"
    "v_mfma_f32_16x16x32_bf16 v[124:127], a[4:7], a[184:187], 0\n"
    "s_waitcnt lgkmcnt(6)\n"
    "ds_read_b64_tr_b16 a[4:5], v9 offset:36864\n"
    "buffer_load_dwordx4 v3, s[36:39], s58 offen offset:1024 lds\n"
    "v_mfma_f32_16x16x32_bf16 v[128:131], a[8:11], v[184:187], 0\n"
    "ds_read_b64_tr_b16 a[6:7], v9 offset:37376\n"
    "v_mfma_f32_16x16x32_bf16 v[132:135], a[8:11], a[40:43], 0\n"
    "v_exp_f32 v12, v48\n"
    "v_mfma_f32_16x16x32_bf16 v[136:139], a[8:11], a[112:115], 0\n"
    "v_exp_f32 v13, v49\n"
    "v_mfma_f32_16x16x32_bf16 v[140:143], a[8:11], a[184:187], 0\n"
    "s_waitcnt lgkmcnt(6)\n"
    "ds_read_b64_tr_b16 a[8:9], v8 offset:45056\n"
    "v_mfma_f32_16x16x32_bf16 v[144:147], a[12:15], v[184:187], 0\n"
    "ds_read_b64_tr_b16 a[10:11], v8 offset:45568\n"
    "buffer_load_dwordx4 v4, s[36:39], s58 offen offset:2048 lds\n"
    "v_mfma_f32_16x16x32_bf16 v[148:151], a[12:15], a[40:43], 0\n"
    "v_exp_f32 v14, v50\n"
    "v_mfma_f32_16x16x32_bf16 v[152:155], a[12:15], a[112:115], 0\n"
    "v_exp_f32 v15, v51\n"
    "v_mfma_f32_16x16x32_bf16 v[156:159], a[12:15], a[184:187], 0\n"
    "ds_read_b64_tr_b16 a[12:13], v9 offset:45056\n"
    "s_waitcnt vmcnt(15)\n"
    "v_add_f32 v20, v20, v44\n"
    "s_waitcnt lgkmcnt(7)\n"
    "v_mfma_f32_16x16x32_bf16 v[160:163], a[36:39], v[184:187], 0\n"
    "ds_read_b64_tr_b16 a[14:15], v9 offset:45568\n"
    "v_add_f32 v21, v21, v45\n"
    "v_mfma_f32_16x16x32_bf16 v[164:167], a[36:39], a[40:43], 0\n"
    "buffer_load_dwordx4 v5, s[36:39], s58 offen offset:3072 lds\n"
    "v_add_f32 v22, v22, v46\n"
    "v_mfma_f32_16x16x32_bf16 v[168:171], a[36:39], a[112:115], 0\n"
    "v_add_f32 v23, v23, v47\n"
    "s_waitcnt vmcnt(15)\n"
    "v_add_f32 v24, v24, v176\n"
    "v_mfma_f32_16x16x32_bf16 v[172:175], a[36:39], a[184:187], 0\n"
    "s_waitcnt lgkmcnt(6)\n"
    "v_add_f32 v25, v25, v177\n"
    "v_add_f32 v26, v26, v178\n"
    "v_mfma_f32_16x16x32_bf16 v[112:115], a[0:3], v[188:191], v[112:115]\n"
    "v_add_f32 v27, v27, v179\n"
    "s_waitcnt vmcnt(14)\n"
    "v_add_f32 v28, v28, v180\n"
    "v_mfma_f32_16x16x32_bf16 v[116:119], a[0:3], a[44:47], v[116:119]\n"
    "v_add_f32 v29, v29, v181\n"
    "v_add_f32 v30, v30, v182\n"
    "v_mfma_f32_16x16x32_bf16 v[120:123], a[0:3], a[116:119], v[120:123]\n"
    "v_add_f32 v31, v31, v183\n"
    "v_mul_f32 v80, v48, v80\n"
    "v_mfma_f32_16x16x32_bf16 v[124:127], a[0:3], a[188:191], v[124:127]\n"
    "s_waitcnt lgkmcnt(4)\n"
    "v_mul_f32 v81, v49, v81\n"
    "v_mul_f32 v82, v50, v82\n"
    "v_mfma_f32_16x16x32_bf16 v[128:131], a[4:7], v[188:191], v[128:131]\n"
    "v_mul_f32 v83, v51, v83\n"
    "v_add_f32 v12, 1.0, v12\n"
    "v_mfma_f32_16x16x32_bf16 v[132:135], a[4:7], a[44:47], v[132:135]\n"
    "v_add_f32 v13, 1.0, v13\n"
    "v_add_f32 v14, 1.0, v14\n"
    "v_mfma_f32_16x16x32_bf16 v[136:139], a[4:7], a[116:119], v[136:139]\n"
    "v_add_f32 v15, 1.0, v15\n"
    "v_mfma_f32_16x16x32_bf16 v[140:143], a[4:7], a[188:191], v[140:143]\n"
    "s_waitcnt lgkmcnt(2)\n"
    "v_rcp_f32 v12, v12\n"
    "v_mfma_f32_16x16x32_bf16 v[144:147], a[8:11], v[188:191], v[144:147]\n"
    "v_rcp_f32 v13, v13\n"
    "v_mfma_f32_16x16x32_bf16 v[148:151], a[8:11], a[44:47], v[148:151]\n"
    "v_rcp_f32 v14, v14\n"
    "v_mfma_f32_16x16x32_bf16 v[152:155], a[8:11], a[116:119], v[152:155]\n"
    "v_rcp_f32 v15, v15\n"
    "v_mfma_f32_16x16x32_bf16 v[156:159], a[8:11], a[188:191], v[156:159]\n"
    "s_waitcnt lgkmcnt(0)\n"
    "s_barrier\n"
    "ds_read_b64_tr_b16 a[36:37], v6 offset:0\n"
    "ds_read_b64_tr_b16 a[38:39], v6 offset:512\n"
    "v_mfma_f32_16x16x32_bf16 v[160:163], a[12:15], v[188:191], v[160:163]\n"
    "s_add_u32 m0, s56, 49152\n"
    "ds_read_b64_tr_b16 a[0:1], v7 offset:0\n"
    "ds_read_b64_tr_b16 a[2:3], v7 offset:512\n"
    "v_mfma_f32_16x16x32_bf16 v[164:167], a[12:15], a[44:47], v[164:167]\n"
    "s_add_u32 s58, s61, 4129024\n"
    "ds_read_b64_tr_b16 a[4:5], v6 offset:8192\n"
    "s_add_u32 s86, s71, 4608\n"
    "v_mfma_f32_16x16x32_bf16 v[168:171], a[12:15], a[116:119], v[168:171]\n"
    "ds_read_b64_tr_b16 a[6:7], v6 offset:8704\n"
    "buffer_load_dwordx4 v2, s[36:39], s58 offen offset:0 lds\n"
    "v_mfma_f32_16x16x32_bf16 v[172:175], a[12:15], a[188:191], v[172:175]\n"
    "s_waitcnt lgkmcnt(4)\n"
    "ds_read_b64_tr_b16 a[8:9], v7 offset:8192\n"
    "buffer_load_dwordx4 v[32:35], v18, s[48:51], s86 offen offset:0 sc1\n"
    "v_mfma_f32_16x16x32_bf16 v[112:115], a[36:39], v[192:195], v[112:115]\n"
    "ds_read_b64_tr_b16 a[10:11], v7 offset:8704\n"
    "v_mul_f32 v80, v80, v12\n"
    "v_mfma_f32_16x16x32_bf16 v[116:119], a[36:39], a[48:51], v[116:119]\n"
    "ds_read_b64_tr_b16 a[12:13], v6 offset:4096\n"
    "v_mul_f32 v81, v81, v13\n"
    "v_mfma_f32_16x16x32_bf16 v[120:123], a[36:39], a[120:123], v[120:123]\n"
    "ds_read_b64_tr_b16 a[14:15], v6 offset:4608\n"
    "buffer_load_dwordx4 v[36:39], v18, s[48:51], s86 offen offset:1024 sc1\n"
    "v_mfma_f32_16x16x32_bf16 v[124:127], a[36:39], a[192:195], v[124:127]\n"
    "s_waitcnt lgkmcnt(6)\n"
    "ds_read_b64_tr_b16 a[36:37], v7 offset:4096\n"
    "buffer_load_dwordx4 v3, s[36:39], s58 offen offset:1024 lds\n"
    "v_mfma_f32_16x16x32_bf16 v[128:131], a[0:3], v[192:195], v[128:131]\n"
    "ds_read_b64_tr_b16 a[38:39], v7 offset:4608\n"
    "v_mul_f32 v82, v82, v14\n"
    "v_mfma_f32_16x16x32_bf16 v[132:135], a[0:3], a[48:51], v[132:135]\n"
    "v_mul_f32 v83, v83, v15\n"
    "v_cvt_pk_bf16_f32 v48, v80, v81\n"
    "v_mfma_f32_16x16x32_bf16 v[136:139], a[0:3], a[120:123], v[136:139]\n"
    "v_cvt_pk_bf16_f32 v49, v82, v83\n"
    "v_mfma_f32_16x16x32_bf16 v[140:143], a[0:3], a[192:195], v[140:143]\n"
    "s_waitcnt lgkmcnt(6)\n"
    "ds_read_b64_tr_b16 a[0:1], v6 offset:12288\n"
    "v_mfma_f32_16x16x32_bf16 v[144:147], a[4:7], v[192:195], v[144:147]\n"
    "ds_read_b64_tr_b16 a[2:3], v6 offset:12800\n"
    "buffer_load_dwordx4 v4, s[36:39], s58 offen offset:2048 lds\n"
    "v_mfma_f32_16x16x32_bf16 v[148:151], a[4:7], a[48:51], v[148:151]\n"
    "v_exp_f32 v12, v64\n"
    "v_mfma_f32_16x16x32_bf16 v[152:155], a[4:7], a[120:123], v[152:155]\n"
    "v_exp_f32 v13, v65\n"
    "v_mfma_f32_16x16x32_bf16 v[156:159], a[4:7], a[192:195], v[156:159]\n"
    "s_waitcnt lgkmcnt(6)\n"
    "ds_read_b64_tr_b16 a[4:5], v7 offset:12288\n"
    "v_mfma_f32_16x16x32_bf16 v[160:163], a[8:11], v[192:195], v[160:163]\n"
    "ds_read_b64_tr_b16 a[6:7], v7 offset:12800\n"
    "buffer_load_dwordx4 v[40:43], v19, s[48:51], s86 offen offset:0 sc1\n"
    "s_add_u32 s86, s71, 6912\n"
    "v_mfma_f32_16x16x32_bf16 v[164:167], a[8:11], a[48:51], v[164:167]\n"
    "buffer_load_dwordx4 v5, s[36:39], s58 offen offset:3072 lds\n"
    "v_mfma_f32_16x16x32_bf16 v[168:171], a[8:11], a[120:123], v[168:171]\n"
    "v_exp_f32 v14, v66\n"
    "v_mfma_f32_16x16x32_bf16 v[172:175], a[8:11], a[192:195], v[172:175]\n"
    "s_waitcnt lgkmcnt(6)\n"
    "v_exp_f32 v15, v67\n"
    "v_mfma_f32_16x16x32_bf16 v[112:115], a[12:15], v[196:199], v[112:115]\n"
    "v_mul_f32 v96, v64, v96\n"
    "v_mul_f32 v97, v65, v97\n"
    "v_mfma_f32_16x16x32_bf16 v[116:119], a[12:15], a[52:55], v[116:119]\n"
    "v_mul_f32 v98, v66, v98\n"
    "v_mul_f32 v99, v67, v99\n"
    "v_mfma_f32_16x16x32_bf16 v[120:123], a[12:15], a[124:127], v[120:123]\n"
    "buffer_load_dwordx4 v[44:47], v18, s[48:51], s86 offen offset:0 sc1\n"
    "v_add_f32 v12, 1.0, v12\n"
    "v_mfma_f32_16x16x32_bf16 v[124:127], a[12:15], a[196:199], v[124:127]\n"
    "s_waitcnt lgkmcnt(4)\n"
    "v_add_f32 v13, 1.0, v13\n"
    "v_add_f32 v14, 1.0, v14\n"
    "v_mfma_f32_16x16x32_bf16 v[128:131], a[36:39], v[196:199], v[128:131]\n"
    "v_add_f32 v15, 1.0, v15\n"
    "v_mfma_f32_16x16x32_bf16 v[132:135], a[36:39], a[52:55], v[132:135]\n"
    "v_rcp_f32 v12, v12\n"
    "v_mfma_f32_16x16x32_bf16 v[136:139], a[36:39], a[124:127], v[136:139]\n"
    "buffer_load_dwordx4 v[176:179], v18, s[48:51], s86 offen offset:1024 sc1\n"
    "v_mfma_f32_16x16x32_bf16 v[140:143], a[36:39], a[196:199], v[140:143]\n"
    "s_waitcnt lgkmcnt(2)\n"
    "v_rcp_f32 v13, v13\n"
    "v_mfma_f32_16x16x32_bf16 v[144:147], a[0:3], v[196:199], v[144:147]\n"
    "v_rcp_f32 v14, v14\n"
    "v_mfma_f32_16x16x32_bf16 v[148:151], a[0:3], a[52:55], v[148:151]\n"
    "buffer_load_dwordx4 v[180:183], v19, s[48:51], s86 offen offset:0 sc1\n"
    "v_mfma_f32_16x16x32_bf16 v[152:155], a[0:3], a[124:127], v[152:155]\n"
    "v_rcp_f32 v15, v15\n"
    "v_mfma_f32_16x16x32_bf16 v[156:159], a[0:3], a[196:199], v[156:159]\n"
    "s_waitcnt lgkmcnt(0)\n"
    "s_barrier\n"
    "ds_read_b64_tr_b16 a[8:9], v8 offset:49152\n"
    "ds_read_b64_tr_b16 a[10:11], v8 offset:49664\n"
    "v_mfma_f32_16x16x32_bf16 v[160:163], a[4:7], v[196:199], v[160:163]\n"
    "s_add_u32 m0, s56, 4294926336\n"
    "ds_read_b64_tr_b16 a[12:13], v9 offset:49152\n"
    "ds_read_b64_tr_b16 a[14:15], v9 offset:49664\n"
    "v_mfma_f32_16x16x32_bf16 v[164:167], a[4:7], a[52:55], v[164:167]\n"
    "s_add_u32 s58, s61, 4718848\n"
    "ds_read_b64_tr_b16 a[36:37], v8 offset:57344\n"
    "v_mul_f32 v96, v96, v12\n"
    "v_mfma_f32_16x16x32_bf16 v[168:171], a[4:7], a[124:127], v[168:171]\n"
    "ds_read_b64_tr_b16 a[38:39], v8 offset:57856\n"
    "buffer_load_dwordx4 v2, s[36:39], s58 offen offset:0 lds\n"
    "v_mfma_f32_16x16x32_bf16 v[172:175], a[4:7], a[196:199], v[172:175]\n"
    "s_waitcnt lgkmcnt(4)\n"
    "ds_read_b64_tr_b16 a[0:1], v9 offset:57344\n"
    "v_mul_f32 v97, v97, v13\n"
    "v_mfma_f32_16x16x32_bf16 v[112:115], a[8:11], v[200:203], v[112:115]\n"
    "ds_read_b64_tr_b16 a[2:3], v9 offset:57856\n"
    "v_mul_f32 v98, v98, v14\n"
    "v_mfma_f32_16x16x32_bf16 v[116:119], a[8:11], a[56:59], v[116:119]\n"
    "ds_read_b64_tr_b16 a[4:5], v8 offset:53248\n"
    "v_mul_f32 v99, v99, v15\n"
    "v_mfma_f32_16x16x32_bf16 v[120:123], a[8:11], a[128:131], v[120:123]\n"
    "ds_read_b64_tr_b16 a[6:7], v8 offset:53760\n"
    "v_cvt_pk_bf16_f32 v50, v96, v97\n"
    "v_mfma_f32_16x16x32_bf16 v[124:127], a[8:11], a[200:203], v[124:127]\n"
    "s_waitcnt lgkmcnt(6)\n"
    "ds_read_b64_tr_b16 a[8:9], v9 offset:53248\n"
    "buffer_load_dwordx4 v3, s[36:39], s58 offen offset:1024 lds\n"
    "v_mfma_f32_16x16x32_bf16 v[128:131], a[12:15], v[200:203], v[128:131]\n"
    "ds_read_b64_tr_b16 a[10:11], v9 offset:53760\n"
    "v_cvt_pk_bf16_f32 v51, v98, v99\n"
    "v_mfma_f32_16x16x32_bf16 v[132:135], a[12:15], a[56:59], v[132:135]\n"
    "ds_write_b128 v10, v[48:51] offset:16384\n"
    "v_mfma_f32_16x16x32_bf16 v[136:139], a[12:15], a[128:131], v[136:139]\n"
    "buffer_store_dwordx4 v[48:51], v10, s[48:51], s60 offen offset:0 sc1\n"
    "v_mfma_f32_16x16x32_bf16 v[140:143], a[12:15], a[200:203], v[140:143]\n"
    "s_waitcnt lgkmcnt(7)\n"
    "ds_read_b64_tr_b16 a[12:13], v8 offset:61440\n"
    "v_mfma_f32_16x16x32_bf16 v[144:147], a[36:39], v[200:203], v[144:147]\n"
    "ds_read_b64_tr_b16 a[14:15], v8 offset:61952\n"
    "buffer_load_dwordx4 v4, s[36:39], s58 offen offset:2048 lds\n"
    "v_mfma_f32_16x16x32_bf16 v[148:151], a[36:39], a[56:59], v[148:151]\n"
    "v_exp_f32 v12, v52\n"
    "v_mfma_f32_16x16x32_bf16 v[152:155], a[36:39], a[128:131], v[152:155]\n"
    "v_exp_f32 v13, v53\n"
    "v_mfma_f32_16x16x32_bf16 v[156:159], a[36:39], a[200:203], v[156:159]\n"
    "s_waitcnt lgkmcnt(7)\n"
    "ds_read_b64_tr_b16 a[36:37], v9 offset:61440\n"
    "v_mfma_f32_16x16x32_bf16 v[160:163], a[0:3], v[200:203], v[160:163]\n"
    "ds_read_b64_tr_b16 a[38:39], v9 offset:61952\n"
    "v_mfma_f32_16x16x32_bf16 v[164:167], a[0:3], a[56:59], v[164:167]\n"
    "buffer_load_dwordx4 v5, s[36:39], s58 offen offset:3072 lds\n"
    "v_mfma_f32_16x16x32_bf16 v[168:171], a[0:3], a[128:131], v[168:171]\n"
    "v_exp_f32 v14, v54\n"
    "v_mfma_f32_16x16x32_bf16 v[172:175], a[0:3], a[200:203], v[172:175]\n"
    "s_waitcnt lgkmcnt(7)\n"
    "v_exp_f32 v15, v55\n"
    "v_mfma_f32_16x16x32_bf16 v[112:115], a[4:7], v[204:207], v[112:115]\n"
    "v_mul_f32 v84, v52, v84\n"
    "v_mul_f32 v85, v53, v85\n"
    "v_mfma_f32_16x16x32_bf16 v[116:119], a[4:7], a[60:63], v[116:119]\n"
    "v_mul_f32 v86, v54, v86\n"
    "v_mul_f32 v87, v55, v87\n"
    "v_mfma_f32_16x16x32_bf16 v[120:123], a[4:7], a[132:135], v[120:123]\n"
    "v_add_f32 v12, 1.0, v12\n"
    "v_add_f32 v13, 1.0, v13\n"
    "v_mfma_f32_16x16x32_bf16 v[124:127], a[4:7], a[204:207], v[124:127]\n"
    "s_waitcnt lgkmcnt(5)\n"
    "v_add_f32 v14, 1.0, v14\n"
    "v_add_f32 v15, 1.0, v15\n"
    "v_mfma_f32_16x16x32_bf16 v[128:131], a[8:11], v[204:207], v[128:131]\n"
    "v_rcp_f32 v12, v12\n"
    "v_mfma_f32_16x16x32_bf16 v[132:135], a[8:11], a[60:63], v[132:135]\n"
    "v_rcp_f32 v13, v13\n"
    "v_mfma_f32_16x16x32_bf16 v[136:139], a[8:11], a[132:135], v[136:139]\n"
    "v_rcp_f32 v14, v14\n"
    "v_mfma_f32_16x16x32_bf16 v[140:143], a[8:11], a[204:207], v[140:143]\n"
    "s_waitcnt lgkmcnt(2)\n"
    "v_rcp_f32 v15, v15\n"
    "v_mfma_f32_16x16x32_bf16 v[144:147], a[12:15], v[204:207], v[144:147]\n"
    "v_mul_f32 v84, v84, v12\n"
    "v_mul_f32 v85, v85, v13\n"
    "v_mfma_f32_16x16x32_bf16 v[148:151], a[12:15], a[60:63], v[148:151]\n"
    "L_hop6:\n"
    "s_cbranch_execz L_hop10\n"
    "v_mul_f32 v86, v86, v14\n"
    "v_mul_f32 v87, v87, v15\n"
    "v_mfma_f32_16x16x32_bf16 v[152:155], a[12:15], a[132:135], v[152:155]\n"
    "v_cvt_pk_bf16_f32 v52, v84, v85\n"
    "v_cvt_pk_bf16_f32 v53, v86, v87\n"
    "v_mfma_f32_16x16x32_bf16 v[156:159], a[12:15], a[204:207], v[156:159]\n"
    "s_waitcnt lgkmcnt(0)\n"
    "s_barrier\n"
    "ds_read_b64_tr_b16 a[0:1], v6 offset:16384\n"
    "ds_read_b64_tr_b16 a[2:3], v6 offset:16896\n"
    "v_mfma_f32_16x16x32_bf16 v[160:163], a[36:39], v[204:207], v[160:163]\n"
    "s_add_u32 m0, s56, 65536\n"
    "ds_read_b64_tr_b16 a[4:5], v7 offset:16384\n"
    "ds_read_b64_tr_b16 a[6:7], v7 offset:16896\n"
    "v_mfma_f32_16x16x32_bf16 v[164:167], a[36:39], a[60:63], v[164:167]\n"
    "s_add_u32 s58, s61, 384\n"
    "ds_read_b64_tr_b16 a[8:9], v6 offset:24576\n"
    "v_mfma_f32_16x16x32_bf16 v[168:171], a[36:39], a[132:135], v[168:171]\n"
    "ds_read_b64_tr_b16 a[10:11], v6 offset:25088\n"
    "buffer_load_dwordx4 v2, s[36:39], s58 offen offset:0 lds\n"
    "v_mfma_f32_16x16x32_bf16 v[172:175], a[36:39], a[204:207], v[172:175]\n"
    "s_waitcnt lgkmcnt(4)\n"
    "ds_read_b64_tr_b16 a[12:13], v7 offset:24576\n"
    "v_mfma_f32_16x16x32_bf16 v[112:115], a[0:3], v[208:211], v[112:115]\n"
    "ds_read_b64_tr_b16 a[14:15], v7 offset:25088\n"
    "v_mfma_f32_16x16x32_bf16 v[116:119], a[0:3], a[64:67], v[116:119]\n"
    "ds_read_b64_tr_b16 a[36:37], v6 offset:20480\n"
    "v_mfma_f32_16x16x32_bf16 v[120:123], a[0:3], a[136:139], v[120:123]\n"
    "ds_read_b64_tr_b16 a[38:39], v6 offset:20992\n"
    "v_mfma_f32_16x16x32_bf16 v[124:127], a[0:3], a[208:211], v[124:127]\n"
    "s_waitcnt lgkmcnt(6)\n"
    "ds_read_b64_tr_b16 a[0:1], v7 offset:20480\n"
    "buffer_load_dwordx4 v3, s[36:39], s58 offen offset:1024 lds\n"
    "v_mfma_f32_16x16x32_bf16 v[128:131], a[4:7], v[208:211], v[128:131]\n"
    "ds_read_b64_tr_b16 a[2:3], v7 offset:20992\n"
    "v_mfma_f32_16x16x32_bf16 v[132:135], a[4:7], a[64:67], v[132:135]\n"
    "v_exp_f32 v12, v68\n"
    "v_mfma_f32_16x16x32_bf16 v[136:139], a[4:7], a[136:139], v[136:139]\n"
    "v_exp_f32 v13, v69\n"
    "v_mfma_f32_16x16x32_bf16 v[140:143], a[4:7], a[208:211], v[140:143]\n"
    "s_waitcnt lgkmcnt(6)\n"
    "ds_read_b64_tr_b16 a[4:5], v6 offset:28672\n"
    "v_mfma_f32_16x16x32_bf16 v[144:147], a[8:11], v[208:211], v[144:147]\n"
    "ds_read_b64_tr_b16 a[6:7], v6 offset:29184\n"
    "buffer_load_dwordx4 v4, s[36:39], s58 offen offset:2048 lds\n"
    "v_mfma_f32_16x16x32_bf16 v[148:151], a[8:11], a[64:67], v[148:151]\n"
    "v_exp_f32 v14, v70\n"
    "v_mfma_f32_16x16x32_bf16 v[152:155], a[8:11], a[136:139], v[152:155]\n"
    "v_exp_f32 v15, v71\n"
    "v_mfma_f32_16x16x32_bf16 v[156:159], a[8:11], a[208:211], v[156:159]\n"
    "s_waitcnt lgkmcnt(6)\n"
    "ds_read_b64_tr_b16 a[8:9], v7 offset:28672\n"
    "v_mul_f32 v100, v68, v100\n"
    "v_mfma_f32_16x16x32_bf16 v[160:163], a[12:15], v[208:211], v[160:163]\n"
    "ds_read_b64_tr_b16 a[10:11], v7 offset:29184\n"
    "v_mul_f32 v101, v69, v101\n"
    "v_mfma_f32_16x16x32_bf16 v[164:167], a[12:15], a[64:67], v[164:167]\n"
    "s_and_b32 s59, s23, 15\n"
    "buffer_load_dwordx4 v5, s[36:39], s58 offen offset:3072 lds\n"
    "s_lshl_b32 s59, s59, 1\n"
    "v_mfma_f32_16x16x32_bf16 v[168:171], a[12:15], a[136:139], v[168:171]\n"
    "s_and_b32 s58, s22, 1\n"
    "s_add_u32 s59, s59, s58\n"
    "s_lshl_b32 s59, s59, 2\n"
    "v_mfma_f32_16x16x32_bf16 v[172:175], a[12:15], a[208:211], v[172:175]\n"
    "s_waitcnt lgkmcnt(6)\n"
    "s_add_u32 s59, s59, s21\n"
    "s_mul_i32 s59, s59, 5248\n"
    "v_mfma_f32_16x16x32_bf16 v[112:115], a[36:39], v[212:215], v[112:115]\n"
    "s_lshr_b32 s58, s22, 1\n"
    "s_mul_i32 s58, s58, 663552\n"
    "s_add_u32 s59, s59, s58\n"
    "v_mfma_f32_16x16x32_bf16 v[116:119], a[36:39], a[68:71], v[116:119]\n"
    "s_add_u32 s59, s59, 3072\n"
    "v_min_u32 v1, 40, v0\n"
    "v_lshlrev_b32 v1, 7, v1\n"
    "v_mfma_f32_16x16x32_bf16 v[120:123], a[36:39], a[140:143], v[120:123]\n"
    "s_waitcnt vmcnt(23)\n"
    "buffer_load_dword a34, v1, s[40:43], s59 offen offset:0\n"
    "v_mul_f32 v102, v70, v102\n"
    "v_mfma_f32_16x16x32_bf16 v[124:127], a[36:39], a[212:215], v[124:127]\n"
    "s_waitcnt lgkmcnt(4)\n"
    "v_mul_f32 v103, v71, v103\n"
    "v_add_f32 v12, 1.0, v12\n"
    "v_mfma_f32_16x16x32_bf16 v[128:131], a[0:3], v[212:215], v[128:131]\n"
    "v_add_f32 v13, 1.0, v13\n"
    "v_add_f32 v14, 1.0, v14\n"
    "v_mfma_f32_16x16x32_bf16 v[132:135], a[0:3], a[68:71], v[132:135]\n"
    "v_add_f32 v15, 1.0, v15\n"
    "v_mfma_f32_16x16x32_bf16 v[136:139], a[0:3], a[140:143], v[136:139]\n"
    "v_rcp_f32 v12, v12\n"
    "v_mfma_f32_16x16x32_bf16 v[140:143], a[0:3], a[212:215], v[140:143]\n"
    "s_waitcnt lgkmcnt(2)\n"
    "v_rcp_f32 v13, v13\n"
    "v_mfma_f32_16x16x32_bf16 v[144:147], a[4:7], v[212:215], v[144:147]\n"
    "v_rcp_f32 v14, v14\n"
    "v_mfma_f32_16x16x32_bf16 v[148:151], a[4:7], a[68:71], v[148:151]\n"
    "v_rcp_f32 v15, v15\n"
    "v_mfma_f32_16x16x32_bf16 v[152:155], a[4:7], a[140:143], v[152:155]\n"
    "v_mul_f32 v100, v100, v12\n"
    "v_mul_f32 v101, v101, v13\n"
    "v_mfma_f32_16x16x32_bf16 v[156:159], a[4:7], a[212:215], v[156:159]\n"
    "s_waitcnt lgkmcnt(0)\n"
    "s_barrier\n"
    "ds_read_b64_tr_b16 a[12:13], v6 offset:40960\n"
    "ds_read_b64_tr_b16 a[14:15], v6 offset:41472\n"
    "v_mfma_f32_16x16x32_bf16 v[160:163], a[8:11], v[212:215], v[160:163]\n"
    "s_add_u32 m0, s56, 4294942720\n"
    "ds_read_b64_tr_b16 a[36:37], v7 offset:40960\n"
    "ds_read_b64_tr_b16 a[38:39], v7 offset:41472\n"
    "v_mfma_f32_16x16x32_bf16 v[164:167], a[8:11], a[68:71], v[164:167]\n"
    "s_add_u32 s58, s61, 590208\n"
    "ds_read_b64_tr_b16 a[0:1], v6 offset:49152\n"
    "v_mul_f32 v102, v102, v14\n"
    "v_mfma_f32_16x16x32_bf16 v[168:171], a[8:11], a[140:143], v[168:171]\n"
    "ds_read_b64_tr_b16 a[2:3], v6 offset:49664\n"
    "buffer_load_dwordx4 v2, s[36:39], s58 offen offset:0 lds\n"
    "v_mfma_f32_16x16x32_bf16 v[172:175], a[8:11], a[212:215], v[172:175]\n"
    "s_waitcnt lgkmcnt(4)\n"
    "ds_read_b64_tr_b16 a[4:5], v7 offset:49152\n"
    "v_mul_f32 v103, v103, v15\n"
    "v_mfma_f32_16x16x32_bf16 v[112:115], a[12:15], v[216:219], v[112:115]\n"
    "ds_read_b64_tr_b16 a[6:7], v7 offset:49664\n"
    "v_cvt_pk_bf16_f32 v54, v100, v101\n"
    "v_mfma_f32_16x16x32_bf16 v[116:119], a[12:15], a[72:75], v[116:119]\n"
    "ds_read_b64_tr_b16 a[8:9], v6 offset:45056\n"
    "v_cvt_pk_bf16_f32 v55, v102, v103\n"
    "v_mfma_f32_16x16x32_bf16 v[120:123], a[12:15], a[144:147], v[120:123]\n"
    "ds_read_b64_tr_b16 a[10:11], v6 offset:45568\n"
    "v_mfma_f32_16x16x32_bf16 v[124:127], a[12:15], a[216:219], v[124:127]\n"
    "s_waitcnt lgkmcnt(6)\n"
    "ds_read_b64_tr_b16 a[12:13], v7 offset:45056\n"
    "buffer_load_dwordx4 v3, s[36:39], s58 offen offset:1024 lds\n"
    "v_mfma_f32_16x16x32_bf16 v[128:131], a[36:39], v[216:219], v[128:131]\n"
    "ds_read_b64_tr_b16 a[14:15], v7 offset:45568\n"
    "v_mfma_f32_16x16x32_bf16 v[132:135], a[36:39], a[72:75], v[132:135]\n"
    "ds_write_b128 v10, v[52:55] offset:17408\n"
    "v_mfma_f32_16x16x32_bf16 v[136:139], a[36:39], a[144:147], v[136:139]\n"
    "buffer_store_dwordx4 v[52:55], v10, s[48:51], s60 offen offset:1024 sc1\n"
    "v_mfma_f32_16x16x32_bf16 v[140:143], a[36:39], a[216:219], v[140:143]\n"
    "s_waitcnt lgkmcnt(7)\n"
    "ds_read_b64_tr_b16 a[36:37], v6 offset:53248\n"
    "v_mfma_f32_16x16x32_bf16 v[144:147], a[0:3], v[216:219], v[144:147]\n"
    "ds_read_b64_tr_b16 a[38:39], v6 offset:53760\n"
    "buffer_load_dwordx4 v4, s[36:39], s58 offen offset:2048 lds\n"
    "v_mfma_f32_16x16x32_bf16 v[148:151], a[0:3], a[72:75], v[148:151]\n"
    "v_exp_f32 v12, v56\n"
    "v_mfma_f32_16x16x32_bf16 v[152:155], a[0:3], a[144:147], v[152:155]\n"
    "v_exp_f32 v13, v57\n"
    "v_mfma_f32_16x16x32_bf16 v[156:159], a[0:3], a[216:219], v[156:159]\n"
    "ds_read_b64_tr_b16 a[0:1], v7 offset:53248\n"
    "s_waitcnt vmcnt(22)\n"
    "v_add_f32 v20, v20, v32\n"
    "s_waitcnt lgkmcnt(8)\n"
    "v_mfma_f32_16x16x32_bf16 v[160:163], a[4:7], v[216:219], v[160:163]\n"
    "ds_read_b64_tr_b16 a[2:3], v7 offset:53760\n"
    "v_add_f32 v21, v21, v33\n"
    "v_mfma_f32_16x16x32_bf16 v[164:167], a[4:7], a[72:75], v[164:167]\n"
    "buffer_load_dwordx4 v5, s[36:39], s58 offen offset:3072 lds\n"
    "v_add_f32 v22, v22, v34\n"
    "v_mfma_f32_16x16x32_bf16 v[168:171], a[4:7], a[144:147], v[168:171]\n"
    "v_add_f32 v23, v23, v35\n"
    "s_waitcnt vmcnt(22)\n"
    "v_add_f32 v24, v24, v36\n"
    "v_mfma_f32_16x16x32_bf16 v[172:175], a[4:7], a[216:219], v[172:175]\n"
    "s_waitcnt lgkmcnt(7)\n"
    "v_add_f32 v25, v25, v37\n"
    "v_add_f32 v26, v26, v38\n"
    "v_mfma_f32_16x16x32_bf16 v[112:115], a[8:11], v[220:223], v[112:115]\n"
    "v_add_f32 v27, v27, v39\n"
    "s_waitcnt vmcnt(19)\n"
    "v_add_f32 v28, v28, v40\n"
    "v_mfma_f32_16x16x32_bf16 v[116:119], a[8:11], a[76:79], v[116:119]\n"
    "v_add_f32 v29, v29, v41\n"
    "v_add_f32 v30, v30, v42\n"
    "v_mfma_f32_16x16x32_bf16 v[120:123], a[8:11], a[148:151], v[120:123]\n"
    "v_add_f32 v31, v31, v43\n"
    "s_waitcnt vmcnt(17)\n"
    "v_add_f32 v20, v20, v44\n"
    "v_mfma_f32_16x16x32_bf16 v[124:127], a[8:11], a[220:223], v[124:127]\n"
    "s_waitcnt lgkmcnt(5)\n"
    "v_add_f32 v21, v21, v45\n"
    "v_add_f32 v22, v22, v46\n"
    "v_mfma_f32_16x16x32_bf16 v[128:131], a[12:15], v[220:223], v[128:131]\n"
    "v_add_f32 v23, v23, v47\n"
    "s_waitcnt vmcnt(16)\n"
    "v_add_f32 v24, v24, v176\n"
    "v_mfma_f32_16x16x32_bf16 v[132:135], a[12:15], a[76:79], v[132:135]\n"
    "v_add_f32 v25, v25, v177\n"
    "v_add_f32 v26, v26, v178\n"
    "v_mfma_f32_16x16x32_bf16 v[136:139], a[12:15], a[148:151], v[136:139]\n"
    "v_add_f32 v27, v27, v179\n"
    "s_waitcnt vmcnt(15)\n"
    "v_add_f32 v28, v28, v180\n"
    "v_mfma_f32_16x16x32_bf16 v[140:143], a[12:15], a[220:223], v[140:143]\n"
    "s_waitcnt lgkmcnt(2)\n"
    "v_add_f32 v29, v29, v181\n"
    "v_add_f32 v30, v30, v182\n"
    "v_mfma_f32_16x16x32_bf16 v[144:147], a[36:39], v[220:223], v[144:147]\n"
    "v_add_f32 v31, v31, v183\n"
    "v_mfma_f32_16x16x32_bf16 v[148:151], a[36:39], a[76:79], v[148:151]\n"
    "v_exp_f32 v14, v58\n"
    "v_mfma_f32_16x16x32_bf16 v[152:155], a[36:39], a[148:151], v[152:155]\n"
    "v_exp_f32 v15, v59\n"
    "v_mfma_f32_16x16x32_bf16 v[156:159], a[36:39], a[220:223], v[156:159]\n"
    "s_waitcnt lgkmcnt(0)\n"
    "s_barrier\n"
    "ds_read_b64_tr_b16 a[4:5], v8 offset:0\n"
    "ds_read_b64_tr_b16 a[6:7], v8 offset:512\n"
    "v_mfma_f32_16x16x32_bf16 v[160:163], a[0:3], v[220:223], v[160:163]\n"
    "s_add_u32 m0, s56, 0\n"
    "ds_read_b64_tr_b16 a[8:9], v9 offset:0\n"
    "ds_read_b64_tr_b16 a[10:11], v9 offset:512\n"
    "v_mfma_f32_16x16x32_bf16 v[164:167], a[0:3], a[76:79], v[164:167]\n"
    "s_add_u32 s58, s61, 1180032\n"
    "ds_read_b64_tr_b16 a[12:13], v8 offset:8192\n"
    "s_add_u32 s86, s71, 9216\n"
    "v_mfma_f32_16x16x32_bf16 v[168:171], a[0:3], a[148:151], v[168:171]\n"
    "ds_read_b64_tr_b16 a[14:15], v8 offset:8704\n"
    "buffer_load_dwordx4 v2, s[36:39], s58 offen offset:0 lds\n"
    "v_mfma_f32_16x16x32_bf16 v[172:175], a[0:3], a[220:223], v[172:175]\n"
    "s_waitcnt lgkmcnt(4)\n"
    "ds_read_b64_tr_b16 a[36:37], v9 offset:8192\n"
    "buffer_load_dwordx4 v[32:35], v18, s[48:51], s86 offen offset:0 sc1\n"
    "v_mfma_f32_16x16x32_bf16 v[112:115], a[4:7], v[224:227], v[112:115]\n"
    "ds_read_b64_tr_b16 a[38:39], v9 offset:8704\n"
    "v_mul_f32 v88, v56, v88\n"
    "v_mfma_f32_16x16x32_bf16 v[116:119], a[4:7], a[80:83], v[116:119]\n"
    "ds_read_b64_tr_b16 a[0:1], v8 offset:4096\n"
    "v_mul_f32 v89, v57, v89\n"
    "v_mfma_f32_16x16x32_bf16 v[120:123], a[4:7], a[152:155], v[120:123]\n"
    "ds_read_b64_tr_b16 a[2:3], v8 offset:4608\n"
    "buffer_load_dwordx4 v[36:39], v18, s[48:51], s86 offen offset:1024 sc1\n"
    "v_mfma_f32_16x16x32_bf16 v[124:127], a[4:7], a[224:227], v[124:127]\n"
    "s_waitcnt lgkmcnt(6)\n"
    "ds_read_b64_tr_b16 a[4:5], v9 offset:4096\n"
    "buffer_load_dwordx4 v3, s[36:39], s58 offen offset:1024 lds\n"
    "v_mfma_f32_16x16x32_bf16 v[128:131], a[8:11], v[224:227], v[128:131]\n"
    "ds_read_b64_tr_b16 a[6:7], v9 offset:4608\n"
    "v_mul_f32 v90, v58, v90\n"
    "v_mfma_f32_16x16x32_bf16 v[132:135], a[8:11], a[80:83], v[132:135]\n"
    "v_mul_f32 v91, v59, v91\n"
    "v_add_f32 v12, 1.0, v12\n"
    "v_mfma_f32_16x16x32_bf16 v[136:139], a[8:11], a[152:155], v[136:139]\n"
    "v_add_f32 v13, 1.0, v13\n"
    "v_add_f32 v14, 1.0, v14\n"
    "v_mfma_f32_16x16x32_bf16 v[140:143], a[8:11], a[224:227], v[140:143]\n"
    "s_waitcnt lgkmcnt(6)\n"
    "ds_read_b64_tr_b16 a[8:9], v8 offset:12288\n"
    "v_add_f32 v15, 1.0, v15\n"
    "v_mfma_f32_16x16x32_bf16 v[144:147], a[12:15], v[224:227], v[144:147]\n"
    "ds_read_b64_tr_b16 a[10:11], v8 offset:12800\n"
    "buffer_load_dwordx4 v4, s[36:39], s58 offen offset:2048 lds\n"
    "v_mfma_f32_16x16x32_bf16 v[148:151], a[12:15], a[80:83], v[148:151]\n"
    "v_rcp_f32 v12, v12\n"
    "v_mfma_f32_16x16x32_bf16 v[152:155], a[12:15], a[152:155], v[152:155]\n"
    "v_rcp_f32 v13, v13\n"
    "v_mfma_f32_16x16x32_bf16 v[156:159], a[12:15], a[224:227], v[156:159]\n"
    "s_waitcnt lgkmcnt(6)\n"
    "ds_read_b64_tr_b16 a[12:13], v9 offset:12288\n"
    "v_mfma_f32_16x16x32_bf16 v[160:163], a[36:39], v[224:227], v[160:163]\n"
    "ds_read_b64_tr_b16 a[14:15], v9 offset:12800\n"
    "buffer_load_dwordx4 v[40:43], v19, s[48:51], s86 offen offset:0 sc1\n"
    "s_add_u32 s86, s71, 11520\n"
    "v_mfma_f32_16x16x32_bf16 v[164:167], a[36:39], a[80:83], v[164:167]\n"
    "buffer_load_dwordx4 v5, s[36:39], s58 offen offset:3072 lds\n"
    "v_mfma_f32_16x16x32_bf16 v[168:171], a[36:39], a[152:155], v[168:171]\n"
    "v_rcp_f32 v14, v14\n"
    "v_mfma_f32_16x16x32_bf16 v[172:175], a[36:39], a[224:227], v[172:175]\n"
    "s_waitcnt lgkmcnt(6)\n"
    "v_rcp_f32 v15, v15\n"
    "v_mfma_f32_16x16x32_bf16 v[112:115], a[0:3], v[228:231], v[112:115]\n"
    "v_mul_f32 v88, v88, v12\n"
    "v_mul_f32 v89, v89, v13\n"
    "v_mfma_f32_16x16x32_bf16 v[116:119], a[0:3], a[84:87], v[116:119]\n"
    "v_mul_f32 v90, v90, v14\n"
    "v_mul_f32 v91, v91, v15\n"
    "v_mfma_f32_16x16x32_bf16 v[120:123], a[0:3], a[156:159], v[120:123]\n"
    "buffer_load_dwordx4 v[44:47], v18, s[48:51], s86 offen offset:0 sc1\n"
    "v_cvt_pk_bf16_f32 v56, v88, v89\n"
    "v_mfma_f32_16x16x32_bf16 v[124:127], a[0:3], a[228:231], v[124:127]\n"
    "s_waitcnt lgkmcnt(4)\n"
    "v_cvt_pk_bf16_f32 v57, v90, v91\n"
    "v_mfma_f32_16x16x32_bf16 v[128:131], a[4:7], v[228:231], v[128:131]\n"
    "v_exp_f32 v12, v72\n"
    "v_mfma_f32_16x16x32_bf16 v[132:135], a[4:7], a[84:87], v[132:135]\n"
    "v_exp_f32 v13, v73\n"
    "v_mfma_f32_16x16x32_bf16 v[136:139], a[4:7], a[156:159], v[136:139]\n"
    "buffer_load_dwordx4 v[176:179], v18, s[48:51], s86 offen offset:1024 sc1\n"
    "v_mfma_f32_16x16x32_bf16 v[140:143], a[4:7], a[228:231], v[140:143]\n"
    "s_waitcnt lgkmcnt(2)\n"
    "v_exp_f32 v14, v74\n"
    "v_mfma_f32_16x16x32_bf16 v[144:147], a[8:11], v[228:231], v[144:147]\n"
    "v_exp_f32 v15, v75\n"
    "v_mfma_f32_16x16x32_bf16 v[148:151], a[8:11], a[84:87], v[148:151]\n"
    "buffer_load_dwordx4 v[180:183], v19, s[48:51], s86 offen offset:0 sc1\n"
    "v_mul_f32 v104, v72, v104\n"
    "v_mfma_f32_16x16x32_bf16 v[152:155], a[8:11], a[156:159], v[152:155]\n"
    "v_mul_f32 v105, v73, v105\n"
    "v_mul_f32 v106, v74, v106\n"
    "v_mfma_f32_16x16x32_bf16 v[156:159], a[8:11], a[228:231], v[156:159]\n"
    "s_waitcnt lgkmcnt(0)\n"
    "s_barrier\n"
    "ds_read_b64_tr_b16 a[36:37], v8 offset:16384\n"
    "ds_read_b64_tr_b16 a[38:39], v8 offset:16896\n"
    "v_mfma_f32_16x16x32_bf16 v[160:163], a[12:15], v[228:231], v[160:163]\n"
    "s_add_u32 m0, s56, 16384\n"
    "ds_read_b64_tr_b16 a[0:1], v9 offset:16384\n"
    "ds_read_b64_tr_b16 a[2:3], v9 offset:16896\n"
    "v_mfma_f32_16x16x32_bf16 v[164:167], a[12:15], a[84:87], v[164:167]\n"
    "s_add_u32 s58, s61, 1769856\n"
    "ds_read_b64_tr_b16 a[4:5], v8 offset:24576\n"
    "v_mul_f32 v107, v75, v107\n"
    "v_mfma_f32_16x16x32_bf16 v[168:171], a[12:15], a[156:159], v[168:171]\n"
    "ds_read_b64_tr_b16 a[6:7], v8 offset:25088\n"
    "buffer_load_dwordx4 v2, s[36:39], s58 offen offset:0 lds\n"
    "v_mfma_f32_16x16x32_bf16 v[172:175], a[12:15], a[228:231], v[172:175]\n"
    "s_waitcnt lgkmcnt(4)\n"
    "ds_read_b64_tr_b16 a[8:9], v9 offset:24576\n"
    "v_add_f32 v12, 1.0, v12\n"
    "v_mfma_f32_16x16x32_bf16 v[112:115], a[36:39], v[232:235], v[112:115]\n"
    "ds_read_b64_tr_b16 a[10:11], v9 offset:25088\n"
    "v_add_f32 v13, 1.0, v13\n"
    "v_mfma_f32_16x16x32_bf16 v[116:119], a[36:39], a[88:91], v[116:119]\n"
    "ds_read_b64_tr_b16 a[12:13], v8 offset:20480\n"
    "v_add_f32 v14, 1.0, v14\n"
    "v_mfma_f32_16x16x32_bf16 v[120:123], a[36:39], a[160:163], v[120:123]\n"
    "ds_read_b64_tr_b16 a[14:15], v8 offset:20992\n"
    "v_add_f32 v15, 1.0, v15\n"
    "v_mfma_f32_16x16x32_bf16 v[124:127], a[36:39], a[232:235], v[124:127]\n"
    "s_waitcnt lgkmcnt(6)\n"
    "ds_read_b64_tr_b16 a[36:37], v9 offset:20480\n"
    "buffer_load_dwordx4 v3, s[36:39], s58 offen offset:1024 lds\n"
    "v_mfma_f32_16x16x32_bf16 v[128:131], a[0:3], v[232:235], v[128:131]\n"
    "ds_read_b64_tr_b16 a[38:39], v9 offset:20992\n"
    "v_mfma_f32_16x16x32_bf16 v[132:135], a[0:3], a[88:91], v[132:135]\n"
    "v_rcp_f32 v12, v12\n"
    "v_mfma_f32_16x16x32_bf16 v[136:139], a[0:3], a[160:163], v[136:139]\n"
    "v_rcp_f32 v13, v13\n"
    "v_mfma_f32_16x16x32_bf16 v[140:143], a[0:3], a[232:235], v[140:143]\n"
    "s_waitcnt lgkmcnt(6)\n"
    "ds_read_b64_tr_b16 a[0:1], v8 offset:28672\n"
    "v_mfma_f32_16x16x32_bf16 v[144:147], a[4:7], v[232:235], v[144:147]\n"
    "ds_read_b64_tr_b16 a[2:3], v8 offset:29184\n"
    "buffer_load_dwordx4 v4, s[36:39], s58 offen offset:2048 lds\n"
    "v_mfma_f32_16x16x32_bf16 v[148:151], a[4:7], a[88:91], v[148:151]\n"
    "v_rcp_f32 v14, v14\n"
    "v_mfma_f32_16x16x32_bf16 v[152:155], a[4:7], a[160:163], v[152:155]\n"
    "v_rcp_f32 v15, v15\n"
    "v_mfma_f32_16x16x32_bf16 v[156:159], a[4:7], a[232:235], v[156:159]\n"
    "s_waitcnt lgkmcnt(6)\n"
    "ds_read_b64_tr_b16 a[4:5], v9 offset:28672\n"
    "v_mul_f32 v104, v104, v12\n"
    "v_mfma_f32_16x16x32_bf16 v[160:163], a[8:11], v[232:235], v[160:163]\n"
    "ds_read_b64_tr_b16 a[6:7], v9 offset:29184\n"
    "v_mul_f32 v105, v105, v13\n"
    "v_mfma_f32_16x16x32_bf16 v[164:167], a[8:11], a[88:91], v[164:167]\n"
    "buffer_load_dwordx4 v5, s[36:39], s58 offen offset:3072 lds\n"
    "v_mul_f32 v106, v106, v14\n"
    "v_mfma_f32_16x16x32_bf16 v[168:171], a[8:11], a[160:163], v[168:171]\n"
    "v_mul_f32 v107, v107, v15\n"
    "v_cvt_pk_bf16_f32 v58, v104, v105\n"
    "v_mfma_f32_16x16x32_bf16 v[172:175], a[8:11], a[232:235], v[172:175]\n"
    "s_waitcnt lgkmcnt(6)\n"
    "v_cvt_pk_bf16_f32 v59, v106, v107\n"
    "v_mfma_f32_16x16x32_bf16 v[112:115], a[12:15], v[236:239], v[112:115]\n"
    "ds_write_b128 v11, v[56:59] offset:16384\n"
    "v_mfma_f32_16x16x32_bf16 v[116:119], a[12:15], a[92:95], v[116:119]\n"
    "buffer_store_dwordx4 v[56:59], v11, s[48:51], s60 offen offset:0 sc1\n"
    "v_mfma_f32_16x16x32_bf16 v[120:123], a[12:15], a[164:167], v[120:123]\n"
    "v_exp_f32 v12, v60\n"
    "v_mfma_f32_16x16x32_bf16 v[124:127], a[12:15], a[236:239], v[124:127]\n"
    "s_waitcnt lgkmcnt(5)\n"
    "v_exp_f32 v13, v61\n"
    "v_mfma_f32_16x16x32_bf16 v[128:131], a[36:39], v[236:239], v[128:131]\n"
    "v_exp_f32 v14, v62\n"
    "v_mfma_f32_16x16x32_bf16 v[132:135], a[36:39], a[92:95], v[132:135]\n"
    "v_exp_f32 v15, v63\n"
    "v_mfma_f32_16x16x32_bf16 v[136:139], a[36:39], a[164:167], v[136:139]\n"
    "v_mul_f32 v92, v60, v92\n"
    "v_mul_f32 v93, v61, v93\n"
    "v_mfma_f32_16x16x32_bf16 v[140:143], a[36:39], a[236:239], v[140:143]\n"
    "s_waitcnt lgkmcnt(3)\n"
    "v_mul_f32 v94, v62, v94\n"
    "v_mul_f32 v95, v63, v95\n"
    "v_mfma_f32_16x16x32_bf16 v[144:147], a[0:3], v[236:239], v[144:147]\n"
    "v_add_f32 v12, 1.0, v12\n"
    "v_add_f32 v13, 1.0, v13\n"
    "v_mfma_f32_16x16x32_bf16 v[148:151], a[0:3], a[92:95], v[148:151]\n"
    "v_add_f32 v14, 1.0, v14\n"
    "v_add_f32 v15, 1.0, v15\n"
    "v_mfma_f32_16x16x32_bf16 v[152:155], a[0:3], a[164:167], v[152:155]\n"
    "v_rcp_f32 v12, v12\n"
    "v_mfma_f32_16x16x32_bf16 v[156:159], a[0:3], a[236:239], v[156:159]\n"
    "s_waitcnt lgkmcnt(0)\n"
    "s_barrier\n"
    "ds_read_b64_tr_b16 a[8:9], v8 offset:32768\n"
    "ds_read_b64_tr_b16 a[10:11], v8 offset:33280\n"
    "v_mfma_f32_16x16x32_bf16 v[160:163], a[4:7], v[236:239], v[160:163]\n"
    "s_add_u32 m0, s56, 32768\n"
    "ds_read_b64_tr_b16 a[12:13], v9 offset:32768\n"
    "ds_read_b64_tr_b16 a[14:15], v9 offset:33280\n"
    "v_mfma_f32_16x16x32_bf16 v[164:167], a[4:7], a[92:95], v[164:167]\n"
    "s_add_u32 s58, s61, 2359680\n"
    "ds_read_b64_tr_b16 a[36:37], v8 offset:40960\n"
    "v_mfma_f32_16x16x32_bf16 v[168:171], a[4:7], a[164:167], v[168:171]\n"
    "ds_read_b64_tr_b16 a[38:39], v8 offset:41472\n"
    "buffer_load_dwordx4 v2, s[36:39], s58 offen offset:0 lds\n"
    "v_mfma_f32_16x16x32_bf16 v[172:175], a[4:7], a[236:239], v[172:175]\n"
    "s_waitcnt lgkmcnt(4)\n"
    "ds_read_b64_tr_b16 a[0:1], v9 offset:40960\n"
    "v_mfma_f32_16x16x32_bf16 v[112:115], a[8:11], v[240:243], v[112:115]\n"
    "ds_read_b64_tr_b16 a[2:3], v9 offset:41472\n"
    "v_mfma_f32_16x16x32_bf16 v[116:119], a[8:11], a[96:99], v[116:119]\n"
    "ds_read_b64_tr_b16 a[4:5], v8 offset:36864\n"
    "v_mfma_f32_16x16x32_bf16 v[120:123], a[8:11], a[168:171], v[120:123]\n"
    "ds_read_b64_tr_b16 a[6:7], v8 offset:37376\n"
    "v_mfma_f32_16x16x32_bf16 v[124:127], a[8:11], a[240:243], v[124:127]\n"
    "s_waitcnt lgkmcnt(6)\n"
    "ds_read_b64_tr_b16 a[8:9], v9 offset:36864\n"
    "buffer_load_dwordx4 v3, s[36:39], s58 offen offset:1024 lds\n"
    "v_mfma_f32_16x16x32_bf16 v[128:131], a[12:15], v[240:243], v[128:131]\n"
    "ds_read_b64_tr_b16 a[10:11], v9 offset:37376\n"
    "v_mfma_f32_16x16x32_bf16 v[132:135], a[12:15], a[96:99], v[132:135]\n"
    "v_rcp_f32 v13, v13\n"
    "v_mfma_f32_16x16x32_bf16 v[136:139], a[12:15], a[168:171], v[136:139]\n"
    "v_rcp_f32 v14, v14\n"
    "v_mfma_f32_16x16x32_bf16 v[140:143], a[12:15], a[240:243], v[140:143]\n"
    "s_waitcnt lgkmcnt(6)\n"
    "ds_read_b64_tr_b16 a[12:13], v8 offset:45056\n"
    "v_mfma_f32_16x16x32_bf16 v[144:147], a[36:39], v[240:243], v[144:147]\n"
    "ds_read_b64_tr_b16 a[14:15], v8 offset:45568\n"
    "buffer_load_dwordx4 v4, s[36:39], s58 offen offset:2048 lds\n"
    "v_mfma_f32_16x16x32_bf16 v[148:151], a[36:39], a[96:99], v[148:151]\n"
    "v_rcp_f32 v15, v15\n"
    "v_mfma_f32_16x16x32_bf16 v[152:155], a[36:39], a[168:171], v[152:155]\n"
    "v_mul_f32 v92, v92, v12\n"
    "v_mul_f32 v93, v93, v13\n"
    "v_mfma_f32_16x16x32_bf16 v[156:159], a[36:39], a[240:243], v[156:159]\n"
    "s_waitcnt lgkmcnt(6)\n"
    "ds_read_b64_tr_b16 a[36:37], v9 offset:45056\n"
    "v_mul_f32 v94, v94, v14\n"
    "v_mfma_f32_16x16x32_bf16 v[160:163], a[0:3], v[240:243], v[160:163]\n"
    "ds_read_b64_tr_b16 a[38:39], v9 offset:45568\n"
    "v_mul_f32 v95, v95, v15\n"
    "v_mfma_f32_16x16x32_bf16 v[164:167], a[0:3], a[96:99], v[164:167]\n"
    "s_and_b32 s59, s23, 15\n"
    "buffer_load_dwordx4 v5, s[36:39], s58 offen offset:3072 lds\n"
    "s_lshl_b32 s59, s59, 1\n"
    "v_mfma_f32_16x16x32_bf16 v[168:171], a[0:3], a[168:171], v[168:171]\n"
    "s_add_u32 s59, s59, s25\n"
    "s_mul_i32 s59, s59, 165888\n"
    "s_mul_i32 s58, s25, 589824\n"
    "v_mfma_f32_16x16x32_bf16 v[172:175], a[0:3], a[240:243], v[172:175]\n"
    "s_waitcnt lgkmcnt(6)\n"
    "s_sub_u32 s59, s59, s58\n"
    "s_add_u32 s59, s59, s62\n"
    "v_mfma_f32_16x16x32_bf16 v[112:115], a[4:7], v[244:247], v[112:115]\n"
    "s_add_u32 s59, s59, 0\n"
    "v_min_u32 v1, 17, v0\n"
    "v_mul_u32_u24 v1, 9216, v1\n"
    "v_mfma_f32_16x16x32_bf16 v[116:119], a[4:7], a[100:103], v[116:119]\n"
    "s_waitcnt vmcnt(24)\n"
    "buffer_load_dword a34, v1, s[36:39], s59 offen offset:0\n"
    "v_cvt_pk_bf16_f32 v60, v92, v93\n"
    "v_mfma_f32_16x16x32_bf16 v[120:123], a[4:7], a[172:175], v[120:123]\n"
    "v_cvt_pk_bf16_f32 v61, v94, v95\n"
    "v_mfma_f32_16x16x32_bf16 v[124:127], a[4:7], a[244:247], v[124:127]\n"
    "s_waitcnt lgkmcnt(4)\n"
    "v_exp_f32 v12, v76\n"
    "v_mfma_f32_16x16x32_bf16 v[128:131], a[8:11], v[244:247], v[128:131]\n"
    "v_exp_f32 v13, v77\n"
    "v_mfma_f32_16x16x32_bf16 v[132:135], a[8:11], a[100:103], v[132:135]\n"
    "v_exp_f32 v14, v78\n"
    "v_mfma_f32_16x16x32_bf16 v[136:139], a[8:11], a[172:175], v[136:139]\n"
    "v_exp_f32 v15, v79\n"
    "v_mfma_f32_16x16x32_bf16 v[140:143], a[8:11], a[244:247], v[140:143]\n"
    "s_waitcnt lgkmcnt(2)\n"
    "v_mul_f32 v108, v76, v108\n"
    "v_mul_f32 v109, v77, v109\n"
    "v_mul_f32 v110, v78, v110\n"
    "v_mfma_f32_16x16x32_bf16 v[144:147], a[12:15], v[244:247], v[144:147]\n"
    "v_mul_f32 v111, v79, v111\n"
    "v_add_f32 v12, 1.0, v12\n"
    "v_mfma_f32_16x16x32_bf16 v[148:151], a[12:15], a[100:103], v[148:151]\n"
    "v_add_f32 v13, 1.0, v13\n"
    "v_add_f32 v14, 1.0, v14\n"
    "v_mfma_f32_16x16x32_bf16 v[152:155], a[12:15], a[172:175], v[152:155]\n"
    "v_add_f32 v15, 1.0, v15\n"
    "v_mfma_f32_16x16x32_bf16 v[156:159], a[12:15], a[244:247], v[156:159]\n"
    "s_waitcnt lgkmcnt(0)\n"
    "s_barrier\n"
    "ds_read_b64_tr_b16 a[0:1], v6 offset:0\n"
    "ds_read_b64_tr_b16 a[2:3], v6 offset:512\n"
    "v_mfma_f32_16x16x32_bf16 v[160:163], a[36:39], v[244:247], v[160:163]\n"
    "s_add_u32 m0, s56, 49152\n"
    "ds_read_b64_tr_b16 a[4:5], v7 offset:0\n"
    "ds_read_b64_tr_b16 a[6:7], v7 offset:512\n"
    "v_mfma_f32_16x16x32_bf16 v[164:167], a[36:39], a[100:103], v[164:167]\n"
    "s_add_u32 s58, s61, 2949504\n"
    "ds_read_b64_tr_b16 a[8:9], v6 offset:8192\n"
    "v_rcp_f32 v12, v12\n"
    "v_mfma_f32_16x16x32_bf16 v[168:171], a[36:39], a[172:175], v[168:171]\n"
    "ds_read_b64_tr_b16 a[10:11], v6 offset:8704\n"
    "buffer_load_dwordx4 v2, s[36:39], s58 offen offset:0 lds\n"
    "v_mfma_f32_16x16x32_bf16 v[172:175], a[36:39], a[244:247], v[172:175]\n"
    "s_waitcnt lgkmcnt(4)\n"
    "ds_read_b64_tr_b16 a[12:13], v7 offset:8192\n"
    "v_mfma_f32_16x16x32_bf16 v[112:115], a[0:3], v[248:251], v[112:115]\n"
    "ds_read_b64_tr_b16 a[14:15], v7 offset:8704\n"
    "v_mfma_f32_16x16x32_bf16 v[116:119], a[0:3], a[104:107], v[116:119]\n"
    "ds_read_b64_tr_b16 a[36:37], v6 offset:4096\n"
    "v_mfma_f32_16x16x32_bf16 v[120:123], a[0:3], a[176:179], v[120:123]\n"
    "ds_read_b64_tr_b16 a[38:39], v6 offset:4608\n"
    "v_mfma_f32_16x16x32_bf16 v[124:127], a[0:3], a[248:251], v[124:127]\n"
    "s_waitcnt lgkmcnt(6)\n"
    "ds_read_b64_tr_b16 a[0:1], v7 offset:4096\n"
    "buffer_load_dwordx4 v3, s[36:39], s58 offen offset:1024 lds\n"
    "v_mfma_f32_16x16x32_bf16 v[128:131], a[4:7], v[248:251], v[128:131]\n"
    "ds_read_b64_tr_b16 a[2:3], v7 offset:4608\n"
    "v_mfma_f32_16x16x32_bf16 v[132:135], a[4:7], a[104:107], v[132:135]\n"
    "v_rcp_f32 v13, v13\n"
    "v_mfma_f32_16x16x32_bf16 v[136:139], a[4:7], a[176:179], v[136:139]\n"
    "v_rcp_f32 v14, v14\n"
    "v_mfma_f32_16x16x32_bf16 v[140:143], a[4:7], a[248:251], v[140:143]\n"
    "s_waitcnt lgkmcnt(6)\n"
    "ds_read_b64_tr_b16 a[4:5], v6 offset:12288\n"
    "v_mfma_f32_16x16x32_bf16 v[144:147], a[8:11], v[248:251], v[144:147]\n"
    "ds_read_b64_tr_b16 a[6:7], v6 offset:12800\n"
    "buffer_load_dwordx4 v4, s[36:39], s58 offen offset:2048 lds\n"
    "v_mfma_f32_16x16x32_bf16 v[148:151], a[8:11], a[104:107], v[148:151]\n"
    "v_rcp_f32 v15, v15\n"
    "v_mfma_f32_16x16x32_bf16 v[152:155], a[8:11], a[176:179], v[152:155]\n"
    "v_mul_f32 v108, v108, v12\n"
    "v_mul_f32 v109, v109, v13\n"
    "v_mfma_f32_16x16x32_bf16 v[156:159], a[8:11], a[248:251], v[156:159]\n"
    "s_waitcnt lgkmcnt(6)\n"
    "ds_read_b64_tr_b16 a[8:9], v7 offset:12288\n"
    "v_mul_f32 v110, v110, v14\n"
    "v_mfma_f32_16x16x32_bf16 v[160:163], a[12:15], v[248:251], v[160:163]\n"
    "ds_read_b64_tr_b16 a[10:11], v7 offset:12800\n"
    "v_mul_f32 v111, v111, v15\n"
    "v_mfma_f32_16x16x32_bf16 v[164:167], a[12:15], a[104:107], v[164:167]\n"
    "buffer_load_dwordx4 v5, s[36:39], s58 offen offset:3072 lds\n"
    "v_cvt_pk_bf16_f32 v62, v108, v109\n"
    "v_cvt_pk_bf16_f32 v63, v110, v111\n"
    "v_mfma_f32_16x16x32_bf16 v[168:171], a[12:15], a[176:179], v[168:171]\n"
    "ds_write_b128 v11, v[60:63] offset:17408\n"
    "v_mfma_f32_16x16x32_bf16 v[172:175], a[12:15], a[248:251], v[172:175]\n"
    "buffer_store_dwordx4 v[60:63], v11, s[48:51], s60 offen offset:1024 sc1\n"
    "s_waitcnt vmcnt(23)\n"
    "v_add_f32 v20, v20, v32\n"
    "s_waitcnt lgkmcnt(7)\n"
    "v_mfma_f32_16x16x32_bf16 v[112:115], a[36:39], v[252:255], v[112:115]\n"
    "v_add_f32 v21, v21, v33\n"
    "v_add_f32 v22, v22, v34\n"
    "v_mfma_f32_16x16x32_bf16 v[116:119], a[36:39], a[108:111], v[116:119]\n"
    "v_add_f32 v23, v23, v35\n"
    "s_waitcnt vmcnt(22)\n"
    "v_add_f32 v24, v24, v36\n"
    "v_mfma_f32_16x16x32_bf16 v[120:123], a[36:39], a[180:183], v[120:123]\n"
    "v_add_f32 v25, v25, v37\n"
    "v_add_f32 v26, v26, v38\n"
    "v_add_f32 v27, v27, v39\n"
    "v_mfma_f32_16x16x32_bf16 v[124:127], a[36:39], a[252:255], v[124:127]\n"
    "s_waitcnt vmcnt(19)\n"
    "v_add_f32 v28, v28, v40\n"
    "v_add_f32 v29, v29, v41\n"
    "s_waitcnt lgkmcnt(5)\n"
    "v_mfma_f32_16x16x32_bf16 v[128:131], a[0:3], v[252:255], v[128:131]\n"
    "v_add_f32 v30, v30, v42\n"
    "v_add_f32 v31, v31, v43\n"
    "v_mfma_f32_16x16x32_bf16 v[132:135], a[0:3], a[108:111], v[132:135]\n"
    "s_waitcnt vmcnt(17)\n"
    "v_add_f32 v20, v20, v44\n"
    "v_add_f32 v21, v21, v45\n"
    "v_mfma_f32_16x16x32_bf16 v[136:139], a[0:3], a[180:183], v[136:139]\n"
    "v_add_f32 v22, v22, v46\n"
    "v_add_f32 v23, v23, v47\n"
    "v_mfma_f32_16x16x32_bf16 v[140:143], a[0:3], a[252:255], v[140:143]\n"
    "s_waitcnt vmcnt(16)\n"
    "v_add_f32 v24, v24, v176\n"
    "v_add_f32 v25, v25, v177\n"
    "v_add_f32 v26, v26, v178\n"
    "s_waitcnt lgkmcnt(3)\n"
    "v_mfma_f32_16x16x32_bf16 v[144:147], a[4:7], v[252:255], v[144:147]\n"
    "v_add_f32 v27, v27, v179\n"
    "s_waitcnt vmcnt(15)\n"
    "v_add_f32 v28, v28, v180\n"
    "v_mfma_f32_16x16x32_bf16 v[148:151], a[4:7], a[108:111], v[148:151]\n"
    "v_add_f32 v29, v29, v181\n"
    "v_add_f32 v30, v30, v182\n"
    "v_mfma_f32_16x16x32_bf16 v[152:155], a[4:7], a[180:183], v[152:155]\n"
    "v_add_f32 v31, v31, v183\n"
    "v_mfma_f32_16x16x32_bf16 v[156:159], a[4:7], a[252:255], v[156:159]\n"
    "s_waitcnt lgkmcnt(0)\n"
    "s_barrier\n"
    "ds_read_b64_tr_b16 a[12:13], v8 offset:49152\n"
    "ds_read_b64_tr_b16 a[14:15], v8 offset:49664\n"
    "v_mfma_f32_16x16x32_bf16 v[160:163], a[8:11], v[252:255], v[160:163]\n"
    "s_add_u32 s86, s71, 13824\n"
    "ds_read_b64_tr_b16 a[36:37], v9 offset:49152\n"
    "ds_read_b64_tr_b16 a[38:39], v9 offset:49664\n"
    "buffer_load_dwordx4 v[32:35], v18, s[48:51], s86 offen offset:0 sc1\n"
    "v_mfma_f32_16x16x32_bf16 v[164:167], a[8:11], a[108:111], v[164:167]\n"
    "ds_read_b64_tr_b16 a[0:1], v8 offset:57344\n"
    "buffer_load_dwordx4 v[36:39], v18, s[48:51], s86 offen offset:1024 sc1\n"
    "v_mfma_f32_16x16x32_bf16 v[168:171], a[8:11], a[180:183], v[168:171]\n"
    "ds_read_b64_tr_b16 a[2:3], v8 offset:57856\n"
    "v_mfma_f32_16x16x32_bf16 v[172:175], a[8:11], a[252:255], v[172:175]\n"
    "s_waitcnt lgkmcnt(4)\n"
    "ds_read_b64_tr_b16 a[4:5], v9 offset:57344\n"
    "v_mfma_f32_16x16x32_bf16 v[48:51], a[12:15], v[184:187], 0\n"
    "ds_read_b64_tr_b16 a[6:7], v9 offset:57856\n"
    "buffer_load_dwordx4 v[40:43], v19, s[48:51], s86 offen offset:0 sc1\n"
    "s_add_u32 s86, s71, 16128\n"
    "v_mfma_f32_16x16x32_bf16 v[52:55], a[12:15], a[40:43], 0\n"
    "ds_read_b64_tr_b16 a[8:9], v8 offset:53248\n"
    "v_mfma_f32_16x16x32_bf16 v[56:59], a[12:15], a[112:115], 0\n"
    "s_add_u32 s60, s92, 32768\n"
    "ds_read_b64_tr_b16 a[10:11], v8 offset:53760\n"
    "v_mfma_f32_16x16x32_bf16 v[60:63], a[12:15], a[184:187], 0\n"
    "s_waitcnt lgkmcnt(6)\n"
    "ds_read_b64_tr_b16 a[12:13], v9 offset:53248\n"
    "v_mfma_f32_16x16x32_bf16 v[64:67], a[36:39], v[184:187], 0\n"
    "ds_read_b64_tr_b16 a[14:15], v9 offset:53760\n"
    "buffer_load_dwordx4 v[44:47], v18, s[48:51], s86 offen offset:0 sc1\n"
    "v_mfma_f32_16x16x32_bf16 v[68:71], a[36:39], a[40:43], 0\n"
    "v_exp_f32 v12, v112\n"
    "v_mfma_f32_16x16x32_bf16 v[72:75], a[36:39], a[112:115], 0\n"
    "v_exp_f32 v13, v113\n"
    "v_mfma_f32_16x16x32_bf16 v[76:79], a[36:39], a[184:187], 0\n"
    "L_hop7:\n"
    "s_cbranch_execz L_hop11\n"
    "s_waitcnt lgkmcnt(6)\n"
    "ds_read_b64_tr_b16 a[36:37], v8 offset:61440\n"
    "buffer_load_dwordx4 v[176:179], v18, s[48:51], s86 offen offset:1024 sc1\n"
    "v_mfma_f32_16x16x32_bf16 v[80:83], a[0:3], v[184:187], 0\n"
    "ds_read_b64_tr_b16 a[38:39], v8 offset:61952\n"
    "v_mfma_f32_16x16x32_bf16 v[84:87], a[0:3], a[40:43], 0\n"
    "v_exp_f32 v14, v114\n"
    "v_mfma_f32_16x16x32_bf16 v[88:91], a[0:3], a[112:115], 0\n"
    "buffer_load_dwordx4 v[180:183], v19, s[48:51], s86 offen offset:0 sc1\n"
    "v_lshrrev_b32 v18, 4, v0\n"
    "v_bfe_u32 v19, v0, 2, 2\n"
    "v_mfma_f32_16x16x32_bf16 v[92:95], a[0:3], a[184:187], 0\n"
    "s_waitcnt lgkmcnt(6)\n"
    "ds_read_b64_tr_b16 a[0:1], v9 offset:61440\n"
    "v_lshl_add_u32 v19, v18, 3, v19\n"
    "v_mfma_f32_16x16x32_bf16 v[96:99], a[4:7], v[184:187], 0\n"
    "ds_read_b64_tr_b16 a[2:3], v9 offset:61952\n"
    "v_lshlrev_b32 v19, 6, v19\n"
    "v_mfma_f32_16x16x32_bf16 v[100:103], a[4:7], a[40:43], 0\n"
    "v_and_b32 v18, 1, v18\n"
    "v_xor_b32 v18, s25, v18\n"
    "v_mfma_f32_16x16x32_bf16 v[104:107], a[4:7], a[112:115], 0\n"
    "v_lshl_add_u32 v19, v18, 5, v19\n"
    "v_and_b32 v18, 3, v0\n"
    "v_lshl_add_u32 v19, v18, 3, v19\n"
    "v_mfma_f32_16x16x32_bf16 v[108:111], a[4:7], a[184:187], 0\n"
    "s_waitcnt lgkmcnt(6)\n"
    "v_add_u32 v18, 32768, v19\n"
    "v_add_u32 v19, 90112, v19\n"
    "v_mfma_f32_16x16x32_bf16 v[48:51], a[8:11], v[188:191], v[48:51]\n"
    "v_exp_f32 v15, v115\n"
    "v_mfma_f32_16x16x32_bf16 v[52:55], a[8:11], a[44:47], v[52:55]\n"
    "v_mul_f32 v144, v112, v144\n"
    "v_mul_f32 v145, v113, v145\n"
    "v_mfma_f32_16x16x32_bf16 v[56:59], a[8:11], a[116:119], v[56:59]\n"
    "v_mul_f32 v146, v114, v146\n"
    "v_mul_f32 v147, v115, v147\n"
    "v_add_f32 v12, 1.0, v12\n"
    "v_mfma_f32_16x16x32_bf16 v[60:63], a[8:11], a[188:191], v[60:63]\n"
    "s_waitcnt lgkmcnt(4)\n"
    "v_add_f32 v13, 1.0, v13\n"
    "v_add_f32 v14, 1.0, v14\n"
    "v_mfma_f32_16x16x32_bf16 v[64:67], a[12:15], v[188:191], v[64:67]\n"
    "v_add_f32 v15, 1.0, v15\n"
    "v_mfma_f32_16x16x32_bf16 v[68:71], a[12:15], a[44:47], v[68:71]\n"
    "v_rcp_f32 v12, v12\n"
    "v_mfma_f32_16x16x32_bf16 v[72:75], a[12:15], a[116:119], v[72:75]\n"
    "v_rcp_f32 v13, v13\n"
    "v_mfma_f32_16x16x32_bf16 v[76:79], a[12:15], a[188:191], v[76:79]\n"
    "s_waitcnt lgkmcnt(2)\n"
    "v_rcp_f32 v14, v14\n"
    "v_mfma_f32_16x16x32_bf16 v[80:83], a[36:39], v[188:191], v[80:83]\n"
    "v_rcp_f32 v15, v15\n"
    "v_mfma_f32_16x16x32_bf16 v[84:87], a[36:39], a[44:47], v[84:87]\n"
    "v_mul_f32 v144, v144, v12\n"
    "v_mul_f32 v145, v145, v13\n"
    "v_mfma_f32_16x16x32_bf16 v[88:91], a[36:39], a[116:119], v[88:91]\n"
    "v_mul_f32 v146, v146, v14\n"
    "v_mul_f32 v147, v147, v15\n"
    "v_mfma_f32_16x16x32_bf16 v[92:95], a[36:39], a[188:191], v[92:95]\n"
    "s_waitcnt lgkmcnt(0)\n"
    "s_barrier\n"
    "ds_read_b64_tr_b16 a[4:5], v6 offset:16384\n"
    "ds_read_b64_tr_b16 a[6:7], v6 offset:16896\n"
    "v_mfma_f32_16x16x32_bf16 v[96:99], a[0:3], v[188:191], v[96:99]\n"
    "s_add_u32 m0, s56, 65536\n"
    "ds_read_b64_tr_b16 a[8:9], v7 offset:16384\n"
    "ds_read_b64_tr_b16 a[10:11], v7 offset:16896\n"
    "v_mfma_f32_16x16x32_bf16 v[100:103], a[0:3], a[44:47], v[100:103]\n"
    "s_add_u32 s58, s61, 3539328\n"
    "ds_read_b64_tr_b16 a[12:13], v6 offset:24576\n"
    "v_cvt_pk_bf16_f32 v112, v144, v145\n"
    "v_mfma_f32_16x16x32_bf16 v[104:107], a[0:3], a[116:119], v[104:107]\n"
    "ds_read_b64_tr_b16 a[14:15], v6 offset:25088\n"
    "buffer_load_dwordx4 v2, s[36:39], s58 offen offset:0 lds\n"
    "v_mfma_f32_16x16x32_bf16 v[108:111], a[0:3], a[188:191], v[108:111]\n"
    "s_waitcnt lgkmcnt(4)\n"
    "ds_read_b64_tr_b16 a[36:37], v7 offset:24576\n"
    "v_cvt_pk_bf16_f32 v113, v146, v147\n"
    "v_mfma_f32_16x16x32_bf16 v[48:51], a[4:7], v[192:195], v[48:51]\n"
    "ds_read_b64_tr_b16 a[38:39], v7 offset:25088\n"
    "v_mfma_f32_16x16x32_bf16 v[52:55], a[4:7], a[48:51], v[52:55]\n"
    "ds_read_b64_tr_b16 a[0:1], v6 offset:20480\n"
    "v_mfma_f32_16x16x32_bf16 v[56:59], a[4:7], a[120:123], v[56:59]\n"
    "ds_read_b64_tr_b16 a[2:3], v6 offset:20992\n"
    "v_mfma_f32_16x16x32_bf16 v[60:63], a[4:7], a[192:195], v[60:63]\n"
    "s_waitcnt lgkmcnt(6)\n"
    "ds_read_b64_tr_b16 a[4:5], v7 offset:20480\n"
    "buffer_load_dwordx4 v3, s[36:39], s58 offen offset:1024 lds\n"
    "v_mfma_f32_16x16x32_bf16 v[64:67], a[8:11], v[192:195], v[64:67]\n"
    "ds_read_b64_tr_b16 a[6:7], v7 offset:20992\n"
    "v_mfma_f32_16x16x32_bf16 v[68:71], a[8:11], a[48:51], v[68:71]\n"
    "v_exp_f32 v12, v128\n"
    "v_mfma_f32_16x16x32_bf16 v[72:75], a[8:11], a[120:123], v[72:75]\n"
    "v_exp_f32 v13, v129\n"
    "v_mfma_f32_16x16x32_bf16 v[76:79], a[8:11], a[192:195], v[76:79]\n"
    "s_waitcnt lgkmcnt(6)\n"
    "ds_read_b64_tr_b16 a[8:9], v6 offset:28672\n"
    "v_exp_f32 v14, v130\n"
    "v_mfma_f32_16x16x32_bf16 v[80:83], a[12:15], v[192:195], v[80:83]\n"
    "ds_read_b64_tr_b16 a[10:11], v6 offset:29184\n"
    "buffer_load_dwordx4 v4, s[36:39], s58 offen offset:2048 lds\n"
    "v_mfma_f32_16x16x32_bf16 v[84:87], a[12:15], a[48:51], v[84:87]\n"
    "v_exp_f32 v15, v131\n"
    "v_mfma_f32_16x16x32_bf16 v[88:91], a[12:15], a[120:123], v[88:91]\n"
    "v_mul_f32 v160, v128, v160\n"
    "v_mul_f32 v161, v129, v161\n"
    "v_mfma_f32_16x16x32_bf16 v[92:95], a[12:15], a[192:195], v[92:95]\n"
    "s_waitcnt lgkmcnt(6)\n"
    "ds_read_b64_tr_b16 a[12:13], v7 offset:28672\n"
    "v_mul_f32 v162, v130, v162\n"
    "v_mul_f32 v163, v131, v163\n"
    "v_mfma_f32_16x16x32_bf16 v[96:99], a[36:39], v[192:195], v[96:99]\n"
    "ds_read_b64_tr_b16 a[14:15], v7 offset:29184\n"
    "v_add_f32 v12, 1.0, v12\n"
    "v_mfma_f32_16x16x32_bf16 v[100:103], a[36:39], a[48:51], v[100:103]\n"
    "buffer_load_dwordx4 v5, s[36:39], s58 offen offset:3072 lds\n"
    "v_add_f32 v13, 1.0, v13\n"
    "v_mfma_f32_16x16x32_bf16 v[104:107], a[36:39], a[120:123], v[104:107]\n"
    "v_add_f32 v14, 1.0, v14\n"
    "v_add_f32 v15, 1.0, v15\n"
    "v_mfma_f32_16x16x32_bf16 v[108:111], a[36:39], a[192:195], v[108:111]\n"
    "s_waitcnt lgkmcnt(6)\n"
    "v_rcp_f32 v12, v12\n"
    "v_mfma_f32_16x16x32_bf16 v[48:51], a[0:3], v[196:199], v[48:51]\n"
    "v_rcp_f32 v13, v13\n"
    "v_mfma_f32_16x16x32_bf16 v[52:55], a[0:3], a[52:55], v[52:55]\n"
    "v_rcp_f32 v14, v14\n"
    "v_mfma_f32_16x16x32_bf16 v[56:59], a[0:3], a[124:127], v[56:59]\n"
    "v_rcp_f32 v15, v15\n"
    "v_mul_f32 v160, v160, v12\n"
    "v_mfma_f32_16x16x32_bf16 v[60:63], a[0:3], a[196:199], v[60:63]\n"
    "s_waitcnt lgkmcnt(4)\n"
    "v_mul_f32 v161, v161, v13\n"
    "v_mul_f32 v162, v162, v14\n"
    "v_mfma_f32_16x16x32_bf16 v[64:67], a[4:7], v[196:199], v[64:67]\n"
    "v_mul_f32 v163, v163, v15\n"
    "v_cvt_pk_bf16_f32 v114, v160, v161\n"
    "v_mfma_f32_16x16x32_bf16 v[68:71], a[4:7], a[52:55], v[68:71]\n"
    "v_cvt_pk_bf16_f32 v115, v162, v163\n"
    "v_mfma_f32_16x16x32_bf16 v[72:75], a[4:7], a[124:127], v[72:75]\n"
    "v_exp_f32 v12, v116\n"
    "v_mfma_f32_16x16x32_bf16 v[76:79], a[4:7], a[196:199], v[76:79]\n"
    "s_waitcnt lgkmcnt(2)\n"
    "v_exp_f32 v13, v117\n"
    "v_mfma_f32_16x16x32_bf16 v[80:83], a[8:11], v[196:199], v[80:83]\n"
    "v_exp_f32 v14, v118\n"
    "v_mfma_f32_16x16x32_bf16 v[84:87], a[8:11], a[52:55], v[84:87]\n"
    "v_exp_f32 v15, v119\n"
    "v_mfma_f32_16x16x32_bf16 v[88:91], a[8:11], a[124:127], v[88:91]\n"
    "v_mul_f32 v148, v116, v148\n"
    "v_mul_f32 v149, v117, v149\n"
    "v_mul_f32 v150, v118, v150\n"
    "v_mfma_f32_16x16x32_bf16 v[92:95], a[8:11], a[196:199], v[92:95]\n"
    "s_waitcnt lgkmcnt(0)\n"
    "s_barrier\n"
    "ds_read_b64_tr_b16 a[36:37], v6 offset:40960\n"
    "ds_read_b64_tr_b16 a[38:39], v6 offset:41472\n"
    "v_mfma_f32_16x16x32_bf16 v[96:99], a[12:15], v[196:199], v[96:99]\n"
    "s_add_u32 m0, s56, 4294942720\n"
    "ds_read_b64_tr_b16 a[0:1], v7 offset:40960\n"
    "ds_read_b64_tr_b16 a[2:3], v7 offset:41472\n"
    "v_mfma_f32_16x16x32_bf16 v[100:103], a[12:15], a[52:55], v[100:103]\n"
    "s_add_u32 s58, s61, 4129152\n"
    "ds_read_b64_tr_b16 a[4:5], v6 offset:49152\n"
    "ds_write_b128 v10, v[112:115] offset:32768\n"
    "v_mfma_f32_16x16x32_bf16 v[104:107], a[12:15], a[124:127], v[104:107]\n"
    "ds_read_b64_tr_b16 a[6:7], v6 offset:49664\n"
    "buffer_load_dwordx4 v2, s[36:39], s58 offen offset:0 lds\n"
    "v_mfma_f32_16x16x32_bf16 v[108:111], a[12:15], a[196:199], v[108:111]\n"
    "s_waitcnt lgkmcnt(5)\n"
    "ds_read_b64_tr_b16 a[8:9], v7 offset:49152\n"
    "buffer_store_dwordx4 v[112:115], v10, s[48:51], s60 offen offset:0 sc1\n"
    "v_mfma_f32_16x16x32_bf16 v[48:51], a[36:39], v[200:203], v[48:51]\n"
    "ds_read_b64_tr_b16 a[10:11], v7 offset:49664\n"
    "v_mul_f32 v151, v119, v151\n"
    "v_mfma_f32_16x16x32_bf16 v[52:55], a[36:39], a[56:59], v[52:55]\n"
    "ds_read_b64_tr_b16 a[12:13], v6 offset:45056\n"
    "v_add_f32 v12, 1.0, v12\n"
    "v_add_f32 v13, 1.0, v13\n"
    "v_mfma_f32_16x16x32_bf16 v[56:59], a[36:39], a[128:131], v[56:59]\n"
    "ds_read_b64_tr_b16 a[14:15], v6 offset:45568\n"
    "v_add_f32 v14, 1.0, v14\n"
    "v_mfma_f32_16x16x32_bf16 v[60:63], a[36:39], a[200:203], v[60:63]\n"
    "s_waitcnt lgkmcnt(7)\n"
    "ds_read_b64_tr_b16 a[36:37], v7 offset:45056\n"
    "buffer_load_dwordx4 v3, s[36:39], s58 offen offset:1024 lds\n"
    "v_mfma_f32_16x16x32_bf16 v[64:67], a[0:3], v[200:203], v[64:67]\n"
    "ds_read_b64_tr_b16 a[38:39], v7 offset:45568\n"
    "v_add_f32 v15, 1.0, v15\n"
    "v_mfma_f32_16x16x32_bf16 v[68:71], a[0:3], a[56:59], v[68:71]\n"
    "v_rcp_f32 v12, v12\n"
    "v_mfma_f32_16x16x32_bf16 v[72:75], a[0:3], a[128:131], v[72:75]\n"
    "v_rcp_f32 v13, v13\n"
    "v_mfma_f32_16x16x32_bf16 v[76:79], a[0:3], a[200:203], v[76:79]\n"
    "s_waitcnt lgkmcnt(6)\n"
    "ds_read_b64_tr_b16 a[0:1], v6 offset:53248\n"
    "v_mfma_f32_16x16x32_bf16 v[80:83], a[4:7], v[200:203], v[80:83]\n"
    "ds_read_b64_tr_b16 a[2:3], v6 offset:53760\n"
    "buffer_load_dwordx4 v4, s[36:39], s58 offen offset:2048 lds\n"
    "v_mfma_f32_16x16x32_bf16 v[84:87], a[4:7], a[56:59], v[84:87]\n"
    "v_rcp_f32 v14, v14\n"
    "v_mfma_f32_16x16x32_bf16 v[88:91], a[4:7], a[128:131], v[88:91]\n"
    "v_rcp_f32 v15, v15\n"
    "v_mfma_f32_16x16x32_bf16 v[92:95], a[4:7], a[200:203], v[92:95]\n"
    "s_waitcnt lgkmcnt(6)\n"
    "ds_read_b64_tr_b16 a[4:5], v7 offset:53248\n"
    "v_mul_f32 v148, v148, v12\n"
    "v_mfma_f32_16x16x32_bf16 v[96:99], a[8:11], v[200:203], v[96:99]\n"
    "ds_read_b64_tr_b16 a[6:7], v7 offset:53760\n"
    "v_mul_f32 v149, v149, v13\n"
    "v_mul_f32 v150, v150, v14\n"
    "v_mfma_f32_16x16x32_bf16 v[100:103], a[8:11], a[56:59], v[100:103]\n"
    "s_and_b32 s59, s23, 15\n"
    "buffer_load_dwordx4 v5, s[36:39], s58 offen offset:3072 lds\n"
    "s_lshl_b32 s59, s59, 1\n"
    "v_mfma_f32_16x16x32_bf16 v[104:107], a[8:11], a[128:131], v[104:107]\n"
    "s_and_b32 s58, s22, 1\n"
    "s_add_u32 s59, s59, s58\n"
    "s_lshl_b32 s59, s59, 2\n"
    "v_mfma_f32_16x16x32_bf16 v[108:111], a[8:11], a[200:203], v[108:111]\n"
    "s_waitcnt lgkmcnt(6)\n"
    "s_add_u32 s59, s59, s21\n"
    "s_mul_i32 s59, s59, 768\n"
    "s_lshr_b32 s58, s22, 1\n"
    "v_mfma_f32_16x16x32_bf16 v[48:51], a[12:15], v[204:207], v[48:51]\n"
    "s_mul_i32 s58, s58, 82944\n"
    "s_add_u32 s59, s59, s58\n"
    "s_add_u32 s59, s59, 3072\n"
    "v_mfma_f32_16x16x32_bf16 v[52:55], a[12:15], a[60:63], v[52:55]\n"
    "v_min_u32 v1, 5, v0\n"
    "v_lshlrev_b32 v1, 7, v1\n"
    "v_mfma_f32_16x16x32_bf16 v[56:59], a[12:15], a[132:135], v[56:59]\n"
    "s_waitcnt vmcnt(20)\n"
    "buffer_load_dword a34, v1, s[44:47], s59 offen offset:0\n"
    "v_mul_f32 v151, v151, v15\n"
    "v_mfma_f32_16x16x32_bf16 v[60:63], a[12:15], a[204:207], v[60:63]\n"
    "s_waitcnt lgkmcnt(4)\n"
    "v_cvt_pk_bf16_f32 v116, v148, v149\n"
    "v_cvt_pk_bf16_f32 v117, v150, v151\n"
    "v_mfma_f32_16x16x32_bf16 v[64:67], a[36:39], v[204:207], v[64:67]\n"
    "v_exp_f32 v12, v132\n"
    "v_mfma_f32_16x16x32_bf16 v[68:71], a[36:39], a[60:63], v[68:71]\n"
    "v_exp_f32 v13, v133\n"
    "v_mfma_f32_16x16x32_bf16 v[72:75], a[36:39], a[132:135], v[72:75]\n"
    "v_exp_f32 v14, v134\n"
    "v_mfma_f32_16x16x32_bf16 v[76:79], a[36:39], a[204:207], v[76:79]\n"
    "s_waitcnt lgkmcnt(2)\n"
    "v_exp_f32 v15, v135\n"
    "v_mfma_f32_16x16x32_bf16 v[80:83], a[0:3], v[204:207], v[80:83]\n"
    "v_mul_f32 v164, v132, v164\n"
    "v_mul_f32 v165, v133, v165\n"
    "v_mfma_f32_16x16x32_bf16 v[84:87], a[0:3], a[60:63], v[84:87]\n"
    "v_mul_f32 v166, v134, v166\n"
    "v_mul_f32 v167, v135, v167\n"
    "v_add_f32 v12, 1.0, v12\n"
    "v_mfma_f32_16x16x32_bf16 v[88:91], a[0:3], a[132:135], v[88:91]\n"
    "v_add_f32 v13, 1.0, v13\n"
    "v_add_f32 v14, 1.0, v14\n"
    "v_mfma_f32_16x16x32_bf16 v[92:95], a[0:3], a[204:207], v[92:95]\n"
    "s_waitcnt lgkmcnt(0)\n"
    "s_barrier\n"
    "ds_read_b64_tr_b16 a[8:9], v8 offset:0\n"
    "ds_read_b64_tr_b16 a[10:11], v8 offset:512\n"
    "v_mfma_f32_16x16x32_bf16 v[96:99], a[4:7], v[204:207], v[96:99]\n"
    "s_add_u32 m0, s56, 0\n"
    "ds_read_b64_tr_b16 a[12:13], v9 offset:0\n"
    "ds_read_b64_tr_b16 a[14:15], v9 offset:512\n"
    "s_add_u32 s58, s61, 4718976\n"
    "v_mfma_f32_16x16x32_bf16 v[100:103], a[4:7], a[60:63], v[100:103]\n"
    "ds_read_b64_tr_b16 a[36:37], v8 offset:8192\n"
    "v_add_f32 v15, 1.0, v15\n"
    "v_mfma_f32_16x16x32_bf16 v[104:107], a[4:7], a[132:135], v[104:107]\n"
    "ds_read_b64_tr_b16 a[38:39], v8 offset:8704\n"
    "buffer_load_dwordx4 v2, s[36:39], s58 offen offset:0 lds\n"
    "v_mfma_f32_16x16x32_bf16 v[108:111], a[4:7], a[204:207], v[108:111]\n"
    "s_waitcnt lgkmcnt(4)\n"
    "ds_read_b64_tr_b16 a[0:1], v9 offset:8192\n"
    "v_mfma_f32_16x16x32_bf16 v[48:51], a[8:11], v[208:211], v[48:51]\n"
    "ds_read_b64_tr_b16 a[2:3], v9 offset:8704\n"
    "v_rcp_f32 v12, v12\n"
    "v_mfma_f32_16x16x32_bf16 v[52:55], a[8:11], a[64:67], v[52:55]\n"
    "ds_read_b64_tr_b16 a[4:5], v8 offset:4096\n"
    "v_mfma_f32_16x16x32_bf16 v[56:59], a[8:11], a[136:139], v[56:59]\n"
    "ds_read_b64_tr_b16 a[6:7], v8 offset:4608\n"
    "v_mfma_f32_16x16x32_bf16 v[60:63], a[8:11], a[208:211], v[60:63]\n"
    "s_waitcnt lgkmcnt(6)\n"
    "ds_read_b64_tr_b16 a[8:9], v9 offset:4096\n"
    "buffer_load_dwordx4 v3, s[36:39], s58 offen offset:1024 lds\n"
    "v_mfma_f32_16x16x32_bf16 v[64:67], a[12:15], v[208:211], v[64:67]\n"
    "ds_read_b64_tr_b16 a[10:11], v9 offset:4608\n"
    "v_mfma_f32_16x16x32_bf16 v[68:71], a[12:15], a[64:67], v[68:71]\n"
    "v_rcp_f32 v13, v13\n"
    "v_mfma_f32_16x16x32_bf16 v[72:75], a[12:15], a[136:139], v[72:75]\n"
    "v_rcp_f32 v14, v14\n"
    "v_mfma_f32_16x16x32_bf16 v[76:79], a[12:15], a[208:211], v[76:79]\n"
    "s_waitcnt lgkmcnt(6)\n"
    "ds_read_b64_tr_b16 a[12:13], v8 offset:12288\n"
    "v_mfma_f32_16x16x32_bf16 v[80:83], a[36:39], v[208:211], v[80:83]\n"
    "ds_read_b64_tr_b16 a[14:15], v8 offset:12800\n"
    "buffer_load_dwordx4 v4, s[36:39], s58 offen offset:2048 lds\n"
    "v_mfma_f32_16x16x32_bf16 v[84:87], a[36:39], a[64:67], v[84:87]\n"
    "v_rcp_f32 v15, v15\n"
    "v_mul_f32 v164, v164, v12\n"
    "v_mfma_f32_16x16x32_bf16 v[88:91], a[36:39], a[136:139], v[88:91]\n"
    "v_mul_f32 v165, v165, v13\n"
    "v_mul_f32 v166, v166, v14\n"
    "v_mfma_f32_16x16x32_bf16 v[92:95], a[36:39], a[208:211], v[92:95]\n"
    "ds_read_b64_tr_b16 a[36:37], v9 offset:12288\n"
    "s_waitcnt vmcnt(18)\n"
    "v_add_f32 v20, v20, v32\n"
    "s_waitcnt lgkmcnt(7)\n"
    "v_mfma_f32_16x16x32_bf16 v[96:99], a[0:3], v[208:211], v[96:99]\n"
    "ds_read_b64_tr_b16 a[38:39], v9 offset:12800\n"
    "v_add_f32 v21, v21, v33\n"
    "v_add_f32 v22, v22, v34\n"
    "v_mfma_f32_16x16x32_bf16 v[100:103], a[0:3], a[64:67], v[100:103]\n"
    "buffer_load_dwordx4 v5, s[36:39], s58 offen offset:3072 lds\n"
    "v_add_f32 v23, v23, v35\n"
    "v_mfma_f32_16x16x32_bf16 v[104:107], a[0:3], a[136:139], v[104:107]\n"
    "s_waitcnt vmcnt(18)\n"
    "v_add_f32 v24, v24, v36\n"
    "v_add_f32 v25, v25, v37\n"
    "v_mfma_f32_16x16x32_bf16 v[108:111], a[0:3], a[208:211], v[108:111]\n"
    "v_add_f32 v26, v26, v38\n"
    "v_add_f32 v27, v27, v39\n"
    "s_waitcnt vmcnt(17)\n"
    "v_add_f32 v28, v28, v40\n"
    "s_waitcnt lgkmcnt(6)\n"
    "v_mfma_f32_16x16x32_bf16 v[48:51], a[4:7], v[212:215], v[48:51]\n"
    "v_add_f32 v29, v29, v41\n"
    "v_add_f32 v30, v30, v42\n"
    "v_mfma_f32_16x16x32_bf16 v[52:55], a[4:7], a[68:71], v[52:55]\n"
    "v_lshrrev_b32 v1, 2, v0\n"
    "v_mul_u32_u24 v5, 9216, v1\n"
    "v_lshrrev_b32 v1, 5, v0\n"
    "v_mfma_f32_16x16x32_bf16 v[56:59], a[4:7], a[140:143], v[56:59]\n"
    "v_lshlrev_b32 v1, 1, v1\n"
    "v_and_b32 v4, 3, v0\n"
    "v_mfma_f32_16x16x32_bf16 v[60:63], a[4:7], a[212:215], v[60:63]\n"
    "s_waitcnt lgkmcnt(4)\n"
    "v_xor_b32 v1, v1, v4\n"
    "v_lshl_add_u32 v2, v1, 4, v5\n"
    "v_mfma_f32_16x16x32_bf16 v[64:67], a[8:11], v[212:215], v[64:67]\n"
    "v_add_u32 v3, 146432, v2\n"
    "v_add_u32 v4, 292864, v2\n"
    "v_add_u32 v5, 439296, v2\n"
    "v_mfma_f32_16x16x32_bf16 v[68:71], a[8:11], a[68:71], v[68:71]\n"
    "v_add_f32 v31, v31, v43\n"
    "s_waitcnt vmcnt(16)\n"
    "v_add_f32 v20, v20, v44\n"
    "v_mfma_f32_16x16x32_bf16 v[72:75], a[8:11], a[140:143], v[72:75]\n"
    "v_add_f32 v21, v21, v45\n"
    "v_add_f32 v22, v22, v46\n"
    "v_mfma_f32_16x16x32_bf16 v[76:79], a[8:11], a[212:215], v[76:79]\n"
    "v_add_f32 v23, v23, v47\n"
    "s_waitcnt vmcnt(15)\n"
    "v_add_f32 v24, v24, v176\n"
    "v_add_f32 v25, v25, v177\n"
    "s_waitcnt lgkmcnt(2)\n"
    "v_mfma_f32_16x16x32_bf16 v[80:83], a[12:15], v[212:215], v[80:83]\n"
    "v_add_f32 v26, v26, v178\n"
    "v_add_f32 v27, v27, v179\n"
    "v_mfma_f32_16x16x32_bf16 v[84:87], a[12:15], a[68:71], v[84:87]\n"
    "s_waitcnt vmcnt(14)\n"
    "v_add_f32 v28, v28, v180\n"
    "v_add_f32 v29, v29, v181\n"
    "v_mfma_f32_16x16x32_bf16 v[88:91], a[12:15], a[140:143], v[88:91]\n"
    "s_lshl_b32 s86, s21, 11\n"
    "v_add_f32 v30, v30, v182\n"
    "v_add_f32 v31, v31, v183\n"
    "s_add_u32 s86, s86, 65536\n"
    "v_mfma_f32_16x16x32_bf16 v[92:95], a[12:15], a[212:215], v[92:95]\n"
    "s_waitcnt lgkmcnt(0)\n"
    "s_barrier\n"
    "ds_read_b64_tr_b16 a[0:1], v8 offset:16384\n"
    "ds_read_b64_tr_b16 a[2:3], v8 offset:16896\n"
    "v_mfma_f32_16x16x32_bf16 v[96:99], a[36:39], v[212:215], v[96:99]\n"
    "s_add_u32 m0, s56, 16384\n"
    "ds_read_b64_tr_b16 a[4:5], v9 offset:16384\n"
    "ds_read_b64_tr_b16 a[6:7], v9 offset:16896\n"
    "v_mfma_f32_16x16x32_bf16 v[100:103], a[36:39], a[68:71], v[100:103]\n"
    "s_add_u32 s58, s62, 0\n"
    "ds_read_b64_tr_b16 a[8:9], v8 offset:24576\n"
    "s_xor_b32 s87, s86, 4096\n"
    "v_lshl_add_u32 v32, v0, 4, s86\n"
    "v_mfma_f32_16x16x32_bf16 v[104:107], a[36:39], a[140:143], v[104:107]\n"
    "ds_read_b64_tr_b16 a[10:11], v8 offset:25088\n"
    "buffer_load_dwordx4 v2, s[36:39], s58 offen offset:0 lds\n"
    "v_mfma_f32_16x16x32_bf16 v[108:111], a[36:39], a[212:215], v[108:111]\n"
    "s_waitcnt lgkmcnt(4)\n"
    "ds_read_b64_tr_b16 a[12:13], v9 offset:24576\n"
    "v_lshl_add_u32 v33, v0, 4, s87\n"
    "v_mfma_f32_16x16x32_bf16 v[48:51], a[0:3], v[216:219], v[48:51]\n"
    "s_lshl_b32 s86, s21, 10\n"
    "ds_read_b64_tr_b16 a[14:15], v9 offset:25088\n"
    "s_xor_b32 s87, s86, 2048\n"
    "s_add_u32 s86, s86, 155648\n"
    "v_mfma_f32_16x16x32_bf16 v[52:55], a[0:3], a[72:75], v[52:55]\n"
    "s_add_u32 s87, s87, 155648\n"
    "ds_read_b64_tr_b16 a[36:37], v8 offset:20480\n"
    "v_lshl_add_u32 v34, v0, 4, s86\n"
    "v_mfma_f32_16x16x32_bf16 v[56:59], a[0:3], a[144:147], v[56:59]\n"
    "ds_read_b64_tr_b16 a[38:39], v8 offset:20992\n"
    "v_lshl_add_u32 v35, v0, 4, s87\n"
    "v_mfma_f32_16x16x32_bf16 v[60:63], a[0:3], a[216:219], v[60:63]\n"
    "s_waitcnt lgkmcnt(6)\n"
    "ds_read_b64_tr_b16 a[0:1], v9 offset:20480\n"
    "buffer_load_dwordx4 v3, s[36:39], s58 offen offset:1024 lds\n"
    "v_mul_f32 v167, v167, v15\n"
    "v_mfma_f32_16x16x32_bf16 v[64:67], a[4:7], v[216:219], v[64:67]\n"
    "ds_read_b64_tr_b16 a[2:3], v9 offset:20992\n"
    "v_cvt_pk_bf16_f32 v118, v164, v165\n"
    "v_mfma_f32_16x16x32_bf16 v[68:71], a[4:7], a[72:75], v[68:71]\n"
    "ds_write_b128 v32, v[20:23] offset:0\n"
    "v_mfma_f32_16x16x32_bf16 v[72:75], a[4:7], a[144:147], v[72:75]\n"
    "ds_write_b128 v32, v[24:27] offset:1024\n"
    "v_cvt_pk_bf16_f32 v119, v166, v167\n"
    "v_mfma_f32_16x16x32_bf16 v[76:79], a[4:7], a[216:219], v[76:79]\n"
    "s_waitcnt lgkmcnt(8)\n"
    "ds_read_b64_tr_b16 a[4:5], v8 offset:28672\n"
    "v_mfma_f32_16x16x32_bf16 v[80:83], a[8:11], v[216:219], v[80:83]\n"
    "ds_read_b64_tr_b16 a[6:7], v8 offset:29184\n"
    "buffer_load_dwordx4 v4, s[36:39], s58 offen offset:2048 lds\n"
    "v_mfma_f32_16x16x32_bf16 v[84:87], a[8:11], a[72:75], v[84:87]\n"
    "ds_write_b128 v10, v[116:119] offset:33792\n"
    "v_mfma_f32_16x16x32_bf16 v[88:91], a[8:11], a[144:147], v[88:91]\n"
    "ds_write_b128 v34, v[28:31] offset:0\n"
    "v_mfma_f32_16x16x32_bf16 v[92:95], a[8:11], a[216:219], v[92:95]\n"
    "s_waitcnt lgkmcnt(10)\n"
    "ds_read_b64_tr_b16 a[8:9], v9 offset:28672\n"
    "v_exp_f32 v12, v120\n"
    "v_mfma_f32_16x16x32_bf16 v[96:99], a[12:15], v[216:219], v[96:99]\n"
    "ds_read_b64_tr_b16 a[10:11], v9 offset:29184\n"
    "v_mfma_f32_16x16x32_bf16 v[100:103], a[12:15], a[72:75], v[100:103]\n"
    "buffer_load_dwordx4 v5, s[36:39], s58 offen offset:3072 lds\n"
    "v_mfma_f32_16x16x32_bf16 v[104:107], a[12:15], a[144:147], v[104:107]\n"
    "v_exp_f32 v13, v121\n"
    "v_mfma_f32_16x16x32_bf16 v[108:111], a[12:15], a[216:219], v[108:111]\n"
    "s_waitcnt lgkmcnt(10)\n"
    "v_exp_f32 v14, v122\n"
    "v_mfma_f32_16x16x32_bf16 v[48:51], a[36:39], v[220:223], v[48:51]\n"
    "v_exp_f32 v15, v123\n"
    "v_mfma_f32_16x16x32_bf16 v[52:55], a[36:39], a[76:79], v[52:55]\n"
    "v_mul_f32 v152, v120, v152\n"
    "v_mul_f32 v153, v121, v153\n"
    "v_mul_f32 v154, v122, v154\n"
    "v_mfma_f32_16x16x32_bf16 v[56:59], a[36:39], a[148:151], v[56:59]\n"
    "v_mul_f32 v155, v123, v155\n"
    "v_add_f32 v12, 1.0, v12\n"
    "v_mfma_f32_16x16x32_bf16 v[60:63], a[36:39], a[220:223], v[60:63]\n"
    "s_waitcnt lgkmcnt(8)\n"
    "buffer_store_dwordx4 v[116:119], v10, s[48:51], s60 offen offset:1024 sc1\n"
    "v_add_f32 v13, 1.0, v13\n"
    "v_add_f32 v14, 1.0, v14\n"
    "v_mfma_f32_16x16x32_bf16 v[64:67], a[0:3], v[220:223], v[64:67]\n"
    "v_add_f32 v15, 1.0, v15\n"
    "v_mfma_f32_16x16x32_bf16 v[68:71], a[0:3], a[76:79], v[68:71]\n"
    "v_rcp_f32 v12, v12\n"
    "v_mfma_f32_16x16x32_bf16 v[72:75], a[0:3], a[148:151], v[72:75]\n"
    "v_rcp_f32 v13, v13\n"
    "v_mfma_f32_16x16x32_bf16 v[76:79], a[0:3], a[220:223], v[76:79]\n"
    "s_waitcnt lgkmcnt(4)\n"
    "v_rcp_f32 v14, v14\n"
    "v_mfma_f32_16x16x32_bf16 v[80:83], a[4:7], v[220:223], v[80:83]\n"
    "v_rcp_f32 v15, v15\n"
    "v_mul_f32 v152, v152, v12\n"
    "v_mfma_f32_16x16x32_bf16 v[84:87], a[4:7], a[76:79], v[84:87]\n"
    "v_mul_f32 v153, v153, v13\n"
    "v_mul_f32 v154, v154, v14\n"
    "v_mfma_f32_16x16x32_bf16 v[88:91], a[4:7], a[148:151], v[88:91]\n"
    "v_mul_f32 v155, v155, v15\n"
    "v_cvt_pk_bf16_f32 v120, v152, v153\n"
    "v_mfma_f32_16x16x32_bf16 v[92:95], a[4:7], a[220:223], v[92:95]\n"
    "s_waitcnt lgkmcnt(0)\n"
    "s_barrier\n"
    "ds_read_b64_tr_b16 a[12:13], v8 offset:32768\n"
    "ds_read_b64_tr_b16 a[14:15], v8 offset:33280\n"
    "v_mfma_f32_16x16x32_bf16 v[96:99], a[8:11], v[220:223], v[96:99]\n"
    "s_add_u32 m0, s56, 32768\n"
    "ds_read_b64_tr_b16 a[36:37], v9 offset:32768\n"
    "ds_read_b64_tr_b16 a[38:39], v9 offset:33280\n"
    "v_mfma_f32_16x16x32_bf16 v[100:103], a[8:11], a[76:79], v[100:103]\n"
    "s_add_u32 s58, s62, 1179648\n"
    "ds_read_b64_tr_b16 a[0:1], v8 offset:40960\n"
    "ds_read_b128 v[44:47], v33 offset:0\n"
    "ds_read_b128 v[176:179], v33 offset:1024\n"
    "v_mfma_f32_16x16x32_bf16 v[104:107], a[8:11], a[148:151], v[104:107]\n"
    "ds_read_b64_tr_b16 a[2:3], v8 offset:41472\n"
    "buffer_load_dwordx4 v2, s[36:39], s58 offen offset:0 lds\n"
    "v_mfma_f32_16x16x32_bf16 v[108:111], a[8:11], a[220:223], v[108:111]\n"
    "s_waitcnt lgkmcnt(6)\n"
    "ds_read_b64_tr_b16 a[4:5], v9 offset:40960\n"
    "ds_read_b128 v[180:183], v35 offset:0\n"
    "v_mfma_f32_16x16x32_bf16 v[48:51], a[12:15], v[224:227], v[48:51]\n"
    "s_lshl_b32 s86, s24, 12\n"
    "ds_read_b64_tr_b16 a[6:7], v9 offset:41472\n"
    "s_lshl_b32 s87, s25, 11\n"
    "s_add_u32 s86, s86, s87\n"
    "v_mfma_f32_16x16x32_bf16 v[52:55], a[12:15], a[80:83], v[52:55]\n"
    "s_xor_b32 s87, s86, 2048\n"
    "ds_read_b64_tr_b16 a[8:9], v8 offset:36864\n"
    "v_lshl_add_u32 v40, v0, 4, s86\n"
    "v_mfma_f32_16x16x32_bf16 v[56:59], a[12:15], a[152:155], v[56:59]\n"
    "ds_read_b64_tr_b16 a[10:11], v8 offset:37376\n"
    "v_lshl_add_u32 v41, v0, 4, s87\n"
    "v_add_u32 v42, 65536, v40\n"
    "v_mfma_f32_16x16x32_bf16 v[60:63], a[12:15], a[224:227], v[60:63]\n"
    "s_waitcnt lgkmcnt(9)\n"
    "ds_read_b64_tr_b16 a[12:13], v9 offset:36864\n"
    "buffer_load_dwordx4 v3, s[36:39], s58 offen offset:1024 lds\n"
    "v_mfma_f32_16x16x32_bf16 v[64:67], a[36:39], v[224:227], v[64:67]\n"
    "ds_read_b64_tr_b16 a[14:15], v9 offset:37376\n"
    "v_add_u32 v43, 65536, v41\n"
    "v_mfma_f32_16x16x32_bf16 v[68:71], a[36:39], a[80:83], v[68:71]\n"
    "s_waitcnt lgkmcnt(9)\n"
    "v_add_f32 v20, v20, v44\n"
    "v_add_f32 v21, v21, v45\n"
    "v_add_f32 v22, v22, v46\n"
    "v_mfma_f32_16x16x32_bf16 v[72:75], a[36:39], a[152:155], v[72:75]\n"
    "v_add_f32 v23, v23, v47\n"
    "s_waitcnt lgkmcnt(8)\n"
    "v_add_f32 v24, v24, v176\n"
    "v_mfma_f32_16x16x32_bf16 v[76:79], a[36:39], a[224:227], v[76:79]\n"
    "s_waitcnt lgkmcnt(7)\n"
    "ds_read_b64_tr_b16 a[36:37], v8 offset:45056\n"
    "v_add_f32 v25, v25, v177\n"
    "v_add_f32 v26, v26, v178\n"
    "v_mfma_f32_16x16x32_bf16 v[80:83], a[0:3], v[224:227], v[80:83]\n"
    "ds_read_b64_tr_b16 a[38:39], v8 offset:45568\n"
    "buffer_load_dwordx4 v4, s[36:39], s58 offen offset:2048 lds\n"
    "v_mfma_f32_16x16x32_bf16 v[84:87], a[0:3], a[80:83], v[84:87]\n"
    "v_add_f32 v27, v27, v179\n"
    "s_waitcnt lgkmcnt(7)\n"
    "v_add_f32 v28, v28, v180\n"
    "v_mfma_f32_16x16x32_bf16 v[88:91], a[0:3], a[152:155], v[88:91]\n"
    "v_add_f32 v29, v29, v181\n"
    "v_add_f32 v30, v30, v182\n"
    "v_add_f32 v31, v31, v183\n"
    "v_mfma_f32_16x16x32_bf16 v[92:95], a[0:3], a[224:227], v[92:95]\n"
    "s_waitcnt lgkmcnt(6)\n"
    "ds_read_b64_tr_b16 a[0:1], v9 offset:45056\n"
    "v_cvt_pk_bf16_f32 v32, v20, v21\n"
    "v_mfma_f32_16x16x32_bf16 v[96:99], a[4:7], v[224:227], v[96:99]\n"
    "ds_read_b64_tr_b16 a[2:3], v9 offset:45568\n"
    "v_cvt_pk_bf16_f32 v33, v22, v23\n"
    "v_cvt_pk_bf16_f32 v34, v28, v29\n"
    "v_mfma_f32_16x16x32_bf16 v[100:103], a[4:7], a[80:83], v[100:103]\n"
    "buffer_load_dwordx4 v5, s[36:39], s58 offen offset:3072 lds\n"
    "v_mov_b32 v35, 0\n"
    "v_mfma_f32_16x16x32_bf16 v[104:107], a[4:7], a[152:155], v[104:107]\n"
    "v_cvt_pk_bf16_f32 v36, v24, v25\n"
    "v_cvt_pk_bf16_f32 v37, v26, v27\n"
    "v_mfma_f32_16x16x32_bf16 v[108:111], a[4:7], a[224:227], v[108:111]\n"
    "s_waitcnt lgkmcnt(6)\n"
    "v_cvt_pk_bf16_f32 v38, v30, v31\n"
    "v_mov_b32 v39, 0\n"
    "s_lshl_b32 s88, s25, 3\n"
    "v_mfma_f32_16x16x32_bf16 v[48:51], a[8:11], v[228:231], v[48:51]\n"
    "v_add_u32 v176, s88, v42\n"
    "v_add_u32 v177, s88, v43\n"
    "v_mfma_f32_16x16x32_bf16 v[52:55], a[8:11], a[84:87], v[52:55]\n"
    "s_lshl_b32 s86, s24, 10\n"
    "v_and_b32 v182, 15, v0\n"
    "v_lshlrev_b32 v182, 3, v182\n"
    "v_lshrrev_b32 v183, 4, v0\n"
    "v_mfma_f32_16x16x32_bf16 v[56:59], a[8:11], a[156:159], v[56:59]\n"
    "s_lshl_b32 s87, s25, 9\n"
    "s_add_u32 s86, s86, s87\n"
    "v_add_u32 v182, s86, v182\n"
    "v_mfma_f32_16x16x32_bf16 v[60:63], a[8:11], a[228:231], v[60:63]\n"
    "s_waitcnt lgkmcnt(4)\n"
    "v_lshl_add_u32 v180, v183, 7, v182\n"
    "v_lshrrev_b32 v181, 1, v183\n"
    "v_mfma_f32_16x16x32_bf16 v[64:67], a[12:15], v[228:231], v[64:67]\n"
    "s_and_b32 s86, s22, 1\n"
    "v_lshlrev_b32 v181, 31, v181\n"
    "v_or_b32 v180, v180, v181\n"
    "v_xor_b32 v181, 512, v180\n"
    "v_mfma_f32_16x16x32_bf16 v[68:71], a[12:15], a[84:87], v[68:71]\n"
    "s_xor_b32 s86, s86, 1\n"
    "s_lshl_b32 s86, s86, 7\n"
    "v_add_u32 v182, s86, v182\n"
    "v_mfma_f32_16x16x32_bf16 v[72:75], a[12:15], a[156:159], v[72:75]\n"
    "v_min_u32 v183, 1, v183\n"
    "v_lshlrev_b32 v183, 31, v183\n"
    "v_or_b32 v182, v182, v183\n"
    "v_mfma_f32_16x16x32_bf16 v[76:79], a[12:15], a[228:231], v[76:79]\n"
    "s_waitcnt lgkmcnt(2)\n"
    "v_lshrrev_b32 v183, 1, v40\n"
    "v_add_u32 v183, 155648, v183\n"
    "v_mfma_f32_16x16x32_bf16 v[80:83], a[36:39], v[228:231], v[80:83]\n"
    "s_lshl_b32 s87, s25, 2\n"
    "v_add_u32 v178, s87, v183\n"
    "v_lshrrev_b32 v179, 1, v41\n"
    "v_mfma_f32_16x16x32_bf16 v[84:87], a[36:39], a[84:87], v[84:87]\n"
    "s_add_u32 s88, s92, 73728\n"
    "v_add_u32 v179, 155648, v179\n"
    "v_add_u32 v179, s87, v179\n"
    "v_mfma_f32_16x16x32_bf16 v[20:23], v[32:35], a[16:19], 0\n"
    "v_cvt_pk_bf16_f32 v121, v154, v155\n"
    "v_mfma_f32_16x16x32_bf16 v[24:27], v[36:39], a[16:19], 0\n"
    "v_exp_f32 v12, v136\n"
    "v_mfma_f32_16x16x32_bf16 v[28:31], v[32:35], a[20:23], 0\n"
    "v_exp_f32 v13, v137\n"
    "v_mfma_f32_16x16x32_bf16 v[44:47], v[36:39], a[20:23], 0\n"
    "s_nop 3\n"
    "v_cndmask_b32_e64 v25, v25, v27, s[82:83]\n"
    "v_cndmask_b32_e64 v27, v26, v24, s[82:83]\n"
    "v_cndmask_b32_e64 v24, v24, v26, s[82:83]\n"
    "v_mfma_f32_16x16x32_bf16 v[88:91], a[36:39], a[156:159], v[88:91]\n"
    "v_mul_f32 v26, s73, v27\n"
    "v_add_f32 v26, s76, v26\n"
    "v_mfma_f32_16x16x32_bf16 v[92:95], a[36:39], a[228:231], v[92:95]\n"
    "s_waitcnt vmcnt(19) lgkmcnt(0)\n"
    "s_barrier\n"
    "ds_read_b64_tr_b16 a[4:5], v8 offset:49152\n"
    "ds_read_b64_tr_b16 a[6:7], v8 offset:49664\n"
    "v_mfma_f32_16x16x32_bf16 v[96:99], a[0:3], v[228:231], v[96:99]\n"
    "s_add_u32 m0, s56, 49152\n"
    "ds_read_b64_tr_b16 a[8:9], v9 offset:49152\n"
    "ds_read_b64_tr_b16 a[10:11], v9 offset:49664\n"
    "s_add_u32 s58, s62, 2359296\n"
    "v_mfma_f32_16x16x32_bf16 v[100:103], a[0:3], a[84:87], v[100:103]\n"
    "ds_read_b64_tr_b16 a[12:13], v8 offset:57344\n"
    "v_mfma_f32_16x16x32_bf16 v[104:107], a[0:3], a[156:159], v[104:107]\n"
    "ds_read_b64_tr_b16 a[14:15], v8 offset:57856\n"
    "buffer_load_dwordx4 v2, s[36:39], s58 offen offset:0 lds\n"
    "v_mfma_f32_16x16x32_bf16 v[108:111], a[0:3], a[228:231], v[108:111]\n"
    "s_waitcnt lgkmcnt(4)\n"
    "ds_read_b64_tr_b16 a[36:37], v9 offset:57344\n"
    "v_mfma_f32_16x16x32_bf16 v[48:51], a[4:7], v[232:235], v[48:51]\n"
    "ds_read_b64_tr_b16 a[38:39], v9 offset:57856\n"
    "v_mfma_f32_16x16x32_bf16 v[52:55], a[4:7], a[88:91], v[52:55]\n"
    "ds_read_b64_tr_b16 a[0:1], v8 offset:53248\n"
    "v_rcp_f32 v26, v26\n"
    "v_mfma_f32_16x16x32_bf16 v[56:59], a[4:7], a[160:163], v[56:59]\n"
    "ds_read_b64_tr_b16 a[2:3], v8 offset:53760\n"
    "v_mul_f32 v20, v20, v26\n"
    "v_mfma_f32_16x16x32_bf16 v[60:63], a[4:7], a[232:235], v[60:63]\n"
    "s_waitcnt lgkmcnt(6)\n"
    "L_hop8:\n"
    "s_cbranch_execz L_hop12\n"
    "ds_read_b64_tr_b16 a[4:5], v9 offset:53248\n"
    "buffer_load_dwordx4 v3, s[36:39], s58 offen offset:1024 lds\n"
    "v_mul_f32 v21, v21, v26\n"
    "v_mfma_f32_16x16x32_bf16 v[64:67], a[8:11], v[232:235], v[64:67]\n"
    "ds_read_b64_tr_b16 a[6:7], v9 offset:53760\n"
    "v_mul_f32 v22, v22, v26\n"
    "v_mfma_f32_16x16x32_bf16 v[68:71], a[8:11], a[88:91], v[68:71]\n"
    "v_mul_f32 v23, v23, v26\n"
    "v_mul_f32 v24, v24, v26\n"
    "v_mul_f32 v25, v25, v26\n"
    "v_mfma_f32_16x16x32_bf16 v[72:75], a[8:11], a[160:163], v[72:75]\n"
    "v_cvt_pk_bf16_f32 v20, v20, v21\n"
    "v_cvt_pk_bf16_f32 v21, v22, v23\n"
    "v_mfma_f32_16x16x32_bf16 v[76:79], a[8:11], a[232:235], v[76:79]\n"
    "s_waitcnt lgkmcnt(6)\n"
    "ds_read_b64_tr_b16 a[8:9], v8 offset:61440\n"
    "v_cvt_pk_bf16_f32 v24, v24, v25\n"
    "v_mfma_f32_16x16x32_bf16 v[80:83], a[12:15], v[232:235], v[80:83]\n"
    "ds_read_b64_tr_b16 a[10:11], v8 offset:61952\n"
    "buffer_load_dwordx4 v4, s[36:39], s58 offen offset:2048 lds\n"
    "v_mfma_f32_16x16x32_bf16 v[84:87], a[12:15], a[88:91], v[84:87]\n"
    "ds_write_b64 v176, v[20:21] offset:0\n"
    "v_mfma_f32_16x16x32_bf16 v[88:91], a[12:15], a[160:163], v[88:91]\n"
    "ds_write_b32 v178, v24 offset:0\n"
    "v_cndmask_b32_e64 v45, v45, v47, s[82:83]\n"
    "v_cndmask_b32_e64 v47, v46, v44, s[82:83]\n"
    "v_mfma_f32_16x16x32_bf16 v[92:95], a[12:15], a[232:235], v[92:95]\n"
    "s_waitcnt lgkmcnt(8)\n"
    "ds_read_b64_tr_b16 a[12:13], v9 offset:61440\n"
    "v_cndmask_b32_e64 v44, v44, v46, s[82:83]\n"
    "v_mfma_f32_16x16x32_bf16 v[96:99], a[36:39], v[232:235], v[96:99]\n"
    "ds_read_b64_tr_b16 a[14:15], v9 offset:61952\n"
    "v_mul_f32 v46, s73, v47\n"
    "v_add_f32 v46, s76, v46\n"
    "v_mfma_f32_16x16x32_bf16 v[100:103], a[36:39], a[88:91], v[100:103]\n"
    "buffer_load_dwordx4 v5, s[36:39], s58 offen offset:3072 lds\n"
    "v_mfma_f32_16x16x32_bf16 v[104:107], a[36:39], a[160:163], v[104:107]\n"
    "v_rcp_f32 v46, v46\n"
    "s_nop 0\n"
    "v_mul_f32 v28, v28, v46\n"
    "v_mfma_f32_16x16x32_bf16 v[108:111], a[36:39], a[232:235], v[108:111]\n"
    "s_waitcnt lgkmcnt(8)\n"
    "v_mul_f32 v29, v29, v46\n"
    "v_mul_f32 v30, v30, v46\n"
    "v_mfma_f32_16x16x32_bf16 v[48:51], a[0:3], v[236:239], v[48:51]\n"
    "v_mul_f32 v31, v31, v46\n"
    "v_mul_f32 v44, v44, v46\n"
    "v_mul_f32 v45, v45, v46\n"
    "v_mfma_f32_16x16x32_bf16 v[52:55], a[0:3], a[92:95], v[52:55]\n"
    "v_cvt_pk_bf16_f32 v28, v28, v29\n"
    "v_cvt_pk_bf16_f32 v29, v30, v31\n"
    "v_mfma_f32_16x16x32_bf16 v[56:59], a[0:3], a[164:167], v[56:59]\n"
    "v_cvt_pk_bf16_f32 v44, v44, v45\n"
    "ds_write_b64 v176, v[28:29] offset:1024\n"
    "v_mfma_f32_16x16x32_bf16 v[60:63], a[0:3], a[236:239], v[60:63]\n"
    "ds_write_b32 v178, v44 offset:512\n"
    "v_mfma_f32_16x16x32_bf16 v[20:23], v[32:35], a[24:27], 0\n"
    "v_exp_f32 v14, v138\n"
    "v_mfma_f32_16x16x32_bf16 v[24:27], v[36:39], a[24:27], 0\n"
    "v_exp_f32 v15, v139\n"
    "v_mfma_f32_16x16x32_bf16 v[28:31], v[32:35], a[28:31], 0\n"
    "v_mul_f32 v168, v136, v168\n"
    "v_mfma_f32_16x16x32_bf16 v[44:47], v[36:39], a[28:31], 0\n"
    "s_waitcnt lgkmcnt(8)\n"
    "s_nop 2\n"
    "v_cndmask_b32_e64 v25, v25, v27, s[82:83]\n"
    "v_cndmask_b32_e64 v27, v26, v24, s[82:83]\n"
    "v_mfma_f32_16x16x32_bf16 v[64:67], a[4:7], v[236:239], v[64:67]\n"
    "v_cndmask_b32_e64 v24, v24, v26, s[82:83]\n"
    "v_mul_f32 v26, s73, v27\n"
    "v_add_f32 v26, s76, v26\n"
    "v_mfma_f32_16x16x32_bf16 v[68:71], a[4:7], a[92:95], v[68:71]\n"
    "v_rcp_f32 v26, v26\n"
    "v_mfma_f32_16x16x32_bf16 v[72:75], a[4:7], a[164:167], v[72:75]\n"
    "v_mul_f32 v20, v20, v26\n"
    "v_mul_f32 v21, v21, v26\n"
    "v_mul_f32 v22, v22, v26\n"
    "v_mfma_f32_16x16x32_bf16 v[76:79], a[4:7], a[236:239], v[76:79]\n"
    "s_waitcnt lgkmcnt(6)\n"
    "v_mul_f32 v23, v23, v26\n"
    "v_mul_f32 v24, v24, v26\n"
    "v_mfma_f32_16x16x32_bf16 v[80:83], a[8:11], v[236:239], v[80:83]\n"
    "v_mul_f32 v25, v25, v26\n"
    "v_cvt_pk_bf16_f32 v20, v20, v21\n"
    "v_cvt_pk_bf16_f32 v21, v22, v23\n"
    "v_mfma_f32_16x16x32_bf16 v[84:87], a[8:11], a[92:95], v[84:87]\n"
    "v_cvt_pk_bf16_f32 v24, v24, v25\n"
    "v_mul_f32 v169, v137, v169\n"
    "v_mfma_f32_16x16x32_bf16 v[88:91], a[8:11], a[164:167], v[88:91]\n"
    "v_mul_f32 v170, v138, v170\n"
    "v_mul_f32 v171, v139, v171\n"
    "v_add_f32 v12, 1.0, v12\n"
    "v_mfma_f32_16x16x32_bf16 v[92:95], a[8:11], a[236:239], v[92:95]\n"
    "s_waitcnt vmcnt(18) lgkmcnt(0)\n"
    "s_barrier\n"
    "ds_read_b64_tr_b16 a[36:37], v6 offset:16384\n"
    "ds_read_b64_tr_b16 a[38:39], v6 offset:16896\n"
    "v_mfma_f32_16x16x32_bf16 v[96:99], a[12:15], v[236:239], v[96:99]\n"
    "s_add_u32 m0, s56, 65536\n"
    "ds_read_b64_tr_b16 a[0:1], v7 offset:16384\n"
    "ds_read_b64_tr_b16 a[2:3], v7 offset:16896\n"
    "s_add_u32 s58, s62, 3538944\n"
    "v_mfma_f32_16x16x32_bf16 v[100:103], a[12:15], a[92:95], v[100:103]\n"
    "ds_read_b64_tr_b16 a[4:5], v6 offset:24576\n"
    "v_add_f32 v13, 1.0, v13\n"
    "v_mfma_f32_16x16x32_bf16 v[104:107], a[12:15], a[164:167], v[104:107]\n"
    "ds_read_b64_tr_b16 a[6:7], v6 offset:25088\n"
    "buffer_load_dwordx4 v2, s[36:39], s58 offen offset:0 lds\n"
    "v_add_f32 v14, 1.0, v14\n"
    "v_mfma_f32_16x16x32_bf16 v[108:111], a[12:15], a[236:239], v[108:111]\n"
    "s_waitcnt lgkmcnt(4)\n"
    "ds_read_b64_tr_b16 a[8:9], v7 offset:24576\n"
    "v_add_f32 v15, 1.0, v15\n"
    "v_mfma_f32_16x16x32_bf16 v[48:51], a[36:39], v[240:243], v[48:51]\n"
    "ds_read_b64_tr_b16 a[10:11], v7 offset:25088\n"
    "ds_write_b64 v177, v[20:21] offset:0\n"
    "v_mfma_f32_16x16x32_bf16 v[52:55], a[36:39], a[96:99], v[52:55]\n"
    "ds_read_b64_tr_b16 a[12:13], v6 offset:20480\n"
    "ds_write_b32 v179, v24 offset:0\n"
    "v_mfma_f32_16x16x32_bf16 v[56:59], a[36:39], a[168:171], v[56:59]\n"
    "ds_read_b64_tr_b16 a[14:15], v6 offset:20992\n"
    "v_cndmask_b32_e64 v45, v45, v47, s[82:83]\n"
    "v_cndmask_b32_e64 v47, v46, v44, s[82:83]\n"
    "v_mfma_f32_16x16x32_bf16 v[60:63], a[36:39], a[240:243], v[60:63]\n"
    "s_waitcnt lgkmcnt(8)\n"
    "ds_read_b64_tr_b16 a[36:37], v7 offset:20480\n"
    "buffer_load_dwordx4 v3, s[36:39], s58 offen offset:1024 lds\n"
    "v_mfma_f32_16x16x32_bf16 v[64:67], a[0:3], v[240:243], v[64:67]\n"
    "ds_read_b64_tr_b16 a[38:39], v7 offset:20992\n"
    "v_cndmask_b32_e64 v44, v44, v46, s[82:83]\n"
    "v_mul_f32 v46, s73, v47\n"
    "v_mfma_f32_16x16x32_bf16 v[68:71], a[0:3], a[96:99], v[68:71]\n"
    "v_add_f32 v46, s76, v46\n"
    "v_mfma_f32_16x16x32_bf16 v[72:75], a[0:3], a[168:171], v[72:75]\n"
    "v_rcp_f32 v46, v46\n"
    "s_nop 0\n"
    "v_mul_f32 v28, v28, v46\n"
    "v_mfma_f32_16x16x32_bf16 v[76:79], a[0:3], a[240:243], v[76:79]\n"
    "s_waitcnt lgkmcnt(8)\n"
    "ds_read_b64_tr_b16 a[0:1], v6 offset:28672\n"
    "v_mul_f32 v29, v29, v46\n"
    "v_mfma_f32_16x16x32_bf16 v[80:83], a[4:7], v[240:243], v[80:83]\n"
    "ds_read_b64_tr_b16 a[2:3], v6 offset:29184\n"
    "buffer_load_dwordx4 v4, s[36:39], s58 offen offset:2048 lds\n"
    "v_mul_f32 v30, v30, v46\n"
    "v_mfma_f32_16x16x32_bf16 v[84:87], a[4:7], a[96:99], v[84:87]\n"
    "v_mul_f32 v31, v31, v46\n"
    "v_mul_f32 v44, v44, v46\n"
    "v_mfma_f32_16x16x32_bf16 v[88:91], a[4:7], a[168:171], v[88:91]\n"
    "v_mul_f32 v45, v45, v46\n"
    "v_cvt_pk_bf16_f32 v28, v28, v29\n"
    "v_cvt_pk_bf16_f32 v29, v30, v31\n"
    "v_mfma_f32_16x16x32_bf16 v[92:95], a[4:7], a[240:243], v[92:95]\n"
    "s_waitcnt lgkmcnt(8)\n"
    "ds_read_b64_tr_b16 a[4:5], v7 offset:28672\n"
    "v_cvt_pk_bf16_f32 v44, v44, v45\n"
    "v_mfma_f32_16x16x32_bf16 v[96:99], a[8:11], v[240:243], v[96:99]\n"
    "ds_read_b64_tr_b16 a[6:7], v7 offset:29184\n"
    "ds_write_b64 v177, v[28:29] offset:1024\n"
    "v_mfma_f32_16x16x32_bf16 v[100:103], a[8:11], a[96:99], v[100:103]\n"
    "buffer_load_dwordx4 v5, s[36:39], s58 offen offset:3072 lds\n"
    "ds_write_b32 v179, v44 offset:512\n"
    "v_mfma_f32_16x16x32_bf16 v[104:107], a[8:11], a[168:171], v[104:107]\n"
    "v_rcp_f32 v12, v12\n"
    "v_mfma_f32_16x16x32_bf16 v[108:111], a[8:11], a[240:243], v[108:111]\n"
    "s_waitcnt lgkmcnt(8)\n"
    "v_rcp_f32 v13, v13\n"
    "v_mfma_f32_16x16x32_bf16 v[48:51], a[12:15], v[244:247], v[48:51]\n"
    "v_rcp_f32 v14, v14\n"
    "v_mfma_f32_16x16x32_bf16 v[52:55], a[12:15], a[100:103], v[52:55]\n"
    "v_rcp_f32 v15, v15\n"
    "v_mfma_f32_16x16x32_bf16 v[56:59], a[12:15], a[172:175], v[56:59]\n"
    "v_mul_f32 v168, v168, v12\n"
    "v_mul_f32 v169, v169, v13\n"
    "v_mul_f32 v170, v170, v14\n"
    "v_mfma_f32_16x16x32_bf16 v[60:63], a[12:15], a[244:247], v[60:63]\n"
    "s_waitcnt lgkmcnt(6)\n"
    "v_mul_f32 v171, v171, v15\n"
    "v_cvt_pk_bf16_f32 v122, v168, v169\n"
    "v_cvt_pk_bf16_f32 v123, v170, v171\n"
    "v_mfma_f32_16x16x32_bf16 v[64:67], a[36:39], v[244:247], v[64:67]\n"
    "v_exp_f32 v12, v124\n"
    "v_mfma_f32_16x16x32_bf16 v[68:71], a[36:39], a[100:103], v[68:71]\n"
    "v_exp_f32 v13, v125\n"
    "v_mfma_f32_16x16x32_bf16 v[72:75], a[36:39], a[172:175], v[72:75]\n"
    "v_exp_f32 v14, v126\n"
    "v_mfma_f32_16x16x32_bf16 v[76:79], a[36:39], a[244:247], v[76:79]\n"
    "s_waitcnt lgkmcnt(4)\n"
    "v_exp_f32 v15, v127\n"
    "v_mul_f32 v156, v124, v156\n"
    "v_mfma_f32_16x16x32_bf16 v[80:83], a[0:3], v[244:247], v[80:83]\n"
    "v_mul_f32 v157, v125, v157\n"
    "v_mul_f32 v158, v126, v158\n"
    "v_mfma_f32_16x16x32_bf16 v[84:87], a[0:3], a[100:103], v[84:87]\n"
    "v_mul_f32 v159, v127, v159\n"
    "v_add_f32 v12, 1.0, v12\n"
    "v_add_f32 v13, 1.0, v13\n"
    "v_mfma_f32_16x16x32_bf16 v[88:91], a[0:3], a[172:175], v[88:91]\n"
    "v_add_f32 v14, 1.0, v14\n"
    "v_add_f32 v15, 1.0, v15\n"
    "v_mfma_f32_16x16x32_bf16 v[92:95], a[0:3], a[244:247], v[92:95]\n"
    "s_waitcnt vmcnt(17) lgkmcnt(0)\n"
    "s_barrier\n"
    "ds_read_b64_tr_b16 a[8:9], v6 offset:40960\n"
    "ds_read_b64_tr_b16 a[10:11], v6 offset:41472\n"
    "v_mfma_f32_16x16x32_bf16 v[96:99], a[4:7], v[244:247], v[96:99]\n"
    "ds_read_b64_tr_b16 a[12:13], v7 offset:40960\n"
    "ds_read_b64_tr_b16 a[14:15], v7 offset:41472\n"
    "ds_read_b128 a[16:19], v42 offset:0\n"
    "v_mfma_f32_16x16x32_bf16 v[100:103], a[4:7], a[100:103], v[100:103]\n"
    "ds_read_b64_tr_b16 a[36:37], v6 offset:49152\n"
    "ds_write_b128 v11, v[120:123] offset:32768\n"
    "v_mfma_f32_16x16x32_bf16 v[104:107], a[4:7], a[172:175], v[104:107]\n"
    "ds_read_b64_tr_b16 a[38:39], v6 offset:49664\n"
    "buffer_store_dwordx4 v[120:123], v11, s[48:51], s60 offen offset:0 sc1\n"
    "v_mfma_f32_16x16x32_bf16 v[108:111], a[4:7], a[244:247], v[108:111]\n"
    "s_waitcnt lgkmcnt(6)\n"
    "ds_read_b64_tr_b16 a[0:1], v7 offset:49152\n"
    "v_rcp_f32 v12, v12\n"
    "v_mfma_f32_16x16x32_bf16 v[48:51], a[8:11], v[248:251], v[48:51]\n"
    "ds_read_b64_tr_b16 a[2:3], v7 offset:49664\n"
    "v_rcp_f32 v13, v13\n"
    "v_mfma_f32_16x16x32_bf16 v[52:55], a[8:11], a[104:107], v[52:55]\n"
    "ds_read_b64_tr_b16 a[4:5], v6 offset:45056\n"
    "v_rcp_f32 v14, v14\n"
    "v_mfma_f32_16x16x32_bf16 v[56:59], a[8:11], a[176:179], v[56:59]\n"
    "ds_read_b64_tr_b16 a[6:7], v6 offset:45568\n"
    "v_rcp_f32 v15, v15\n"
    "v_mfma_f32_16x16x32_bf16 v[60:63], a[8:11], a[248:251], v[60:63]\n"
    "s_waitcnt lgkmcnt(8)\n"
    "ds_read_b64_tr_b16 a[8:9], v7 offset:45056\n"
    "v_mul_f32 v156, v156, v12\n"
    "v_mul_f32 v157, v157, v13\n"
    "v_mfma_f32_16x16x32_bf16 v[64:67], a[12:15], v[248:251], v[64:67]\n"
    "ds_read_b64_tr_b16 a[10:11], v7 offset:45568\n"
    "v_mul_f32 v158, v158, v14\n"
    "v_mfma_f32_16x16x32_bf16 v[68:71], a[12:15], a[104:107], v[68:71]\n"
    "v_mul_f32 v159, v159, v15\n"
    "v_cvt_pk_bf16_f32 v124, v156, v157\n"
    "v_cvt_pk_bf16_f32 v125, v158, v159\n"
    "v_mfma_f32_16x16x32_bf16 v[72:75], a[12:15], a[176:179], v[72:75]\n"
    "v_exp_f32 v12, v140\n"
    "ds_read_b128 a[20:23], v42 offset:1024\n"
    "v_mfma_f32_16x16x32_bf16 v[76:79], a[12:15], a[248:251], v[76:79]\n"
    "s_waitcnt lgkmcnt(7)\n"
    "ds_read_b64_tr_b16 a[12:13], v6 offset:53248\n"
    "v_exp_f32 v13, v141\n"
    "v_mfma_f32_16x16x32_bf16 v[80:83], a[36:39], v[248:251], v[80:83]\n"
    "ds_read_b64_tr_b16 a[14:15], v6 offset:53760\n"
    "v_exp_f32 v14, v142\n"
    "v_mfma_f32_16x16x32_bf16 v[84:87], a[36:39], a[104:107], v[84:87]\n"
    "v_exp_f32 v15, v143\n"
    "v_mul_f32 v172, v140, v172\n"
    "v_mfma_f32_16x16x32_bf16 v[88:91], a[36:39], a[176:179], v[88:91]\n"
    "v_mul_f32 v173, v141, v173\n"
    "v_mul_f32 v174, v142, v174\n"
    "v_mul_f32 v175, v143, v175\n"
    "v_mfma_f32_16x16x32_bf16 v[92:95], a[36:39], a[248:251], v[92:95]\n"
    "s_waitcnt lgkmcnt(7)\n"
    "ds_read_b64_tr_b16 a[36:37], v7 offset:53248\n"
    "v_add_f32 v12, 1.0, v12\n"
    "v_mfma_f32_16x16x32_bf16 v[96:99], a[0:3], v[248:251], v[96:99]\n"
    "ds_read_b64_tr_b16 a[38:39], v7 offset:53760\n"
    "v_add_f32 v13, 1.0, v13\n"
    "v_add_f32 v14, 1.0, v14\n"
    "v_mfma_f32_16x16x32_bf16 v[100:103], a[0:3], a[104:107], v[100:103]\n"
    "v_add_f32 v15, 1.0, v15\n"
    "v_rcp_f32 v12, v12\n"
    "v_mfma_f32_16x16x32_bf16 v[104:107], a[0:3], a[176:179], v[104:107]\n"
    "v_rcp_f32 v13, v13\n"
    "ds_read_b128 a[24:27], v43 offset:0\n"
    "v_mfma_f32_16x16x32_bf16 v[108:111], a[0:3], a[248:251], v[108:111]\n"
    "s_waitcnt lgkmcnt(8)\n"
    "v_rcp_f32 v14, v14\n"
    "v_rcp_f32 v15, v15\n"
    "v_mfma_f32_16x16x32_bf16 v[48:51], a[4:7], v[252:255], v[48:51]\n"
    "v_mul_f32 v172, v172, v12\n"
    "v_mul_f32 v173, v173, v13\n"
    "v_mul_f32 v174, v174, v14\n"
    "v_mfma_f32_16x16x32_bf16 v[52:55], a[4:7], a[108:111], v[52:55]\n"
    "v_mul_f32 v175, v175, v15\n"
    "v_cvt_pk_bf16_f32 v126, v172, v173\n"
    "v_cvt_pk_bf16_f32 v127, v174, v175\n"
    "v_mfma_f32_16x16x32_bf16 v[56:59], a[4:7], a[180:183], v[56:59]\n"
    "ds_write_b128 v11, v[124:127] offset:33792\n"
    "v_mfma_f32_16x16x32_bf16 v[60:63], a[4:7], a[252:255], v[60:63]\n"
    "s_waitcnt lgkmcnt(7)\n"
    "buffer_store_dwordx4 v[124:127], v11, s[48:51], s60 offen offset:1024 sc1\n"
    "ds_read_b128 a[28:31], v43 offset:1024\n"
    "v_mfma_f32_16x16x32_bf16 v[64:67], a[8:11], v[252:255], v[64:67]\n"
    "ds_read_b64 v[44:45], v183 offset:0\n"
    "ds_read_b64 v[46:47], v183 offset:512\n"
    "v_mfma_f32_16x16x32_bf16 v[68:71], a[8:11], a[108:111], v[68:71]\n"
    "buffer_store_dwordx4 a[16:19], v40, s[48:51], s88 offen offset:0 sc1\n"
    "v_mfma_f32_16x16x32_bf16 v[72:75], a[8:11], a[180:183], v[72:75]\n"
    "v_mfma_f32_16x16x32_bf16 v[76:79], a[8:11], a[252:255], v[76:79]\n"
    "s_waitcnt lgkmcnt(9)\n"
    "buffer_store_dwordx4 a[20:23], v40, s[48:51], s88 offen offset:1024 sc1\n"
    "s_waitcnt lgkmcnt(7)\n"
    "v_mfma_f32_16x16x32_bf16 v[80:83], a[12:15], v[252:255], v[80:83]\n"
    "v_mfma_f32_16x16x32_bf16 v[84:87], a[12:15], a[108:111], v[84:87]\n"
    "v_mfma_f32_16x16x32_bf16 v[88:91], a[12:15], a[180:183], v[88:91]\n"
    "v_mfma_f32_16x16x32_bf16 v[92:95], a[12:15], a[252:255], v[92:95]\n"
    "s_waitcnt vmcnt(17) lgkmcnt(0)\n"
    "s_barrier\n"
    "ds_read_b64_tr_b16 a[0:1], v19 offset:0\n"
    "ds_read_b64_tr_b16 a[2:3], v19 offset:256\n"
    "v_mfma_f32_16x16x32_bf16 v[96:99], a[36:39], v[252:255], v[96:99]\n"
    "s_add_u32 m0, s57, 0\n"
    "ds_read_b64_tr_b16 a[4:5], v19 offset:8192\n"
    "ds_read_b64_tr_b16 a[6:7], v19 offset:8448\n"
    "v_mfma_f32_16x16x32_bf16 v[100:103], a[36:39], a[108:111], v[100:103]\n"
    "ds_read_b64_tr_b16 a[8:9], v19 offset:2048\n"
    "buffer_store_dwordx2 v[44:45], v182, s[48:51], s94 offen offset:0 sc1\n"
    "v_mfma_f32_16x16x32_bf16 v[104:107], a[36:39], a[180:183], v[104:107]\n"
    "ds_read_b64_tr_b16 a[10:11], v19 offset:2304\n"
    "buffer_load_dwordx4 v2, s[36:39], s63 offen offset:0 lds\n"
    "v_mfma_f32_16x16x32_bf16 v[108:111], a[36:39], a[252:255], v[108:111]\n"
    "s_waitcnt lgkmcnt(4)\n"
    "ds_read_b64_tr_b16 a[12:13], v19 offset:10240\n"
    "v_mfma_f32_16x16x32_bf16 v[112:115], a[0:3], v[184:187], 0\n"
    "ds_read_b64_tr_b16 a[14:15], v19 offset:10496\n"
    "v_mfma_f32_16x16x32_bf16 v[116:119], a[0:3], a[40:43], 0\n"
    "ds_read_b64_tr_b16 a[36:37], v19 offset:4096\n"
    "v_mfma_f32_16x16x32_bf16 v[120:123], a[0:3], a[112:115], 0\n"
    "s_add_u32 s60, s92, 49152\n"
    "ds_read_b64_tr_b16 a[38:39], v19 offset:4352\n"
    "v_mfma_f32_16x16x32_bf16 v[124:127], a[0:3], a[184:187], 0\n"
    "s_waitcnt lgkmcnt(6)\n"
    "ds_read_b64_tr_b16 a[0:1], v19 offset:12288\n"
    "buffer_load_dwordx4 v3, s[36:39], s63 offen offset:1024 lds\n"
    "v_mfma_f32_16x16x32_bf16 v[128:131], a[4:7], v[184:187], 0\n"
    "ds_read_b64_tr_b16 a[2:3], v19 offset:12544\n"
    "v_mfma_f32_16x16x32_bf16 v[132:135], a[4:7], a[40:43], 0\n"
    "v_exp_f32 v12, v48\n"
    "v_mfma_f32_16x16x32_bf16 v[136:139], a[4:7], a[112:115], 0\n"
    "v_exp_f32 v13, v49\n"
    "v_mfma_f32_16x16x32_bf16 v[140:143], a[4:7], a[184:187], 0\n"
    "s_waitcnt lgkmcnt(6)\n"
    "ds_read_b64_tr_b16 a[4:5], v19 offset:6144\n"
    "ds_read_b128 a[40:43], v40 offset:0\n"
    "v_mfma_f32_16x16x32_bf16 v[112:115], a[8:11], v[188:191], v[112:115]\n"
    "ds_read_b64_tr_b16 a[6:7], v19 offset:6400\n"
    "ds_read_b128 a[112:115], v40 offset:1024\n"
    "v_mfma_f32_16x16x32_bf16 v[116:119], a[8:11], a[44:47], v[116:119]\n"
    "s_xor_b32 s86, s22, 1\n"
    "ds_read_b128 a[184:187], v41 offset:0\n"
    "buffer_store_dwordx2 v[46:47], v182, s[48:51], s94 offen offset:256 sc1\n"
    "s_add_u32 s86, s86, 16\n"
    "v_mfma_f32_16x16x32_bf16 v[120:123], a[8:11], a[116:119], v[120:123]\n"
    "v_readlane_b32 s87, v16, s86\n"
    "v_bfe_u32 v37, v0, 1, 1\n"
    "v_mfma_f32_16x16x32_bf16 v[124:127], a[8:11], a[188:191], v[124:127]\n"
    "s_waitcnt lgkmcnt(9)\n"
    "ds_read_b64_tr_b16 a[8:9], v19 offset:14336\n"
    "v_cmp_eq_u32_e64 vcc, 1, v37\n"
    "v_mfma_f32_16x16x32_bf16 v[128:131], a[12:15], v[188:191], v[128:131]\n"
    "ds_read_b64_tr_b16 a[10:11], v19 offset:14592\n"
    "v_mov_b32 v26, s27\n"
    "v_mfma_f32_16x16x32_bf16 v[132:135], a[12:15], a[44:47], v[132:135]\n"
    "s_lshl_b32 s86, s23, 4\n"
    "v_mov_b32 v25, s87\n"
    "v_cndmask_b32_e64 v26, v26, v25, vcc\n"
    "v_mfma_f32_16x16x32_bf16 v[136:139], a[12:15], a[116:119], v[136:139]\n"
    "s_add_u32 s86, s86, s22\n"
    "s_lshl_b32 s86, s86, 10\n"
    "s_lshl_b32 s87, s24, 8\n"
    "v_mfma_f32_16x16x32_bf16 v[140:143], a[12:15], a[188:191], v[140:143]\n"
    "s_waitcnt lgkmcnt(9)\n"
    "ds_read_b128 a[44:47], v41 offset:1024\n"
    "ds_read_b128 a[116:119], v40 offset:8192\n"
    "v_mfma_f32_16x16x32_bf16 v[112:115], a[36:39], v[192:195], v[112:115]\n"
    "s_add_u32 s86, s86, s87\n"
    "ds_read_b128 a[188:191], v40 offset:9216\n"
    "v_lshlrev_b32 v37, 10, v37\n"
    "v_mfma_f32_16x16x32_bf16 v[116:119], a[36:39], a[48:51], v[116:119]\n"
    "v_lshrrev_b32 v1, 4, v0\n"
    "v_lshlrev_b32 v1, 1, v1\n"
    "v_and_b32 v3, 7, v0\n"
    "v_mfma_f32_16x16x32_bf16 v[120:123], a[36:39], a[120:123], v[120:123]\n"
    "v_xor_b32 v1, v1, v3\n"
    "v_lshrrev_b32 v3, 3, v0\n"
    "v_mfma_f32_16x16x32_bf16 v[124:127], a[36:39], a[192:195], v[124:127]\n"
    "s_waitcnt lgkmcnt(10)\n"
    "v_mul_u32_u24 v3, 1152, v3\n"
    "v_lshl_add_u32 v2, v1, 4, v3\n"
    "v_mfma_f32_16x16x32_bf16 v[128:131], a[0:3], v[192:195], v[128:131]\n"
    "v_lshrrev_b32 v1, 5, v0\n"
    "v_mul_u32_u24 v1, 36864, v1\n"
    "v_mfma_f32_16x16x32_bf16 v[132:135], a[0:3], a[48:51], v[132:135]\n"
    "v_add_u32 v3, v2, v1\n"
    "v_xor_b32 v25, s86, v37\n"
    "v_mfma_f32_16x16x32_bf16 v[136:139], a[0:3], a[120:123], v[136:139]\n"
    "v_and_b32 v37, 1, v0\n"
    "v_lshl_add_u32 v25, v37, 7, v25\n"
    "v_mfma_f32_16x16x32_bf16 v[140:143], a[0:3], a[192:195], v[140:143]\n"
    "s_waitcnt lgkmcnt(7)\n"
    "ds_read_b128 a[48:51], v41 offset:8192\n"
    "ds_read_b128 a[120:123], v41 offset:9216\n"
    "v_mfma_f32_16x16x32_bf16 v[112:115], a[4:7], v[196:199], v[112:115]\n"
    "ds_read_b128 a[192:195], v40 offset:16384\n"
    "v_lshrrev_b32 v182, 2, v0\n"
    "v_and_b32 v183, 7, v182\n"
    "v_mfma_f32_16x16x32_bf16 v[116:119], a[4:7], a[52:55], v[116:119]\n"
    "v_lshrrev_b32 v44, 5, v0\n"
    "v_lshlrev_b32 v44, 12, v44\n"
    "v_mfma_f32_16x16x32_bf16 v[120:123], a[4:7], a[124:127], v[120:123]\n"
    "v_lshl_add_u32 v44, v183, 7, v44\n"
    "v_and_b32 v45, 3, v0\n"
    "v_mfma_f32_16x16x32_bf16 v[124:127], a[4:7], a[196:199], v[124:127]\n"
    "s_waitcnt vmcnt(16) lgkmcnt(0)\n"
    "s_barrier\n"
    "ds_read_b64_tr_b16 a[12:13], v19 offset:16384\n"
    "ds_read_b64_tr_b16 a[14:15], v19 offset:16640\n"
    "v_mfma_f32_16x16x32_bf16 v[128:131], a[8:11], v[196:199], v[128:131]\n"
    "s_add_u32 m0, s56, 16384\n"
    "ds_read_b64_tr_b16 a[36:37], v19 offset:24576\n"
    "ds_read_b64_tr_b16 a[38:39], v19 offset:24832\n"
    "v_mfma_f32_16x16x32_bf16 v[132:135], a[8:11], a[52:55], v[132:135]\n"
    "s_add_u32 s58, s64, 0\n"
    "ds_read_b64_tr_b16 a[0:1], v19 offset:18432\n"
    "v_lshl_add_u32 v44, v45, 3, v44\n"
    "v_mfma_f32_16x16x32_bf16 v[136:139], a[8:11], a[124:127], v[136:139]\n"
    "s_sub_u32 s59, s64, 896\n"
    "ds_read_b64_tr_b16 a[2:3], v19 offset:18688\n"
    "buffer_load_dwordx4 v2, s[40:43], s58 offen offset:0 lds\n"
    "v_mfma_f32_16x16x32_bf16 v[140:143], a[8:11], a[196:199], v[140:143]\n"
    "s_waitcnt lgkmcnt(4)\n"
    "ds_read_b64_tr_b16 a[4:5], v19 offset:26624\n"
    "ds_read_b128 a[52:55], v40 offset:17408\n"
    "v_mfma_f32_16x16x32_bf16 v[112:115], a[12:15], v[200:203], v[112:115]\n"
    "ds_read_b64_tr_b16 a[6:7], v19 offset:26880\n"
    "ds_read_b128 a[124:127], v41 offset:16384\n"
    "ds_read_b128 a[196:199], v41 offset:17408\n"
    "v_mfma_f32_16x16x32_bf16 v[116:119], a[12:15], a[56:59], v[116:119]\n"
    "ds_read_b64_tr_b16 a[8:9], v19 offset:20480\n"
    "v_add_u32 v44, 32768, v44\n"
    "v_mfma_f32_16x16x32_bf16 v[120:123], a[12:15], a[128:131], v[120:123]\n"
    "s_lshl_b32 s86, s25, 1\n"
    "ds_read_b64_tr_b16 a[10:11], v19 offset:20736\n"
    "v_lshrrev_b32 v183, 1, v183\n"
    "v_mfma_f32_16x16x32_bf16 v[124:127], a[12:15], a[200:203], v[124:127]\n"
    "s_waitcnt lgkmcnt(9)\n"
    "ds_read_b64_tr_b16 a[12:13], v19 offset:28672\n"
    "buffer_load_dwordx4 v2, s[40:43], s59 offen offset:1024 lds\n"
    "v_mfma_f32_16x16x32_bf16 v[128:131], a[36:39], v[200:203], v[128:131]\n"
    "s_sub_u32 s58, s64, 1792\n"
    "ds_read_b64_tr_b16 a[14:15], v19 offset:28928\n"
    "s_add_u32 s87, s86, 0\n"
    "v_mfma_f32_16x16x32_bf16 v[132:135], a[36:39], a[56:59], v[132:135]\n"
    "v_xor_b32 v20, s87, v183\n"
    "v_lshl_add_u32 v20, v20, 5, v44\n"
    "v_mfma_f32_16x16x32_bf16 v[136:139], a[36:39], a[128:131], v[136:139]\n"
    "s_add_u32 s87, s86, 1\n"
    "v_add_u32 v22, 57344, v20\n"
    "v_xor_b32 v21, s87, v183\n"
    "v_mfma_f32_16x16x32_bf16 v[140:143], a[36:39], a[200:203], v[140:143]\n"
    "s_waitcnt lgkmcnt(9)\n"
    "ds_read_b64_tr_b16 a[36:37], v19 offset:22528\n"
    "ds_read_b128 a[56:59], v40 offset:24576\n"
    "v_mfma_f32_16x16x32_bf16 v[112:115], a[0:3], v[204:207], v[112:115]\n"
    "s_sub_u32 s59, s64, 2688\n"
    "ds_read_b64_tr_b16 a[38:39], v19 offset:22784\n"
    "buffer_load_dwordx4 v2, s[40:43], s58 offen offset:2048 lds\n"
    "ds_read_b128 a[128:131], v40 offset:25600\n"
    "v_mfma_f32_16x16x32_bf16 v[116:119], a[0:3], a[60:63], v[116:119]\n"
    "ds_read_b128 a[200:203], v41 offset:24576\n"
    "v_lshl_add_u32 v21, v21, 5, v44\n"
    "v_mfma_f32_16x16x32_bf16 v[120:123], a[0:3], a[132:135], v[120:123]\n"
    "v_add_u32 v23, 57344, v21\n"
    "v_lshlrev_b32 v27, 4, v0\n"
    "v_mfma_f32_16x16x32_bf16 v[124:127], a[0:3], a[204:207], v[124:127]\n"
    "s_waitcnt lgkmcnt(11)\n"
    "ds_read_b64_tr_b16 a[0:1], v19 offset:30720\n"
    "v_mfma_f32_16x16x32_bf16 v[128:131], a[4:7], v[204:207], v[128:131]\n"
    "ds_read_b64_tr_b16 a[2:3], v19 offset:30976\n"
    "v_mfma_f32_16x16x32_bf16 v[132:135], a[4:7], a[60:63], v[132:135]\n"
    "buffer_load_dwordx4 v2, s[40:43], s59 offen offset:3072 lds\n"
    "v_mfma_f32_16x16x32_bf16 v[136:139], a[4:7], a[132:135], v[136:139]\n"
    "v_exp_f32 v14, v50\n"
    "v_mfma_f32_16x16x32_bf16 v[140:143], a[4:7], a[204:207], v[140:143]\n"
    "ds_read_b128 a[60:63], v41 offset:25600\n"
    "ds_read_b128 a[132:135], v40 offset:32768\n"
    "s_waitcnt lgkmcnt(14)\n"
    "ds_read_b128 a[204:207], v40 offset:33792\n"
    "s_waitcnt lgkmcnt(12)\n"
    "v_mfma_f32_16x16x32_bf16 v[112:115], a[8:11], v[208:211], v[112:115]\n"
    "v_exp_f32 v15, v51\n"
    "v_mfma_f32_16x16x32_bf16 v[116:119], a[8:11], a[64:67], v[116:119]\n"
    "v_mul_f32 v80, v48, v80\n"
    "v_mul_f32 v81, v49, v81\n"
    "v_mfma_f32_16x16x32_bf16 v[120:123], a[8:11], a[136:139], v[120:123]\n"
    "v_mul_f32 v82, v50, v82\n"
    "v_mul_f32 v83, v51, v83\n"
    "v_mfma_f32_16x16x32_bf16 v[124:127], a[8:11], a[208:211], v[124:127]\n"
    "s_waitcnt lgkmcnt(10)\n"
    "v_add_f32 v12, 1.0, v12\n"
    "v_add_f32 v13, 1.0, v13\n"
    "v_mfma_f32_16x16x32_bf16 v[128:131], a[12:15], v[208:211], v[128:131]\n"
    "v_add_f32 v14, 1.0, v14\n"
    "v_add_f32 v15, 1.0, v15\n"
    "v_mfma_f32_16x16x32_bf16 v[132:135], a[12:15], a[64:67], v[132:135]\n"
    "v_rcp_f32 v12, v12\n"
    "v_mfma_f32_16x16x32_bf16 v[136:139], a[12:15], a[136:139], v[136:139]\n"
    "v_rcp_f32 v13, v13\n"
    "v_mfma_f32_16x16x32_bf16 v[140:143], a[12:15], a[208:211], v[140:143]\n"
    "s_waitcnt lgkmcnt(7)\n"
    "ds_read_b128 a[64:67], v41 offset:32768\n"
    "ds_read_b128 a[136:139], v41 offset:33792\n"
    "ds_read_b128 a[208:211], v40 offset:40960\n"
    "v_mfma_f32_16x16x32_bf16 v[112:115], a[36:39], v[212:215], v[112:115]\n"
    "v_rcp_f32 v14, v14\n"
    "v_mfma_f32_16x16x32_bf16 v[116:119], a[36:39], a[68:71], v[116:119]\n"
    "v_rcp_f32 v15, v15\n"
    "v_mfma_f32_16x16x32_bf16 v[120:123], a[36:39], a[140:143], v[120:123]\n"
    "v_mul_f32 v80, v80, v12\n"
    "v_mul_f32 v81, v81, v13\n"
    "v_mfma_f32_16x16x32_bf16 v[124:127], a[36:39], a[212:215], v[124:127]\n"
    "s_waitcnt vmcnt(16) lgkmcnt(0)\n"
    "s_barrier\n"
    "ds_read_b64_tr_b16 a[4:5], v19 offset:32768\n"
    "ds_read_b64_tr_b16 a[6:7], v19 offset:33024\n"
    "v_mfma_f32_16x16x32_bf16 v[128:131], a[0:3], v[212:215], v[128:131]\n"
    "s_add_u32 m0, s56, 32768\n"
    "ds_read_b64_tr_b16 a[8:9], v19 offset:40960\n"
    "ds_read_b64_tr_b16 a[10:11], v19 offset:41216\n"
    "v_mfma_f32_16x16x32_bf16 v[132:135], a[0:3], a[68:71], v[132:135]\n"
    "s_add_u32 s58, s64, s95\n"
    "ds_read_b64_tr_b16 a[12:13], v19 offset:34816\n"
    "s_add_u32 s58, s58, 0\n"
    "v_mfma_f32_16x16x32_bf16 v[136:139], a[0:3], a[140:143], v[136:139]\n"
    "s_add_u32 s59, s64, 35840\n"
    "ds_read_b64_tr_b16 a[14:15], v19 offset:35072\n"
    "buffer_load_dwordx4 v2, s[40:43], s58 offen offset:0 lds\n"
    "v_mul_f32 v82, v82, v14\n"
    "v_mfma_f32_16x16x32_bf16 v[140:143], a[0:3], a[212:215], v[140:143]\n"
    "s_waitcnt lgkmcnt(4)\n"
    "ds_read_b64_tr_b16 a[36:37], v19 offset:43008\n"
    "ds_read_b128 a[68:71], v40 offset:41984\n"
    "v_mfma_f32_16x16x32_bf16 v[112:115], a[4:7], v[216:219], v[112:115]\n"
    "ds_read_b64_tr_b16 a[38:39], v19 offset:43264\n"
    "ds_read_b128 a[140:143], v41 offset:40960\n"
    "v_mfma_f32_16x16x32_bf16 v[116:119], a[4:7], a[72:75], v[116:119]\n"
    "ds_read_b64_tr_b16 a[0:1], v19 offset:36864\n"
    "ds_read_b128 a[212:215], v41 offset:41984\n"
    "v_mfma_f32_16x16x32_bf16 v[120:123], a[4:7], a[144:147], v[120:123]\n"
    "ds_read_b64_tr_b16 a[2:3], v19 offset:37120\n"
    "v_mul_f32 v83, v83, v15\n"
    "v_mfma_f32_16x16x32_bf16 v[124:127], a[4:7], a[216:219], v[124:127]\n"
    "s_waitcnt lgkmcnt(9)\n"
    "ds_read_b64_tr_b16 a[4:5], v19 offset:45056\n"
    "buffer_load_dwordx4 v2, s[40:43], s59 offen offset:1024 lds\n"
    "v_mfma_f32_16x16x32_bf16 v[128:131], a[8:11], v[216:219], v[128:131]\n"
    "s_add_u32 s58, s64, 34944\n"
    "ds_read_b64_tr_b16 a[6:7], v19 offset:45312\n"
    "v_cvt_pk_bf16_f32 v48, v80, v81\n"
    "v_mfma_f32_16x16x32_bf16 v[132:135], a[8:11], a[72:75], v[132:135]\n"
    "v_cvt_pk_bf16_f32 v49, v82, v83\n"
    "v_mfma_f32_16x16x32_bf16 v[136:139], a[8:11], a[144:147], v[136:139]\n"
    "v_exp_f32 v12, v64\n"
    "v_mfma_f32_16x16x32_bf16 v[140:143], a[8:11], a[216:219], v[140:143]\n"
    "s_waitcnt lgkmcnt(9)\n"
    "ds_read_b64_tr_b16 a[8:9], v19 offset:38912\n"
    "v_mfma_f32_16x16x32_bf16 v[112:115], a[12:15], v[220:223], v[112:115]\n"
    "s_add_u32 s59, s64, 34048\n"
    "ds_read_b64_tr_b16 a[10:11], v19 offset:39168\n"
    "buffer_load_dwordx4 v2, s[40:43], s58 offen offset:2048 lds\n"
    "v_mfma_f32_16x16x32_bf16 v[116:119], a[12:15], a[76:79], v[116:119]\n"
    "v_exp_f32 v13, v65\n"
    "v_mfma_f32_16x16x32_bf16 v[120:123], a[12:15], a[148:151], v[120:123]\n"
    "v_exp_f32 v14, v66\n"
    "v_mfma_f32_16x16x32_bf16 v[124:127], a[12:15], a[220:223], v[124:127]\n"
    "s_waitcnt lgkmcnt(8)\n"
    "ds_read_b64_tr_b16 a[12:13], v19 offset:47104\n"
    "v_mfma_f32_16x16x32_bf16 v[128:131], a[36:39], v[220:223], v[128:131]\n"
    "s_add_u32 s89, s27, 2\n"
    "ds_read_b64_tr_b16 a[14:15], v19 offset:47360\n"
    "v_mov_b32 v38, 0x80000000\n"
    "v_mfma_f32_16x16x32_bf16 v[132:135], a[36:39], a[76:79], v[132:135]\n"
    "buffer_load_dwordx4 v2, s[40:43], s59 offen offset:3072 lds\n"
    "v_writelane_b32 v38, s26, 0\n"
    "v_mov_b32 v39, s89\n"
    "v_mfma_f32_16x16x32_bf16 v[136:139], a[36:39], a[148:151], v[136:139]\n"
    "v_exp_f32 v15, v67\n"
    "v_mfma_f32_16x16x32_bf16 v[140:143], a[36:39], a[220:223], v[140:143]\n"
    "s_waitcnt lgkmcnt(6)\n"
    "v_mul_f32 v96, v64, v96\n"
    "v_mul_f32 v97, v65, v97\n"
    "v_mfma_f32_16x16x32_bf16 v[112:115], a[0:3], v[224:227], v[112:115]\n"
    "v_mul_f32 v98, v66, v98\n"
    "v_mul_f32 v99, v67, v99\n"
    "v_mfma_f32_16x16x32_bf16 v[116:119], a[0:3], a[80:83], v[116:119]\n"
    "v_add_f32 v12, 1.0, v12\n"
    "v_add_f32 v13, 1.0, v13\n"
    "v_mfma_f32_16x16x32_bf16 v[120:123], a[0:3], a[152:155], v[120:123]\n"
    "v_add_f32 v14, 1.0, v14\n"
    "v_add_f32 v15, 1.0, v15\n"
    "v_mfma_f32_16x16x32_bf16 v[124:127], a[0:3], a[224:227], v[124:127]\n"
    "s_waitcnt lgkmcnt(4)\n"
    "v_rcp_f32 v12, v12\n"
    "v_mfma_f32_16x16x32_bf16 v[128:131], a[4:7], v[224:227], v[128:131]\n"
    "s_waitcnt vmcnt(14)\n"
    "buffer_store_dword v39, v38, s[48:51], 0 offen offset:0 sc1\n"
    "v_rcp_f32 v13, v13\n"
    "v_mfma_f32_16x16x32_bf16 v[132:135], a[4:7], a[80:83], v[132:135]\n"
    "v_rcp_f32 v14, v14\n"
    "v_mfma_f32_16x16x32_bf16 v[136:139], a[4:7], a[152:155], v[136:139]\n"
    "v_rcp_f32 v15, v15\n"
    "v_mfma_f32_16x16x32_bf16 v[140:143], a[4:7], a[224:227], v[140:143]\n"
    "s_waitcnt lgkmcnt(2)\n"
    "v_mul_f32 v96, v96, v12\n"
    "v_mul_f32 v97, v97, v13\n"
    "v_mfma_f32_16x16x32_bf16 v[112:115], a[8:11], v[228:231], v[112:115]\n"
    "v_mul_f32 v98, v98, v14\n"
    "v_mul_f32 v99, v99, v15\n"
    "v_mfma_f32_16x16x32_bf16 v[116:119], a[8:11], a[84:87], v[116:119]\n"
    "v_cvt_pk_bf16_f32 v50, v96, v97\n"
    "v_cvt_pk_bf16_f32 v51, v98, v99\n"
    "v_mfma_f32_16x16x32_bf16 v[120:123], a[8:11], a[156:159], v[120:123]\n"
    "v_exp_f32 v12, v52\n"
    "v_mfma_f32_16x16x32_bf16 v[124:127], a[8:11], a[228:231], v[124:127]\n"
    "s_waitcnt lgkmcnt(0)\n"
    "s_barrier\n"
    "ds_read_b64_tr_b16 a[36:37], v19 offset:49152\n"
    "ds_read_b64_tr_b16 a[38:39], v19 offset:49408\n"
    "L_hop9:\n"
    "s_cbranch_execz L_hop13\n"
    "v_mfma_f32_16x16x32_bf16 v[128:131], a[12:15], v[228:231], v[128:131]\n"
    "s_add_u32 m0, s56, 49152\n"
    "ds_read_b64_tr_b16 a[0:1], v19 offset:57344\n"
    "ds_read_b64_tr_b16 a[2:3], v19 offset:57600\n"
    "v_mfma_f32_16x16x32_bf16 v[132:135], a[12:15], a[84:87], v[132:135]\n"
    "s_add_u32 s58, s64, 37248\n"
    "ds_read_b64_tr_b16 a[4:5], v19 offset:51200\n"
    "v_mfma_f32_16x16x32_bf16 v[136:139], a[12:15], a[156:159], v[136:139]\n"
    "s_add_u32 s59, s64, s95\n"
    "ds_read_b64_tr_b16 a[6:7], v19 offset:51456\n"
    "buffer_load_dwordx4 v2, s[40:43], s58 offen offset:0 lds\n"
    "v_mfma_f32_16x16x32_bf16 v[140:143], a[12:15], a[228:231], v[140:143]\n"
    "s_waitcnt lgkmcnt(4)\n"
    "ds_read_b64_tr_b16 a[8:9], v19 offset:59392\n"
    "s_add_u32 s59, s59, 35840\n"
    "v_mfma_f32_16x16x32_bf16 v[112:115], a[36:39], v[232:235], v[112:115]\n"
    "ds_read_b64_tr_b16 a[10:11], v19 offset:59648\n"
    "v_mfma_f32_16x16x32_bf16 v[116:119], a[36:39], a[88:91], v[116:119]\n"
    "ds_read_b64_tr_b16 a[12:13], v19 offset:53248\n"
    "v_mfma_f32_16x16x32_bf16 v[120:123], a[36:39], a[160:163], v[120:123]\n"
    "ds_read_b64_tr_b16 a[14:15], v19 offset:53504\n"
    "ds_write_b128 v10, v[48:51] offset:49152\n"
    "v_mfma_f32_16x16x32_bf16 v[124:127], a[36:39], a[232:235], v[124:127]\n"
    "s_waitcnt lgkmcnt(7)\n"
    "ds_read_b64_tr_b16 a[36:37], v19 offset:61440\n"
    "buffer_load_dwordx4 v2, s[40:43], s59 offen offset:1024 lds\n"
    "v_mfma_f32_16x16x32_bf16 v[128:131], a[0:3], v[232:235], v[128:131]\n"
    "s_add_u32 s58, s64, 71680\n"
    "ds_read_b64_tr_b16 a[38:39], v19 offset:61696\n"
    "buffer_store_dwordx4 v[48:51], v10, s[48:51], s60 offen offset:0 sc1\n"
    "v_mfma_f32_16x16x32_bf16 v[132:135], a[0:3], a[88:91], v[132:135]\n"
    "v_exp_f32 v13, v53\n"
    "v_mfma_f32_16x16x32_bf16 v[136:139], a[0:3], a[160:163], v[136:139]\n"
    "v_exp_f32 v14, v54\n"
    "v_mfma_f32_16x16x32_bf16 v[140:143], a[0:3], a[232:235], v[140:143]\n"
    "s_waitcnt lgkmcnt(7)\n"
    "ds_read_b64_tr_b16 a[0:1], v19 offset:55296\n"
    "v_mfma_f32_16x16x32_bf16 v[112:115], a[4:7], v[236:239], v[112:115]\n"
    "s_add_u32 s59, s64, 70784\n"
    "ds_read_b64_tr_b16 a[2:3], v19 offset:55552\n"
    "buffer_load_dwordx4 v2, s[40:43], s58 offen offset:2048 lds\n"
    "v_mfma_f32_16x16x32_bf16 v[116:119], a[4:7], a[92:95], v[116:119]\n"
    "v_exp_f32 v15, v55\n"
    "v_mul_f32 v84, v52, v84\n"
    "v_mfma_f32_16x16x32_bf16 v[120:123], a[4:7], a[164:167], v[120:123]\n"
    "v_mul_f32 v85, v53, v85\n"
    "v_mul_f32 v86, v54, v86\n"
    "v_mfma_f32_16x16x32_bf16 v[124:127], a[4:7], a[236:239], v[124:127]\n"
    "s_waitcnt lgkmcnt(7)\n"
    "ds_read_b64_tr_b16 a[4:5], v19 offset:63488\n"
    "v_mul_f32 v87, v55, v87\n"
    "v_mfma_f32_16x16x32_bf16 v[128:131], a[8:11], v[236:239], v[128:131]\n"
    "ds_read_b64_tr_b16 a[6:7], v19 offset:63744\n"
    "v_add_f32 v12, 1.0, v12\n"
    "v_mfma_f32_16x16x32_bf16 v[132:135], a[8:11], a[92:95], v[132:135]\n"
    "buffer_load_dwordx4 v2, s[40:43], s59 offen offset:3072 lds\n"
    "v_add_f32 v13, 1.0, v13\n"
    "v_mfma_f32_16x16x32_bf16 v[136:139], a[8:11], a[164:167], v[136:139]\n"
    "v_add_f32 v14, 1.0, v14\n"
    "v_add_f32 v15, 1.0, v15\n"
    "v_mfma_f32_16x16x32_bf16 v[140:143], a[8:11], a[236:239], v[140:143]\n"
    "s_waitcnt lgkmcnt(7)\n"
    "v_rcp_f32 v12, v12\n"
    "v_mfma_f32_16x16x32_bf16 v[112:115], a[12:15], v[240:243], v[112:115]\n"
    "v_rcp_f32 v13, v13\n"
    "v_mfma_f32_16x16x32_bf16 v[116:119], a[12:15], a[96:99], v[116:119]\n"
    "v_rcp_f32 v14, v14\n"
    "v_mfma_f32_16x16x32_bf16 v[120:123], a[12:15], a[168:171], v[120:123]\n"
    "v_rcp_f32 v15, v15\n"
    "v_mfma_f32_16x16x32_bf16 v[124:127], a[12:15], a[240:243], v[124:127]\n"
    "s_waitcnt lgkmcnt(4)\n"
    "v_mul_f32 v84, v84, v12\n"
    "v_mul_f32 v85, v85, v13\n"
    "v_mfma_f32_16x16x32_bf16 v[128:131], a[36:39], v[240:243], v[128:131]\n"
    "v_mul_f32 v86, v86, v14\n"
    "v_mul_f32 v87, v87, v15\n"
    "v_mfma_f32_16x16x32_bf16 v[132:135], a[36:39], a[96:99], v[132:135]\n"
    "v_cvt_pk_bf16_f32 v52, v84, v85\n"
    "v_cvt_pk_bf16_f32 v53, v86, v87\n"
    "v_mfma_f32_16x16x32_bf16 v[136:139], a[36:39], a[168:171], v[136:139]\n"
    "v_exp_f32 v12, v68\n"
    "v_mfma_f32_16x16x32_bf16 v[140:143], a[36:39], a[240:243], v[140:143]\n"
    "s_waitcnt lgkmcnt(2)\n"
    "v_exp_f32 v13, v69\n"
    "v_mfma_f32_16x16x32_bf16 v[112:115], a[0:3], v[244:247], v[112:115]\n"
    "v_exp_f32 v14, v70\n"
    "v_mfma_f32_16x16x32_bf16 v[116:119], a[0:3], a[100:103], v[116:119]\n"
    "v_exp_f32 v15, v71\n"
    "v_mfma_f32_16x16x32_bf16 v[120:123], a[0:3], a[172:175], v[120:123]\n"
    "v_mul_f32 v100, v68, v100\n"
    "v_mul_f32 v101, v69, v101\n"
    "v_mfma_f32_16x16x32_bf16 v[124:127], a[0:3], a[244:247], v[124:127]\n"
    "s_waitcnt vmcnt(15) lgkmcnt(0)\n"
    "s_barrier\n"
    "ds_read_b64_tr_b16 a[8:9], v18 offset:40960\n"
    "ds_read_b64_tr_b16 a[10:11], v18 offset:41216\n"
    "v_mfma_f32_16x16x32_bf16 v[128:131], a[4:7], v[244:247], v[128:131]\n"
    "s_add_u32 m0, s56, 65536\n"
    "ds_read_b64_tr_b16 a[12:13], v18 offset:49152\n"
    "ds_read_b64_tr_b16 a[14:15], v18 offset:49408\n"
    "s_add_u32 s58, s64, 73984\n"
    "v_mfma_f32_16x16x32_bf16 v[132:135], a[4:7], a[100:103], v[132:135]\n"
    "ds_read_b64_tr_b16 a[36:37], v18 offset:43008\n"
    "v_mul_f32 v102, v70, v102\n"
    "v_mfma_f32_16x16x32_bf16 v[136:139], a[4:7], a[172:175], v[136:139]\n"
    "ds_read_b64_tr_b16 a[38:39], v18 offset:43264\n"
    "v_mul_f32 v103, v71, v103\n"
    "v_mfma_f32_16x16x32_bf16 v[140:143], a[4:7], a[244:247], v[140:143]\n"
    "s_waitcnt lgkmcnt(4)\n"
    "ds_read_b64_tr_b16 a[0:1], v18 offset:51200\n"
    "buffer_load_dwordx4 v2, s[40:43], s58 offen offset:0 lds\n"
    "v_mfma_f32_16x16x32_bf16 v[112:115], a[8:11], v[248:251], v[112:115]\n"
    "s_add_u32 s59, s64, 73088\n"
    "ds_read_b64_tr_b16 a[2:3], v18 offset:51456\n"
    "v_add_f32 v12, 1.0, v12\n"
    "v_mfma_f32_16x16x32_bf16 v[116:119], a[8:11], a[104:107], v[116:119]\n"
    "s_add_u32 s58, s64, s95\n"
    "buffer_load_dwordx4 v2, s[40:43], s59 offen offset:1024 lds\n"
    "s_add_u32 s58, s58, 71680\n"
    "v_mfma_f32_16x16x32_bf16 v[120:123], a[8:11], a[176:179], v[120:123]\n"
    "v_add_f32 v13, 1.0, v13\n"
    "v_add_f32 v14, 1.0, v14\n"
    "v_add_f32 v15, 1.0, v15\n"
    "v_mfma_f32_16x16x32_bf16 v[124:127], a[8:11], a[248:251], v[124:127]\n"
    "s_waitcnt lgkmcnt(4)\n"
    "buffer_load_dwordx4 v2, s[40:43], s58 offen offset:2048 lds\n"
    "s_add_u32 s59, s64, 107520\n"
    "v_mfma_f32_16x16x32_bf16 v[128:131], a[12:15], v[248:251], v[128:131]\n"
    "v_rcp_f32 v12, v12\n"
    "v_mfma_f32_16x16x32_bf16 v[132:135], a[12:15], a[104:107], v[132:135]\n"
    "buffer_load_dwordx4 v2, s[40:43], s59 offen offset:3072 lds\n"
    "v_mfma_f32_16x16x32_bf16 v[136:139], a[12:15], a[176:179], v[136:139]\n"
    "v_rcp_f32 v13, v13\n"
    "v_mfma_f32_16x16x32_bf16 v[140:143], a[12:15], a[248:251], v[140:143]\n"
    "s_waitcnt lgkmcnt(2)\n"
    "v_rcp_f32 v14, v14\n"
    "v_mfma_f32_16x16x32_bf16 v[112:115], a[36:39], v[252:255], v[112:115]\n"
    "v_rcp_f32 v15, v15\n"
    "v_mul_f32 v100, v100, v12\n"
    "v_mfma_f32_16x16x32_bf16 v[116:119], a[36:39], a[108:111], v[116:119]\n"
    "v_mul_f32 v101, v101, v13\n"
    "v_mul_f32 v102, v102, v14\n"
    "v_mfma_f32_16x16x32_bf16 v[120:123], a[36:39], a[180:183], v[120:123]\n"
    "v_mul_f32 v103, v103, v15\n"
    "v_cvt_pk_bf16_f32 v54, v100, v101\n"
    "v_mfma_f32_16x16x32_bf16 v[124:127], a[36:39], a[252:255], v[124:127]\n"
    "s_waitcnt vmcnt(14) lgkmcnt(0)\n"
    "s_barrier\n"
    "ds_read_b64_tr_b16 a[4:5], v22 offset:0\n"
    "ds_read_b64_tr_b16 a[6:7], v22 offset:8192\n"
    "v_mfma_f32_16x16x32_bf16 v[128:131], a[0:3], v[252:255], v[128:131]\n"
    "s_add_u32 m0, s56, 0\n"
    "ds_read_b64_tr_b16 a[8:9], v23 offset:0\n"
    "ds_read_b64_tr_b16 a[10:11], v23 offset:8192\n"
    "v_mfma_f32_16x16x32_bf16 v[132:135], a[0:3], a[108:111], v[132:135]\n"
    "s_add_u32 s58, s64, 110720\n"
    "ds_read_b64_tr_b16 a[12:13], v22 offset:1024\n"
    "v_cvt_pk_bf16_f32 v55, v102, v103\n"
    "v_mfma_f32_16x16x32_bf16 v[136:139], a[0:3], a[180:183], v[136:139]\n"
    "s_add_u32 s59, s64, 109824\n"
    "ds_read_b64_tr_b16 a[14:15], v22 offset:9216\n"
    "buffer_load_dwordx4 v2, s[40:43], s58 offen offset:0 lds\n"
    "v_mfma_f32_16x16x32_bf16 v[140:143], a[0:3], a[252:255], v[140:143]\n"
    "s_waitcnt lgkmcnt(4)\n"
    "ds_read_b64_tr_b16 a[36:37], v23 offset:1024\n"
    "v_mfma_f32_16x16x32_bf16 a[88:91], a[4:7], a[40:43], 0\n"
    "s_add_u32 s62, s92, 73728\n"
    "ds_read_b64_tr_b16 a[38:39], v23 offset:9216\n"
    "buffer_load_dword v36, v25, s[48:51], 0 offen offset:256 sc1\n"
    "v_mfma_f32_16x16x32_bf16 a[92:95], a[4:7], a[112:115], 0\n"
    "s_add_u32 s63, s93, 73728\n"
    "ds_read_b64_tr_b16 a[0:1], v22 offset:2048\n"
    "v_mfma_f32_16x16x32_bf16 a[96:99], a[4:7], a[184:187], 0\n"
    "ds_read_b64_tr_b16 a[2:3], v22 offset:10240\n"
    "v_mfma_f32_16x16x32_bf16 a[100:103], a[4:7], a[44:47], 0\n"
    "s_waitcnt lgkmcnt(6)\n"
    "ds_read_b64_tr_b16 a[4:5], v23 offset:2048\n"
    "buffer_load_dwordx4 v2, s[40:43], s59 offen offset:1024 lds\n"
    "v_mfma_f32_16x16x32_bf16 a[104:107], a[8:11], a[40:43], 0\n"
    "s_add_u32 s58, s64, 108928\n"
    "ds_read_b64_tr_b16 a[6:7], v23 offset:10240\n"
    "ds_write_b128 v10, v[52:55] offset:50176\n"
    "v_mfma_f32_16x16x32_bf16 a[108:111], a[8:11], a[112:115], 0\n"
    "buffer_store_dwordx4 v[52:55], v10, s[48:51], s60 offen offset:1024 sc1\n"
    "v_mfma_f32_16x16x32_bf16 a[160:163], a[8:11], a[184:187], 0\n"
    "v_exp_f32 v12, v56\n"
    "v_mfma_f32_16x16x32_bf16 a[164:167], a[8:11], a[44:47], 0\n"
    "s_waitcnt lgkmcnt(7)\n"
    "ds_read_b64_tr_b16 a[8:9], v22 offset:3072\n"
    "v_mfma_f32_16x16x32_bf16 v[184:187], a[12:15], a[40:43], 0\n"
    "s_add_u32 s59, s64, s95\n"
    "ds_read_b64_tr_b16 a[10:11], v22 offset:11264\n"
    "buffer_load_dwordx4 v2, s[40:43], s58 offen offset:2048 lds\n"
    "v_mfma_f32_16x16x32_bf16 v[188:191], a[12:15], a[112:115], 0\n"
    "s_add_u32 s59, s59, 107520\n"
    "v_exp_f32 v13, v57\n"
    "v_mfma_f32_16x16x32_bf16 v[192:195], a[12:15], a[184:187], 0\n"
    "v_exp_f32 v14, v58\n"
    "v_mfma_f32_16x16x32_bf16 v[196:199], a[12:15], a[44:47], 0\n"
    "s_waitcnt lgkmcnt(7)\n"
    "ds_read_b64_tr_b16 a[12:13], v23 offset:3072\n"
    "v_mfma_f32_16x16x32_bf16 v[200:203], a[36:39], a[40:43], 0\n"
    "ds_read_b64_tr_b16 a[14:15], v23 offset:11264\n"
    "v_mfma_f32_16x16x32_bf16 v[204:207], a[36:39], a[112:115], 0\n"
    "buffer_load_dwordx4 v2, s[40:43], s59 offen offset:3072 lds\n"
    "v_mfma_f32_16x16x32_bf16 v[208:211], a[36:39], a[184:187], 0\n"
    "v_exp_f32 v15, v59\n"
    "v_mul_f32 v88, v56, v88\n"
    "v_mfma_f32_16x16x32_bf16 v[212:215], a[36:39], a[44:47], 0\n"
    "s_waitcnt lgkmcnt(7)\n"
    "v_mul_f32 v89, v57, v89\n"
    "v_mul_f32 v90, v58, v90\n"
    "v_mfma_f32_16x16x32_bf16 v[216:219], a[0:3], a[40:43], 0\n"
    "v_mul_f32 v91, v59, v91\n"
    "v_add_f32 v12, 1.0, v12\n"
    "v_mfma_f32_16x16x32_bf16 v[220:223], a[0:3], a[112:115], 0\n"
    "v_add_f32 v13, 1.0, v13\n"
    "v_add_f32 v14, 1.0, v14\n"
    "v_mfma_f32_16x16x32_bf16 v[224:227], a[0:3], a[184:187], 0\n"
    "v_add_f32 v15, 1.0, v15\n"
    "v_mfma_f32_16x16x32_bf16 v[228:231], a[0:3], a[44:47], 0\n"
    "s_waitcnt lgkmcnt(5)\n"
    "v_rcp_f32 v12, v12\n"
    "v_mfma_f32_16x16x32_bf16 v[232:235], a[4:7], a[40:43], 0\n"
    "v_rcp_f32 v13, v13\n"
    "v_mfma_f32_16x16x32_bf16 v[236:239], a[4:7], a[112:115], 0\n"
    "v_rcp_f32 v14, v14\n"
    "v_mfma_f32_16x16x32_bf16 v[240:243], a[4:7], a[184:187], 0\n"
    "v_rcp_f32 v15, v15\n"
    "v_mfma_f32_16x16x32_bf16 v[244:247], a[4:7], a[44:47], 0\n"
    "s_waitcnt lgkmcnt(2)\n"
    "v_mul_f32 v88, v88, v12\n"
    "v_mul_f32 v89, v89, v13\n"
    "v_mfma_f32_16x16x32_bf16 v[144:147], a[8:11], a[40:43], 0\n"
    "v_mul_f32 v90, v90, v14\n"
    "v_mul_f32 v91, v91, v15\n"
    "v_cvt_pk_bf16_f32 v56, v88, v89\n"
    "v_mfma_f32_16x16x32_bf16 v[148:151], a[8:11], a[112:115], 0\n"
    "v_cvt_pk_bf16_f32 v57, v90, v91\n"
    "v_mfma_f32_16x16x32_bf16 v[152:155], a[8:11], a[184:187], 0\n"
    "v_exp_f32 v12, v72\n"
    "v_mfma_f32_16x16x32_bf16 v[156:159], a[8:11], a[44:47], 0\n"
    "s_waitcnt vmcnt(16) lgkmcnt(0)\n"
    "s_barrier\n"
    "ds_read_b64_tr_b16 a[252:253], v22 offset:16384\n"
    "ds_read_b64_tr_b16 a[254:255], v22 offset:24576\n"
    "v_mfma_f32_16x16x32_bf16 v[160:163], a[12:15], a[40:43], 0\n"
    "s_add_u32 m0, s56, 16384\n"
    "ds_read_b64_tr_b16 a[0:1], v23 offset:16384\n"
    "ds_read_b64_tr_b16 a[2:3], v23 offset:24576\n"
    "s_add_u32 s58, s64, 147456\n"
    "v_mfma_f32_16x16x32_bf16 v[164:167], a[12:15], a[112:115], 0\n"
    "ds_read_b64_tr_b16 a[4:5], v22 offset:17408\n"
    "v_mfma_f32_16x16x32_bf16 v[168:171], a[12:15], a[184:187], 0\n"
    "s_add_u32 s59, s64, 146560\n"
    "ds_read_b64_tr_b16 a[6:7], v22 offset:25600\n"
    "buffer_load_dwordx4 v2, s[40:43], s58 offen offset:0 lds\n"
    "v_mfma_f32_16x16x32_bf16 v[172:175], a[12:15], a[44:47], 0\n"
    "s_waitcnt lgkmcnt(4)\n"
    "ds_read_b64_tr_b16 a[8:9], v23 offset:17408\n"
    "v_mfma_f32_16x16x32_bf16 v[248:251], a[252:255], a[40:43], 0\n"
    "ds_read_b64_tr_b16 a[10:11], v23 offset:25600\n"
    "v_mfma_f32_16x16x32_bf16 v[252:255], a[252:255], a[112:115], 0\n"
    "ds_read_b64_tr_b16 a[12:13], v22 offset:18432\n"
    "v_exp_f32 v13, v73\n"
    "v_mfma_f32_16x16x32_bf16 v[28:31], a[252:255], a[184:187], 0\n"
    "ds_read_b64_tr_b16 a[14:15], v22 offset:26624\n"
    "v_mfma_f32_16x16x32_bf16 v[32:35], a[252:255], a[44:47], 0\n"
    "s_waitcnt lgkmcnt(6)\n"
    "ds_read_b64_tr_b16 a[252:253], v23 offset:18432\n"
    "buffer_load_dwordx4 v2, s[40:43], s59 offen offset:1024 lds\n"
    "v_mfma_f32_16x16x32_bf16 a[232:235], a[0:3], a[40:43], 0\n"
    "s_add_u32 s58, s64, 145664\n"
    "ds_read_b64_tr_b16 a[254:255], v23 offset:26624\n"
    "s_add_u32 s89, s27, 3\n"
    "v_mfma_f32_16x16x32_bf16 a[236:239], a[0:3], a[112:115], 0\n"
    "v_mov_b32 v38, 0x80000000\n"
    "v_writelane_b32 v38, s26, 0\n"
    "v_mov_b32 v39, s89\n"
    "v_mfma_f32_16x16x32_bf16 a[240:243], a[0:3], a[184:187], 0\n"
    "v_exp_f32 v14, v74\n"
    "v_mfma_f32_16x16x32_bf16 a[244:247], a[0:3], a[44:47], 0\n"
    "s_waitcnt lgkmcnt(6)\n"
    "ds_read_b64_tr_b16 a[0:1], v22 offset:19456\n"
    "v_mfma_f32_16x16x32_bf16 a[88:91], a[4:7], a[116:119], a[88:91]\n"
    "s_add_u32 s59, s64, 144768\n"
    "ds_read_b64_tr_b16 a[2:3], v22 offset:27648\n"
    "buffer_load_dwordx4 v2, s[40:43], s58 offen offset:2048 lds\n"
    "v_mfma_f32_16x16x32_bf16 a[92:95], a[4:7], a[188:191], a[92:95]\n"
    "v_exp_f32 v15, v75\n"
    "v_mul_f32 v104, v72, v104\n"
    "v_mfma_f32_16x16x32_bf16 a[96:99], a[4:7], a[48:51], a[96:99]\n"
    "v_mul_f32 v105, v73, v105\n"
    "v_mul_f32 v106, v74, v106\n"
    "v_mfma_f32_16x16x32_bf16 a[100:103], a[4:7], a[120:123], a[100:103]\n"
    "s_waitcnt lgkmcnt(6)\n"
    "ds_read_b64_tr_b16 a[4:5], v23 offset:19456\n"
    "buffer_store_dword v39, v38, s[48:51], 0 offen offset:0 sc1\n"
    "v_mfma_f32_16x16x32_bf16 a[104:107], a[8:11], a[116:119], a[104:107]\n"
    "ds_read_b64_tr_b16 a[6:7], v23 offset:27648\n"
    "v_mul_f32 v107, v75, v107\n"
    "v_mfma_f32_16x16x32_bf16 a[108:111], a[8:11], a[188:191], a[108:111]\n"
    "buffer_load_dwordx4 v2, s[40:43], s59 offen offset:3072 lds\n"
    "v_add_f32 v12, 1.0, v12\n"
    "v_add_f32 v13, 1.0, v13\n"
    "v_mfma_f32_16x16x32_bf16 a[160:163], a[8:11], a[48:51], a[160:163]\n"
    "v_add_f32 v14, 1.0, v14\n"
    "v_add_f32 v15, 1.0, v15\n"
    "v_mfma_f32_16x16x32_bf16 a[164:167], a[8:11], a[120:123], a[164:167]\n"
    "s_waitcnt lgkmcnt(6)\n"
    "v_rcp_f32 v12, v12\n"
    "v_mfma_f32_16x16x32_bf16 v[184:187], a[12:15], a[116:119], v[184:187]\n"
    "v_rcp_f32 v13, v13\n"
    "v_mfma_f32_16x16x32_bf16 v[188:191], a[12:15], a[188:191], v[188:191]\n"
    "v_rcp_f32 v14, v14\n"
    "v_mfma_f32_16x16x32_bf16 v[192:195], a[12:15], a[48:51], v[192:195]\n"
    "v_rcp_f32 v15, v15\n"
    "v_mfma_f32_16x16x32_bf16 v[196:199], a[12:15], a[120:123], v[196:199]\n"
    "s_waitcnt lgkmcnt(4)\n"
    "v_mul_f32 v104, v104, v12\n"
    "v_mul_f32 v105, v105, v13\n"
    "v_mfma_f32_16x16x32_bf16 v[200:203], a[252:255], a[116:119], v[200:203]\n"
    "v_mul_f32 v106, v106, v14\n"
    "v_mul_f32 v107, v107, v15\n"
    "v_cvt_pk_bf16_f32 v58, v104, v105\n"
    "v_mfma_f32_16x16x32_bf16 v[204:207], a[252:255], a[188:191], v[204:207]\n"
    "v_cvt_pk_bf16_f32 v59, v106, v107\n"
    "v_mfma_f32_16x16x32_bf16 v[208:211], a[252:255], a[48:51], v[208:211]\n"
    "v_exp_f32 v12, v60\n"
    "v_mfma_f32_16x16x32_bf16 v[212:215], a[252:255], a[120:123], v[212:215]\n"
    "s_waitcnt lgkmcnt(2)\n"
    "v_exp_f32 v13, v61\n"
    "v_mfma_f32_16x16x32_bf16 v[216:219], a[0:3], a[116:119], v[216:219]\n"
    "v_exp_f32 v14, v62\n"
    "v_mfma_f32_16x16x32_bf16 v[220:223], a[0:3], a[188:191], v[220:223]\n"
    "v_exp_f32 v15, v63\n"
    "v_mfma_f32_16x16x32_bf16 v[224:227], a[0:3], a[48:51], v[224:227]\n"
    "v_mul_f32 v92, v60, v92\n"
    "v_mul_f32 v93, v61, v93\n"
    "v_mfma_f32_16x16x32_bf16 v[228:231], a[0:3], a[120:123], v[228:231]\n"
    "s_waitcnt vmcnt(15) lgkmcnt(0)\n"
    "s_barrier\n"
    "ds_read_b64_tr_b16 a[8:9], v22 offset:32768\n"
    "ds_read_b64_tr_b16 a[10:11], v22 offset:40960\n"
    "v_mfma_f32_16x16x32_bf16 v[232:235], a[4:7], a[116:119], v[232:235]\n"
    "s_add_u32 m0, s56, 32768\n"
    "ds_read_b64_tr_b16 a[12:13], v23 offset:32768\n"
    "ds_read_b64_tr_b16 a[14:15], v23 offset:40960\n"
    "s_add_u32 s58, s64, s95\n"
    "v_mfma_f32_16x16x32_bf16 v[236:239], a[4:7], a[188:191], v[236:239]\n"
    "s_add_u32 s58, s58, 147456\n"
    "ds_read_b64_tr_b16 a[252:253], v22 offset:33792\n"
    "v_mul_f32 v94, v62, v94\n"
    "v_mfma_f32_16x16x32_bf16 v[240:243], a[4:7], a[48:51], v[240:243]\n"
    "s_add_u32 s59, s64, 183296\n"
    "ds_read_b64_tr_b16 a[254:255], v22 offset:41984\n"
    "buffer_load_dwordx4 v2, s[40:43], s58 offen offset:0 lds\n"
    "v_mfma_f32_16x16x32_bf16 v[244:247], a[4:7], a[120:123], v[244:247]\n"
    "s_waitcnt lgkmcnt(4)\n"
    "ds_read_b64_tr_b16 a[0:1], v23 offset:33792\n"
    "ds_write_b128 v11, v[56:59] offset:49152\n"
    "v_mfma_f32_16x16x32_bf16 v[144:147], a[8:11], a[116:119], v[144:147]\n"
    "ds_read_b64_tr_b16 a[2:3], v23 offset:41984\n"
    "buffer_store_dwordx4 v[56:59], v11, s[48:51], s60 offen offset:0 sc1\n"
    "v_mfma_f32_16x16x32_bf16 v[148:151], a[8:11], a[188:191], v[148:151]\n"
    "ds_read_b64_tr_b16 a[4:5], v22 offset:34816\n"
    "v_mul_f32 v95, v63, v95\n"
    "v_mfma_f32_16x16x32_bf16 v[152:155], a[8:11], a[48:51], v[152:155]\n"
    "ds_read_b64_tr_b16 a[6:7], v22 offset:43008\n"
    "v_add_f32 v12, 1.0, v12\n"
    "v_mfma_f32_16x16x32_bf16 v[156:159], a[8:11], a[120:123], v[156:159]\n"
    "s_waitcnt lgkmcnt(7)\n"
    "ds_read_b64_tr_b16 a[8:9], v23 offset:34816\n"
    "buffer_load_dwordx4 v2, s[40:43], s59 offen offset:1024 lds\n"
    "s_add_u32 s58, s64, 182400\n"
    "v_mfma_f32_16x16x32_bf16 v[160:163], a[12:15], a[116:119], v[160:163]\n"
    "ds_read_b64_tr_b16 a[10:11], v23 offset:43008\n"
    "v_add_f32 v13, 1.0, v13\n"
    "v_mfma_f32_16x16x32_bf16 v[164:167], a[12:15], a[188:191], v[164:167]\n"
    "v_add_f32 v14, 1.0, v14\n"
    "v_add_f32 v15, 1.0, v15\n"
    "v_mfma_f32_16x16x32_bf16 v[168:171], a[12:15], a[48:51], v[168:171]\n"
    "s_waitcnt vmcnt(12)\n"
    "v_sub_u32 v37, v36, v26\n"
    "v_subrev_u32 v37, 2, v37\n"
    "v_cmp_gt_u32 vcc, 32, v37\n"
    "s_nop 1\n"
    "s_cmp_eq_u64 vcc, -1\n"
    "s_cbranch_scc1 L_ok2\n"
    "s_mov_b32 s79, 100000\n"
    "L_sp2:\n"
    "s_sleep 2\n"
    "buffer_load_dword v36, v25, s[48:51], 0 offen offset:256 sc1\n"
    "s_waitcnt vmcnt(0)\n"
    "v_sub_u32 v37, v36, v26\n"
    "v_subrev_u32 v37, 2, v37\n"
    "v_cmp_gt_u32 vcc, 32, v37\n"
    "s_nop 1\n"
    "s_cmp_eq_u64 vcc, -1\n"
    "s_cbranch_scc1 L_ok2\n"
    "s_sub_u32 s79, s79, 1\n"
    "s_cmp_lg_u32 s79, 0\n"
    "s_cbranch_scc1 L_sp2\n"
    "L_ok2:\n"
    "v_mfma_f32_16x16x32_bf16 v[172:175], a[12:15], a[120:123], v[172:175]\n"
    "s_waitcnt lgkmcnt(7)\n"
    "ds_read_b64_tr_b16 a[12:13], v22 offset:35840\n"
    "v_rcp_f32 v12, v12\n"
    "v_mfma_f32_16x16x32_bf16 v[248:251], a[252:255], a[116:119], v[248:251]\n"
    "s_add_u32 s59, s64, 181504\n"
    "ds_read_b64_tr_b16 a[14:15], v22 offset:44032\n"
    "buffer_load_dwordx4 v2, s[40:43], s58 offen offset:2048 lds\n"
    "v_mfma_f32_16x16x32_bf16 v[252:255], a[252:255], a[188:191], v[252:255]\n"
    "s_add_u32 s61, s93, 0\n"
    "v_rcp_f32 v13, v13\n"
    "v_mfma_f32_16x16x32_bf16 v[28:31], a[252:255], a[48:51], v[28:31]\n"
    "v_rcp_f32 v14, v14\n"
    "v_mfma_f32_16x16x32_bf16 v[32:35], a[252:255], a[120:123], v[32:35]\n"
    "s_waitcnt lgkmcnt(6)\n"
    "ds_read_b64_tr_b16 a[252:253], v23 offset:35840\n"
    "v_rcp_f32 v15, v15\n"
    "v_mfma_f32_16x16x32_bf16 a[232:235], a[0:3], a[116:119], a[232:235]\n"
    "ds_read_b64_tr_b16 a[254:255], v23 offset:44032\n"
    "v_mul_f32 v92, v92, v12\n"
    "v_mfma_f32_16x16x32_bf16 a[236:239], a[0:3], a[188:191], a[236:239]\n"
    "buffer_load_dwordx4 v2, s[40:43], s59 offen offset:3072 lds\n"
    "v_mul_f32 v93, v93, v13\n"
    "v_mfma_f32_16x16x32_bf16 a[240:243], a[0:3], a[48:51], a[240:243]\n"
    "v_mul_f32 v94, v94, v14\n"
    "v_mul_f32 v95, v95, v15\n"
    "v_mfma_f32_16x16x32_bf16 a[244:247], a[0:3], a[120:123], a[244:247]\n"
    "s_waitcnt lgkmcnt(6)\n"
    "v_cvt_pk_bf16_f32 v60, v92, v93\n"
    "v_cvt_pk_bf16_f32 v61, v94, v95\n"
    "buffer_load_dwordx4 a[40:43], v40, s[48:51], s61 offen offset:0 sc1\n"
    "v_mfma_f32_16x16x32_bf16 a[88:91], a[4:7], a[192:195], a[88:91]\n"
    "v_exp_f32 v12, v76\n"
    "v_mfma_f32_16x16x32_bf16 a[92:95], a[4:7], a[52:55], a[92:95]\n"
    "v_exp_f32 v13, v77\n"
    "v_mfma_f32_16x16x32_bf16 a[96:99], a[4:7], a[124:127], a[96:99]\n"
    "v_exp_f32 v14, v78\n"
    "v_mfma_f32_16x16x32_bf16 a[100:103], a[4:7], a[196:199], a[100:103]\n"
    "s_waitcnt lgkmcnt(4)\n"
    "v_exp_f32 v15, v79\n"
    "v_mul_f32 v108, v76, v108\n"
    "v_mfma_f32_16x16x32_bf16 a[104:107], a[8:11], a[192:195], a[104:107]\n"
    "v_mul_f32 v109, v77, v109\n"
    "v_mul_f32 v110, v78, v110\n"
    "v_mfma_f32_16x16x32_bf16 a[108:111], a[8:11], a[52:55], a[108:111]\n"
    "v_mul_f32 v111, v79, v111\n"
    "v_add_f32 v12, 1.0, v12\n"
    "v_mfma_f32_16x16x32_bf16 a[160:163], a[8:11], a[124:127], a[160:163]\n"
    "v_add_f32 v13, 1.0, v13\n"
    "v_add_f32 v14, 1.0, v14\n"
    "v_mfma_f32_16x16x32_bf16 a[164:167], a[8:11], a[196:199], a[164:167]\n"
    "s_waitcnt lgkmcnt(2)\n"
    "v_add_f32 v15, 1.0, v15\n"
    "v_rcp_f32 v12, v12\n"
    "v_mfma_f32_16x16x32_bf16 v[184:187], a[12:15], a[192:195], v[184:187]\n"
    "v_rcp_f32 v13, v13\n"
    "v_mfma_f32_16x16x32_bf16 v[188:191], a[12:15], a[52:55], v[188:191]\n"
    "v_rcp_f32 v14, v14\n"
    "v_mfma_f32_16x16x32_bf16 v[192:195], a[12:15], a[124:127], v[192:195]\n"
    "v_rcp_f32 v15, v15\n"
    "v_mfma_f32_16x16x32_bf16 v[196:199], a[12:15], a[196:199], v[196:199]\n"
    "s_waitcnt lgkmcnt(0)\n"
    "s_barrier\n"
    "ds_read_b64_tr_b16 a[0:1], v22 offset:49152\n"
    "ds_read_b64_tr_b16 a[2:3], v22 offset:57344\n"
    "v_mfma_f32_16x16x32_bf16 v[200:203], a[252:255], a[192:195], v[200:203]\n"
    "s_add_u32 m0, s56, 49152\n"
    "ds_read_b64_tr_b16 a[4:5], v23 offset:49152\n"
    "ds_read_b64_tr_b16 a[6:7], v23 offset:57344\n"
    "v_mfma_f32_16x16x32_bf16 v[204:207], a[252:255], a[52:55], v[204:207]\n"
    "s_add_u32 s58, s64, 184704\n"
    "ds_read_b64_tr_b16 a[8:9], v22 offset:50176\n"
    "v_mul_f32 v108, v108, v12\n"
    "v_mfma_f32_16x16x32_bf16 v[208:211], a[252:255], a[124:127], v[208:211]\n"
    "s_add_u32 s59, s64, s95\n"
    "ds_read_b64_tr_b16 a[10:11], v22 offset:58368\n"
    "buffer_load_dwordx4 v2, s[40:43], s58 offen offset:0 lds\n"
    "v_mfma_f32_16x16x32_bf16 v[212:215], a[252:255], a[196:199], v[212:215]\n"
    "s_waitcnt lgkmcnt(4)\n"
    "ds_read_b64_tr_b16 a[12:13], v23 offset:50176\n"
    "s_add_u32 s59, s59, 183296\n"
    "v_mul_f32 v109, v109, v13\n"
    "v_mfma_f32_16x16x32_bf16 v[216:219], a[0:3], a[192:195], v[216:219]\n"
    "ds_read_b64_tr_b16 a[14:15], v23 offset:58368\n"
    "v_mul_f32 v110, v110, v14\n"
    "v_mfma_f32_16x16x32_bf16 v[220:223], a[0:3], a[52:55], v[220:223]\n"
    "ds_read_b64_tr_b16 a[252:253], v22 offset:51200\n"
    "v_mul_f32 v111, v111, v15\n"
    "v_mfma_f32_16x16x32_bf16 v[224:227], a[0:3], a[124:127], v[224:227]\n"
    "ds_read_b64_tr_b16 a[254:255], v22 offset:59392\n"
    "v_cvt_pk_bf16_f32 v62, v108, v109\n"
    "v_mfma_f32_16x16x32_bf16 v[228:231], a[0:3], a[196:199], v[228:231]\n"
    "s_waitcnt lgkmcnt(6)\n"
    "ds_read_b64_tr_b16 a[0:1], v23 offset:51200\n"
    "buffer_load_dwordx4 v2, s[40:43], s59 offen offset:1024 lds\n"
    "s_add_u32 s58, s64, 219136\n"
    "v_mfma_f32_16x16x32_bf16 v[232:235], a[4:7], a[192:195], v[232:235]\n"
    "ds_read_b64_tr_b16 a[2:3], v23 offset:59392\n"
    "v_cvt_pk_bf16_f32 v63, v110, v111\n"
    "v_mfma_f32_16x16x32_bf16 v[236:239], a[4:7], a[52:55], v[236:239]\n"
    "ds_write_b128 v11, v[60:63] offset:50176\n"
    "v_mfma_f32_16x16x32_bf16 v[240:243], a[4:7], a[124:127], v[240:243]\n"
    "buffer_store_dwordx4 v[60:63], v11, s[48:51], s60 offen offset:1024 sc1\n"
    "v_mfma_f32_16x16x32_bf16 v[244:247], a[4:7], a[196:199], v[244:247]\n"
    "s_waitcnt lgkmcnt(7)\n"
    "ds_read_b64_tr_b16 a[4:5], v22 offset:52224\n"
    "v_exp_f32 v12, v112\n"
    "v_mfma_f32_16x16x32_bf16 v[144:147], a[8:11], a[192:195], v[144:147]\n"
    "s_add_u32 s59, s64, 218240\n"
    "ds_read_b64_tr_b16 a[6:7], v22 offset:60416\n"
    "buffer_load_dwordx4 v2, s[40:43], s58 offen offset:2048 lds\n"
    "v_mfma_f32_16x16x32_bf16 v[148:151], a[8:11], a[52:55], v[148:151]\n"
    "v_exp_f32 v13, v113\n"
    "v_mfma_f32_16x16x32_bf16 v[152:155], a[8:11], a[124:127], v[152:155]\n"
    "v_exp_f32 v14, v114\n"
    "v_mfma_f32_16x16x32_bf16 v[156:159], a[8:11], a[196:199], v[156:159]\n"
    "s_waitcnt lgkmcnt(7)\n"
    "ds_read_b64_tr_b16 a[8:9], v23 offset:52224\n"
    "v_exp_f32 v15, v115\n"
    "v_mfma_f32_16x16x32_bf16 v[160:163], a[12:15], a[192:195], v[160:163]\n"
    "ds_read_b64_tr_b16 a[10:11], v23 offset:60416\n"
    "v_mul_f32 v128, v112, v128\n"
    "v_mfma_f32_16x16x32_bf16 v[164:167], a[12:15], a[52:55], v[164:167]\n"
    "buffer_load_dwordx4 v2, s[40:43], s59 offen offset:3072 lds\n"
    "v_mul_f32 v129, v113, v129\n"
    "v_mfma_f32_16x16x32_bf16 v[168:171], a[12:15], a[124:127], v[168:171]\n"
    "v_mul_f32 v130, v114, v130\n"
    "v_mul_f32 v131, v115, v131\n"
    "v_mfma_f32_16x16x32_bf16 v[172:175], a[12:15], a[196:199], v[172:175]\n"
    "s_waitcnt lgkmcnt(7)\n"
    "v_add_f32 v12, 1.0, v12\n"
    "v_add_f32 v13, 1.0, v13\n"
    "v_add_f32 v14, 1.0, v14\n"
    "v_mfma_f32_16x16x32_bf16 v[248:251], a[252:255], a[192:195], v[248:251]\n"
    "v_add_f32 v15, 1.0, v15\n"
    "buffer_load_dwordx4 a[112:115], v40, s[48:51], s61 offen offset:1024 sc1\n"
    "v_mfma_f32_16x16x32_bf16 v[252:255], a[252:255], a[52:55], v[252:255]\n"
    "v_rcp_f32 v12, v12\n"
    "v_mfma_f32_16x16x32_bf16 v[28:31], a[252:255], a[124:127], v[28:31]\n"
    "v_rcp_f32 v13, v13\n"
    "v_mfma_f32_16x16x32_bf16 v[32:35], a[252:255], a[196:199], v[32:35]\n"
    "s_waitcnt lgkmcnt(5)\n"
    "v_rcp_f32 v14, v14\n"
    "buffer_load_dwordx4 a[184:187], v41, s[48:51], s61 offen offset:0 sc1\n"
    "v_mfma_f32_16x16x32_bf16 a[232:235], a[0:3], a[192:195], a[232:235]\n"
    "v_rcp_f32 v15, v15\n"
    "v_mfma_f32_16x16x32_bf16 a[236:239], a[0:3], a[52:55], a[236:239]\n"
    "v_mul_f32 v128, v128, v12\n"
    "v_mul_f32 v129, v129, v13\n"
    "v_mfma_f32_16x16x32_bf16 a[240:243], a[0:3], a[124:127], a[240:243]\n"
    "v_mul_f32 v130, v130, v14\n"
    "v_mul_f32 v131, v131, v15\n"
    "v_cvt_pk_bf16_f32 v112, v128, v129\n"
    "v_mfma_f32_16x16x32_bf16 a[244:247], a[0:3], a[196:199], a[244:247]\n"
    "s_waitcnt lgkmcnt(2)\n"
    "v_cvt_pk_bf16_f32 v113, v130, v131\n"
    "buffer_load_dwordx4 a[44:47], v41, s[48:51], s61 offen offset:1024 sc1\n"
    "v_mfma_f32_16x16x32_bf16 a[88:91], a[4:7], a[56:59], a[88:91]\n"
    "s_add_u32 s61, s93, 8192\n"
    "v_exp_f32 v12, v116\n"
    "v_mfma_f32_16x16x32_bf16 a[92:95], a[4:7], a[128:131], a[92:95]\n"
    "v_exp_f32 v13, v117\n"
    "buffer_load_dwordx4 a[116:119], v40, s[48:51], s61 offen offset:0 sc1\n"
    "v_mfma_f32_16x16x32_bf16 a[96:99], a[4:7], a[200:203], a[96:99]\n"
    "v_exp_f32 v14, v118\n"
    "v_mfma_f32_16x16x32_bf16 a[100:103], a[4:7], a[60:63], a[100:103]\n"
    "s_waitcnt vmcnt(20) lgkmcnt(0)\n"
    "s_barrier\n"
    "ds_read_b64_tr_b16 a[12:13], v20 offset:40960\n"
    "ds_read_b64_tr_b16 a[14:15], v20 offset:49152\n"
    "v_mfma_f32_16x16x32_bf16 a[104:107], a[8:11], a[56:59], a[104:107]\n"
    "s_add_u32 m0, s56, 65536\n"
    "ds_read_b64_tr_b16 a[252:253], v21 offset:40960\n"
    "ds_read_b64_tr_b16 a[254:255], v21 offset:49152\n"
    "v_mfma_f32_16x16x32_bf16 a[108:111], a[8:11], a[128:131], a[108:111]\n"
    "s_add_u32 s58, s64, 221440\n"
    "ds_read_b64_tr_b16 a[0:1], v20 offset:41984\n"
    "ds_write_b64 v176, v[112:113] offset:0\n"
    "v_mfma_f32_16x16x32_bf16 a[160:163], a[8:11], a[200:203], a[160:163]\n"
    "s_add_u32 s59, s64, 220544\n"
    "ds_read_b64_tr_b16 a[2:3], v20 offset:50176\n"
    "buffer_load_dwordx4 v2, s[40:43], s58 offen offset:0 lds\n"
    "v_mfma_f32_16x16x32_bf16 a[164:167], a[8:11], a[60:63], a[164:167]\n"
    "s_waitcnt lgkmcnt(5)\n"
    "ds_read_b64_tr_b16 a[4:5], v21 offset:41984\n"
    "buffer_load_dwordx4 a[188:191], v40, s[48:51], s61 offen offset:1024 sc1\n"
    "v_mfma_f32_16x16x32_bf16 v[184:187], a[12:15], a[56:59], v[184:187]\n"
    "ds_read_b64_tr_b16 a[6:7], v21 offset:50176\n"
    "v_exp_f32 v15, v119\n"
    "v_mfma_f32_16x16x32_bf16 v[188:191], a[12:15], a[128:131], v[188:191]\n"
    "ds_read_b64_tr_b16 a[8:9], v20 offset:43008\n"
    "v_mul_f32 v132, v116, v132\n"
    "v_mfma_f32_16x16x32_bf16 v[192:195], a[12:15], a[200:203], v[192:195]\n"
    "ds_read_b64_tr_b16 a[10:11], v20 offset:51200\n"
    "v_mul_f32 v133, v117, v133\n"
    "v_mfma_f32_16x16x32_bf16 v[196:199], a[12:15], a[60:63], v[196:199]\n"
    "s_waitcnt lgkmcnt(7)\n"
    "ds_read_b64_tr_b16 a[12:13], v21 offset:43008\n"
    "buffer_load_dwordx4 v2, s[40:43], s59 offen offset:1024 lds\n"
    "s_add_u32 s58, s64, s95\n"
    "v_mfma_f32_16x16x32_bf16 v[200:203], a[252:255], a[56:59], v[200:203]\n"
    "s_add_u32 s58, s58, 219136\n"
    "ds_read_b64_tr_b16 a[14:15], v21 offset:51200\n"
    "v_mul_f32 v134, v118, v134\n"
    "v_mfma_f32_16x16x32_bf16 v[204:207], a[252:255], a[128:131], v[204:207]\n"
    "v_mul_f32 v135, v119, v135\n"
    "v_add_f32 v12, 1.0, v12\n"
    "v_mfma_f32_16x16x32_bf16 v[208:211], a[252:255], a[200:203], v[208:211]\n"
    "v_add_f32 v13, 1.0, v13\n"
    "v_add_f32 v14, 1.0, v14\n"
    "v_add_f32 v15, 1.0, v15\n"
    "v_mfma_f32_16x16x32_bf16 v[212:215], a[252:255], a[60:63], v[212:215]\n"
    "s_waitcnt lgkmcnt(6)\n"
    "ds_read_b64_tr_b16 a[252:253], v20 offset:44032\n"
    "buffer_load_dwordx4 a[48:51], v41, s[48:51], s61 offen offset:0 sc1\n"
    "v_mfma_f32_16x16x32_bf16 v[216:219], a[0:3], a[56:59], v[216:219]\n"
    "s_add_u32 s59, s64, 254976\n"
    "ds_read_b64_tr_b16 a[254:255], v20 offset:52224\n"
    "buffer_load_dwordx4 v2, s[40:43], s58 offen offset:2048 lds\n"
    "v_mfma_f32_16x16x32_bf16 v[220:223], a[0:3], a[128:131], v[220:223]\n"
    "v_rcp_f32 v12, v12\n"
    "v_mfma_f32_16x16x32_bf16 v[224:227], a[0:3], a[200:203], v[224:227]\n"
    "v_rcp_f32 v13, v13\n"
    "v_mfma_f32_16x16x32_bf16 v[228:231], a[0:3], a[60:63], v[228:231]\n"
    "s_waitcnt lgkmcnt(6)\n"
    "ds_read_b64_tr_b16 a[0:1], v21 offset:44032\n"
    "v_mfma_f32_16x16x32_bf16 v[232:235], a[4:7], a[56:59], v[232:235]\n"
    "L_hop10:\n"
    "s_cbranch_execz L_back0\n"
    "ds_read_b64_tr_b16 a[2:3], v21 offset:52224\n"
    "v_rcp_f32 v14, v14\n"
    "v_mfma_f32_16x16x32_bf16 v[236:239], a[4:7], a[128:131], v[236:239]\n"
    "buffer_load_dwordx4 v2, s[40:43], s59 offen offset:3072 lds\n"
    "v_mfma_f32_16x16x32_bf16 v[240:243], a[4:7], a[200:203], v[240:243]\n"
    "v_rcp_f32 v15, v15\n"
    "v_mfma_f32_16x16x32_bf16 v[244:247], a[4:7], a[60:63], v[244:247]\n"
    "s_waitcnt lgkmcnt(6)\n"
    "v_mul_f32 v132, v132, v12\n"
    "v_mul_f32 v133, v133, v13\n"
    "v_mul_f32 v134, v134, v14\n"
    "v_mfma_f32_16x16x32_bf16 v[144:147], a[8:11], a[56:59], v[144:147]\n"
    "v_mul_f32 v135, v135, v15\n"
    "v_cvt_pk_bf16_f32 v116, v132, v133\n"
    "v_mfma_f32_16x16x32_bf16 v[148:151], a[8:11], a[128:131], v[148:151]\n"
    "v_cvt_pk_bf16_f32 v117, v134, v135\n"
    "ds_write_b64 v176, v[116:117] offset:1024\n"
    "v_mfma_f32_16x16x32_bf16 v[152:155], a[8:11], a[200:203], v[152:155]\n"
    "v_exp_f32 v12, v120\n"
    "v_mfma_f32_16x16x32_bf16 v[156:159], a[8:11], a[60:63], v[156:159]\n"
    "s_waitcnt lgkmcnt(5)\n"
    "v_exp_f32 v13, v121\n"
    "v_mfma_f32_16x16x32_bf16 v[160:163], a[12:15], a[56:59], v[160:163]\n"
    "v_exp_f32 v14, v122\n"
    "buffer_load_dwordx4 a[120:123], v41, s[48:51], s61 offen offset:1024 sc1\n"
    "s_add_u32 s61, s93, 16384\n"
    "v_mfma_f32_16x16x32_bf16 v[164:167], a[12:15], a[128:131], v[164:167]\n"
    "v_exp_f32 v15, v123\n"
    "v_mfma_f32_16x16x32_bf16 v[168:171], a[12:15], a[200:203], v[168:171]\n"
    "v_mul_f32 v136, v120, v136\n"
    "v_mul_f32 v137, v121, v137\n"
    "v_mul_f32 v138, v122, v138\n"
    "v_mfma_f32_16x16x32_bf16 v[172:175], a[12:15], a[60:63], v[172:175]\n"
    "s_waitcnt lgkmcnt(3)\n"
    "v_mul_f32 v139, v123, v139\n"
    "v_add_f32 v12, 1.0, v12\n"
    "v_mfma_f32_16x16x32_bf16 v[248:251], a[252:255], a[56:59], v[248:251]\n"
    "v_add_f32 v13, 1.0, v13\n"
    "v_add_f32 v14, 1.0, v14\n"
    "v_mfma_f32_16x16x32_bf16 v[252:255], a[252:255], a[128:131], v[252:255]\n"
    "v_add_f32 v15, 1.0, v15\n"
    "v_rcp_f32 v12, v12\n"
    "v_mfma_f32_16x16x32_bf16 v[28:31], a[252:255], a[200:203], v[28:31]\n"
    "v_rcp_f32 v13, v13\n"
    "v_mfma_f32_16x16x32_bf16 v[32:35], a[252:255], a[60:63], v[32:35]\n"
    "s_waitcnt vmcnt(22) lgkmcnt(0)\n"
    "s_barrier\n"
    "ds_read_b64_tr_b16 a[4:5], v22 offset:0\n"
    "ds_read_b64_tr_b16 a[6:7], v22 offset:8192\n"
    "v_mfma_f32_16x16x32_bf16 a[232:235], a[0:3], a[56:59], a[232:235]\n"
    "s_add_u32 m0, s56, 0\n"
    "ds_read_b64_tr_b16 a[8:9], v23 offset:0\n"
    "ds_read_b64_tr_b16 a[10:11], v23 offset:8192\n"
    "v_mfma_f32_16x16x32_bf16 a[236:239], a[0:3], a[128:131], a[236:239]\n"
    "s_add_u32 s58, s64, 258176\n"
    "ds_read_b64_tr_b16 a[12:13], v22 offset:1024\n"
    "ds_read_b128 a[72:75], v40 offset:49152\n"
    "ds_read_b128 a[144:147], v40 offset:50176\n"
    "v_mfma_f32_16x16x32_bf16 a[240:243], a[0:3], a[200:203], a[240:243]\n"
    "s_add_u32 s59, s64, 257280\n"
    "ds_read_b64_tr_b16 a[14:15], v22 offset:9216\n"
    "buffer_load_dwordx4 v2, s[40:43], s58 offen offset:0 lds\n"
    "v_mfma_f32_16x16x32_bf16 a[244:247], a[0:3], a[60:63], a[244:247]\n"
    "s_waitcnt lgkmcnt(6)\n"
    "ds_read_b64_tr_b16 a[252:253], v23 offset:1024\n"
    "ds_read_b128 a[216:219], v41 offset:49152\n"
    "ds_read_b128 a[76:79], v41 offset:50176\n"
    "v_mfma_f32_16x16x32_bf16 a[88:91], a[4:7], a[132:135], a[88:91]\n"
    "ds_read_b64_tr_b16 a[254:255], v23 offset:9216\n"
    "ds_read_b128 a[148:151], v40 offset:57344\n"
    "v_mfma_f32_16x16x32_bf16 a[92:95], a[4:7], a[204:207], a[92:95]\n"
    "ds_read_b64_tr_b16 a[0:1], v22 offset:2048\n"
    "ds_read_b128 a[220:223], v40 offset:58368\n"
    "ds_read_b128 a[80:83], v41 offset:57344\n"
    "v_mfma_f32_16x16x32_bf16 a[96:99], a[4:7], a[64:67], a[96:99]\n"
    "ds_read_b64_tr_b16 a[2:3], v22 offset:10240\n"
    "s_waitcnt lgkmcnt(14)\n"
    "ds_read_b128 a[152:155], v41 offset:58368\n"
    "buffer_load_dwordx4 a[192:195], v40, s[48:51], s61 offen offset:0 sc1\n"
    "v_mfma_f32_16x16x32_bf16 a[100:103], a[4:7], a[136:139], a[100:103]\n"
    "s_waitcnt lgkmcnt(14)\n"
    "ds_read_b64_tr_b16 a[4:5], v23 offset:2048\n"
    "buffer_load_dwordx4 v2, s[40:43], s59 offen offset:1024 lds\n"
    "s_add_u32 s58, s64, 256384\n"
    "v_mfma_f32_16x16x32_bf16 a[104:107], a[8:11], a[132:135], a[104:107]\n"
    "s_waitcnt lgkmcnt(14)\n"
    "ds_read_b64_tr_b16 a[6:7], v23 offset:10240\n"
    "v_rcp_f32 v14, v14\n"
    "v_mfma_f32_16x16x32_bf16 a[108:111], a[8:11], a[204:207], a[108:111]\n"
    "v_rcp_f32 v15, v15\n"
    "v_mul_f32 v136, v136, v12\n"
    "v_mfma_f32_16x16x32_bf16 a[160:163], a[8:11], a[64:67], a[160:163]\n"
    "v_mul_f32 v137, v137, v13\n"
    "v_mul_f32 v138, v138, v14\n"
    "v_mul_f32 v139, v139, v15\n"
    "v_mfma_f32_16x16x32_bf16 a[164:167], a[8:11], a[136:139], a[164:167]\n"
    "s_waitcnt lgkmcnt(14)\n"
    "ds_read_b64_tr_b16 a[8:9], v22 offset:3072\n"
    "v_cvt_pk_bf16_f32 v120, v136, v137\n"
    "v_cvt_pk_bf16_f32 v121, v138, v139\n"
    "s_waitcnt lgkmcnt(13)\n"
    "v_mfma_f32_16x16x32_bf16 v[184:187], a[12:15], a[132:135], v[184:187]\n"
    "s_add_u32 s59, s64, s95\n"
    "ds_read_b64_tr_b16 a[10:11], v22 offset:11264\n"
    "buffer_load_dwordx4 v2, s[40:43], s58 offen offset:2048 lds\n"
    "s_add_u32 s59, s59, 254976\n"
    "v_mfma_f32_16x16x32_bf16 v[188:191], a[12:15], a[204:207], v[188:191]\n"
    "ds_write_b64 v177, v[120:121] offset:0\n"
    "v_mfma_f32_16x16x32_bf16 v[192:195], a[12:15], a[64:67], v[192:195]\n"
    "v_exp_f32 v12, v124\n"
    "v_exp_f32 v13, v125\n"
    "v_mfma_f32_16x16x32_bf16 v[196:199], a[12:15], a[136:139], v[196:199]\n"
    "s_waitcnt lgkmcnt(14)\n"
    "ds_read_b64_tr_b16 a[12:13], v23 offset:3072\n"
    "v_exp_f32 v14, v126\n"
    "buffer_load_dwordx4 a[52:55], v40, s[48:51], s61 offen offset:1024 sc1\n"
    "s_waitcnt lgkmcnt(12)\n"
    "v_mfma_f32_16x16x32_bf16 v[200:203], a[252:255], a[132:135], v[200:203]\n"
    "ds_read_b64_tr_b16 a[14:15], v23 offset:11264\n"
    "v_exp_f32 v15, v127\n"
    "v_mul_f32 v140, v124, v140\n"
    "v_mfma_f32_16x16x32_bf16 v[204:207], a[252:255], a[204:207], v[204:207]\n"
    "buffer_load_dwordx4 v2, s[40:43], s59 offen offset:3072 lds\n"
    "v_mul_f32 v141, v125, v141\n"
    "v_mul_f32 v142, v126, v142\n"
    "v_mul_f32 v143, v127, v143\n"
    "v_mfma_f32_16x16x32_bf16 v[208:211], a[252:255], a[64:67], v[208:211]\n"
    "v_add_f32 v12, 1.0, v12\n"
    "v_add_f32 v13, 1.0, v13\n"
    "v_add_f32 v14, 1.0, v14\n"
    "v_add_f32 v15, 1.0, v15\n"
    "v_mfma_f32_16x16x32_bf16 v[212:215], a[252:255], a[136:139], v[212:215]\n"
    "s_waitcnt lgkmcnt(8)\n"
    "v_rcp_f32 v12, v12\n"
    "v_rcp_f32 v13, v13\n"
    "v_mfma_f32_16x16x32_bf16 v[216:219], a[0:3], a[132:135], v[216:219]\n"
    "v_rcp_f32 v14, v14\n"
    "v_rcp_f32 v15, v15\n"
    "v_mul_f32 v140, v140, v12\n"
    "v_mfma_f32_16x16x32_bf16 v[220:223], a[0:3], a[204:207], v[220:223]\n"
    "v_mul_f32 v141, v141, v13\n"
    "v_mul_f32 v142, v142, v14\n"
    "v_mul_f32 v143, v143, v15\n"
    "v_cvt_pk_bf16_f32 v124, v140, v141\n"
    "v_cvt_pk_bf16_f32 v125, v142, v143\n"
    "v_mfma_f32_16x16x32_bf16 v[224:227], a[0:3], a[64:67], v[224:227]\n"
    "ds_write_b64 v177, v[124:125] offset:1024\n"
    "v_mfma_f32_16x16x32_bf16 v[228:231], a[0:3], a[136:139], v[228:231]\n"
    "s_waitcnt lgkmcnt(6)\n"
    "buffer_load_dwordx4 a[124:127], v41, s[48:51], s61 offen offset:0 sc1\n"
    "v_mfma_f32_16x16x32_bf16 v[232:235], a[4:7], a[132:135], v[232:235]\n"
    "buffer_load_dword v36, v25, s[48:51], 0 offen offset:256 sc1\n"
    "v_mfma_f32_16x16x32_bf16 v[236:239], a[4:7], a[204:207], v[236:239]\n"
    "buffer_load_dwordx4 a[196:199], v41, s[48:51], s61 offen offset:1024 sc1\n"
    "s_add_u32 s61, s93, 24576\n"
    "v_mfma_f32_16x16x32_bf16 v[240:243], a[4:7], a[64:67], v[240:243]\n"
    "v_mfma_f32_16x16x32_bf16 v[244:247], a[4:7], a[136:139], v[244:247]\n"
    "s_waitcnt lgkmcnt(4)\n"
    "v_mfma_f32_16x16x32_bf16 v[144:147], a[8:11], a[132:135], v[144:147]\n"
    "buffer_load_dwordx4 a[56:59], v40, s[48:51], s61 offen offset:0 sc1\n"
    "v_mfma_f32_16x16x32_bf16 v[148:151], a[8:11], a[204:207], v[148:151]\n"
    "v_mfma_f32_16x16x32_bf16 v[152:155], a[8:11], a[64:67], v[152:155]\n"
    "v_mfma_f32_16x16x32_bf16 v[156:159], a[8:11], a[136:139], v[156:159]\n"
    "s_waitcnt vmcnt(27) lgkmcnt(0)\n"
    "s_barrier\n"
    "ds_read_b64_tr_b16 a[252:253], v22 offset:16384\n"
    "ds_read_b64_tr_b16 a[254:255], v22 offset:24576\n"
    "v_mfma_f32_16x16x32_bf16 v[160:163], a[12:15], a[132:135], v[160:163]\n"
    "s_add_u32 m0, s56, 16384\n"
    "ds_read_b64_tr_b16 a[0:1], v23 offset:16384\n"
    "ds_read_b64_tr_b16 a[2:3], v23 offset:24576\n"
    "v_mfma_f32_16x16x32_bf16 v[164:167], a[12:15], a[204:207], v[164:167]\n"
    "s_add_u32 s58, s65, 0\n"
    "ds_read_b64_tr_b16 a[4:5], v22 offset:17408\n"
    "ds_read_b128 a[224:227], v42 offset:0\n"
    "v_mfma_f32_16x16x32_bf16 v[168:171], a[12:15], a[64:67], v[168:171]\n"
    "s_sub_u32 s59, s65, 896\n"
    "ds_read_b64_tr_b16 a[6:7], v22 offset:25600\n"
    "buffer_load_dwordx4 v2, s[40:43], s58 offen offset:0 lds\n"
    "v_mfma_f32_16x16x32_bf16 v[172:175], a[12:15], a[136:139], v[172:175]\n"
    "s_waitcnt lgkmcnt(5)\n"
    "ds_read_b64_tr_b16 a[8:9], v23 offset:17408\n"
    "ds_read_b128 a[84:87], v42 offset:1024\n"
    "v_mfma_f32_16x16x32_bf16 v[248:251], a[252:255], a[132:135], v[248:251]\n"
    "s_and_b32 s86, s22, 1\n"
    "ds_read_b64_tr_b16 a[10:11], v23 offset:25600\n"
    "ds_read_b128 a[156:159], v43 offset:0\n"
    "v_mfma_f32_16x16x32_bf16 v[252:255], a[252:255], a[204:207], v[252:255]\n"
    "s_lshr_b32 s88, s22, 2\n"
    "ds_read_b64_tr_b16 a[12:13], v22 offset:18432\n"
    "ds_read_b128 a[228:231], v43 offset:1024\n"
    "v_mfma_f32_16x16x32_bf16 v[28:31], a[252:255], a[64:67], v[28:31]\n"
    "s_mul_i32 s87, s23, 1310720\n"
    "ds_read_b64_tr_b16 a[14:15], v22 offset:26624\n"
    "buffer_load_dwordx4 a[128:131], v40, s[48:51], s61 offen offset:1024 sc1\n"
    "v_mfma_f32_16x16x32_bf16 v[32:35], a[252:255], a[136:139], v[32:35]\n"
    "s_waitcnt lgkmcnt(10)\n"
    "ds_read_b64_tr_b16 a[252:253], v23 offset:18432\n"
    "buffer_load_dwordx4 v2, s[40:43], s59 offen offset:1024 lds\n"
    "v_mfma_f32_16x16x32_bf16 a[232:235], a[0:3], a[132:135], a[232:235]\n"
    "s_sub_u32 s58, s65, 1792\n"
    "ds_read_b64_tr_b16 a[254:255], v23 offset:26624\n"
    "s_mul_i32 s33, s88, 20480\n"
    "v_mfma_f32_16x16x32_bf16 a[236:239], a[0:3], a[204:207], a[236:239]\n"
    "s_add_u32 s33, s33, s87\n"
    "s_add_u32 s33, s33, 23855104\n"
    "s_mul_i32 s32, s86, 81920\n"
    "v_mfma_f32_16x16x32_bf16 a[240:243], a[0:3], a[64:67], a[240:243]\n"
    "s_add_u32 s32, s32, s33\n"
    "buffer_load_dwordx4 a[200:203], v41, s[48:51], s61 offen offset:0 sc1\n"
    "s_bfe_u32 s87, s22, 0x10001\n"
    "v_mfma_f32_16x16x32_bf16 a[244:247], a[0:3], a[136:139], a[244:247]\n"
    "s_waitcnt lgkmcnt(9)\n"
    "ds_read_b64_tr_b16 a[0:1], v22 offset:19456\n"
    "s_lshl_b32 s34, s87, 11\n"
    "v_mfma_f32_16x16x32_bf16 a[88:91], a[4:7], a[208:211], a[88:91]\n"
    "s_sub_u32 s59, s65, 2688\n"
    "ds_read_b64_tr_b16 a[2:3], v22 offset:27648\n"
    "buffer_load_dwordx4 v2, s[40:43], s58 offen offset:2048 lds\n"
    "v_mfma_f32_16x16x32_bf16 a[92:95], a[4:7], a[68:71], a[92:95]\n"
    "s_add_u32 s32, s32, s34\n"
    "s_add_u32 s33, s33, s34\n"
    "s_lshl_b32 s34, s24, 10\n"
    "v_mfma_f32_16x16x32_bf16 a[96:99], a[4:7], a[140:143], a[96:99]\n"
    "s_add_u32 s32, s32, s34\n"
    "s_lshl_b32 s34, s25, 7\n"
    "s_add_u32 s32, s32, s34\n"
    "v_mfma_f32_16x16x32_bf16 a[100:103], a[4:7], a[212:215], a[100:103]\n"
    "s_waitcnt lgkmcnt(8)\n"
    "ds_read_b64_tr_b16 a[4:5], v23 offset:19456\n"
    "s_lshl_b32 s34, s86, 10\n"
    "v_mfma_f32_16x16x32_bf16 a[104:107], a[8:11], a[208:211], a[104:107]\n"
    "s_add_u32 s33, s33, s34\n"
    "ds_read_b64_tr_b16 a[6:7], v23 offset:27648\n"
    "s_lshl_b32 s34, s21, 6\n"
    "v_mfma_f32_16x16x32_bf16 a[108:111], a[8:11], a[68:71], a[108:111]\n"
    "s_add_u32 s33, s33, s34\n"
    "buffer_load_dwordx4 v2, s[40:43], s59 offen offset:3072 lds\n"
    "s_add_u32 s33, s33, 16384\n"
    "v_mfma_f32_16x16x32_bf16 a[160:163], a[8:11], a[140:143], a[160:163]\n"
    "s_xor_b32 s87, s87, s88\n"
    "s_lshl_b32 s87, s87, 5\n"
    "s_lshl_b32 s34, s23, 7\n"
    "v_mfma_f32_16x16x32_bf16 a[164:167], a[8:11], a[212:215], a[164:167]\n"
    "s_waitcnt lgkmcnt(6)\n"
    "s_add_u32 s87, s87, s34\n"
    "s_lshl_b32 s34, s21, 3\n"
    "v_mfma_f32_16x16x32_bf16 v[184:187], a[12:15], a[208:211], v[184:187]\n"
    "s_add_u32 s34, s34, s87\n"
    "buffer_store_dwordx4 a[224:227], v42, s[48:51], s92 offen offset:0 sc1\n"
    "s_mul_i32 s34, s34, 1152\n"
    "v_mfma_f32_16x16x32_bf16 v[188:191], a[12:15], a[68:71], v[188:191]\n"
    "s_lshl_b32 s35, s21, 2\n"
    "s_add_u32 s35, s35, s87\n"
    "s_lshl_b32 s87, s86, 4\n"
    "v_mfma_f32_16x16x32_bf16 v[192:195], a[12:15], a[140:143], v[192:195]\n"
    "s_add_u32 s35, s35, s87\n"
    "s_mul_i32 s35, s35, 1152\n"
    "s_and_b32 s88, s88, 1\n"
    "v_mfma_f32_16x16x32_bf16 v[196:199], a[12:15], a[212:215], v[196:199]\n"
    "s_waitcnt lgkmcnt(4)\n"
    "buffer_store_dwordx4 a[84:87], v42, s[48:51], s92 offen offset:1024 sc1\n"
    "s_lshl_b32 s87, s88, 6\n"
    "v_mfma_f32_16x16x32_bf16 v[200:203], a[252:255], a[208:211], v[200:203]\n"
    "s_add_u32 s34, s34, s87\n"
    "s_add_u32 s35, s35, s87\n"
    "s_add_u32 s35, s35, 512\n"
    "v_mfma_f32_16x16x32_bf16 v[204:207], a[252:255], a[68:71], v[204:207]\n"
    "s_mul_i32 s87, s86, 640\n"
    "s_add_u32 s34, s34, s87\n"
    "s_lshl_b32 s87, 0x55550000, s86\n"
    "v_mfma_f32_16x16x32_bf16 v[208:211], a[252:255], a[140:143], v[208:211]\n"
    "s_not_b32 s82, s87\n"
    "s_mov_b32 s83, s82\n"
    "s_mov_b32 s84, 0xffff\n"
    "v_mfma_f32_16x16x32_bf16 v[212:215], a[252:255], a[212:215], v[212:215]\n"
    "s_waitcnt lgkmcnt(2)\n"
    "buffer_load_dwordx4 a[60:63], v41, s[48:51], s61 offen offset:1024 sc1\n"
    "s_add_u32 s61, s93, 32768\n"
    "v_mfma_f32_16x16x32_bf16 v[216:219], a[0:3], a[208:211], v[216:219]\n"
    "s_mov_b32 s85, 0xffff\n"
    "v_mov_b32 v11, 0x80000000\n"
    "v_cndmask_b32_e64 v17, v17, v11, s[84:85]\n"
    "v_mfma_f32_16x16x32_bf16 v[220:223], a[0:3], a[68:71], v[220:223]\n"
    "v_lshrrev_b32 v11, 5, v0\n"
    "v_mul_u32_u24 v6, 163840, v11\n"
    "v_mfma_f32_16x16x32_bf16 v[224:227], a[0:3], a[140:143], v[224:227]\n"
    "buffer_load_dwordx4 a[132:135], v40, s[48:51], s61 offen offset:0 sc1\n"
    "v_lshlrev_b32 v7, 5, v11\n"
    "v_mfma_f32_16x16x32_bf16 v[228:231], a[0:3], a[212:215], v[228:231]\n"
    "s_waitcnt vmcnt(31) lgkmcnt(0)\n"
    "s_barrier\n"
    "ds_read_b64_tr_b16 a[8:9], v22 offset:32768\n"
    "ds_read_b64_tr_b16 a[10:11], v22 offset:40960\n"
    "v_mfma_f32_16x16x32_bf16 v[232:235], a[4:7], a[208:211], v[232:235]\n"
    "s_add_u32 m0, s56, 32768\n"
    "ds_read_b64_tr_b16 a[12:13], v23 offset:32768\n"
    "ds_read_b64_tr_b16 a[14:15], v23 offset:40960\n"
    "v_mfma_f32_16x16x32_bf16 v[236:239], a[4:7], a[68:71], v[236:239]\n"
    "s_add_u32 s58, s65, s95\n"
    "ds_read_b64_tr_b16 a[252:253], v22 offset:33792\n"
    "s_add_u32 s58, s58, 0\n"
    "v_mfma_f32_16x16x32_bf16 v[240:243], a[4:7], a[140:143], v[240:243]\n"
    "s_add_u32 s59, s90, s95\n"
    "ds_read_b64_tr_b16 a[254:255], v22 offset:41984\n"
    "buffer_load_dwordx4 v2, s[40:43], s58 offen offset:0 lds\n"
    "v_mfma_f32_16x16x32_bf16 v[244:247], a[4:7], a[212:215], v[244:247]\n"
    "s_waitcnt lgkmcnt(4)\n"
    "ds_read_b64_tr_b16 a[0:1], v23 offset:33792\n"
    "s_sub_u32 s59, s59, 1024\n"
    "v_mfma_f32_16x16x32_bf16 v[144:147], a[8:11], a[208:211], v[144:147]\n"
    "ds_read_b64_tr_b16 a[2:3], v23 offset:41984\n"
    "buffer_load_dwordx4 a[204:207], v40, s[48:51], s61 offen offset:1024 sc1\n"
    "v_mfma_f32_16x16x32_bf16 v[148:151], a[8:11], a[68:71], v[148:151]\n"
    "ds_read_b64_tr_b16 a[4:5], v22 offset:34816\n"
    "v_lshlrev_b32 v9, 5, v11\n"
    "v_mfma_f32_16x16x32_bf16 v[152:155], a[8:11], a[140:143], v[152:155]\n"
    "ds_read_b64_tr_b16 a[6:7], v22 offset:43008\n"
    "v_bfe_u32 v11, v0, 3, 2\n"
    "v_mfma_f32_16x16x32_bf16 v[156:159], a[8:11], a[212:215], v[156:159]\n"
    "s_waitcnt lgkmcnt(6)\n"
    "ds_read_b64_tr_b16 a[8:9], v23 offset:34816\n"
    "buffer_load_dwordx4 v2, s[44:47], s59 offen offset:1024 lds\n"
    "v_mfma_f32_16x16x32_bf16 v[160:163], a[12:15], a[208:211], v[160:163]\n"
    "s_add_u32 s58, s91, s95\n"
    "ds_read_b64_tr_b16 a[10:11], v23 offset:43008\n"
    "s_lshl_b32 s57, s22, 31\n"
    "v_mfma_f32_16x16x32_bf16 v[164:167], a[12:15], a[68:71], v[164:167]\n"
    "s_or_b32 s58, s58, s57\n"
    "s_sub_u32 s58, s58, 2048\n"
    "v_lshl_add_u32 v6, v11, 8, v6\n"
    "v_mfma_f32_16x16x32_bf16 v[168:171], a[12:15], a[140:143], v[168:171]\n"
    "v_lshl_add_u32 v7, v11, 3, v7\n"
    "v_and_b32 v11, 7, v0\n"
    "v_mfma_f32_16x16x32_bf16 v[172:175], a[12:15], a[212:215], v[172:175]\n"
    "s_waitcnt lgkmcnt(6)\n"
    "ds_read_b64_tr_b16 a[12:13], v22 offset:35840\n"
    "buffer_load_dwordx4 a[64:67], v41, s[48:51], s61 offen offset:0 sc1\n"
    "v_mfma_f32_16x16x32_bf16 v[248:251], a[252:255], a[208:211], v[248:251]\n"
    "s_sub_u32 s59, s66, 3072\n"
    "ds_read_b64_tr_b16 a[14:15], v22 offset:44032\n"
    "buffer_load_dwordx4 v3, s[44:47], s58 offen offset:2048 lds\n"
    "v_mfma_f32_16x16x32_bf16 v[252:255], a[252:255], a[68:71], v[252:255]\n"
    "v_lshl_add_u32 v6, v11, 4, v6\n"
    "v_mul_u32_u24 v11, 1152, v11\n"
    "v_mfma_f32_16x16x32_bf16 v[28:31], a[252:255], a[140:143], v[28:31]\n"
    "v_add_u32 v7, v7, v11\n"
    "v_lshrrev_b32 v11, 4, v0\n"
    "v_mfma_f32_16x16x32_bf16 v[32:35], a[252:255], a[212:215], v[32:35]\n"
    "s_waitcnt lgkmcnt(6)\n"
    "ds_read_b64_tr_b16 a[252:253], v23 offset:35840\n"
    "v_mul_u32_u24 v8, 81920, v11\n"
    "v_mfma_f32_16x16x32_bf16 a[232:235], a[0:3], a[208:211], a[232:235]\n"
    "ds_read_b64_tr_b16 a[254:255], v23 offset:44032\n"
    "v_and_b32 v11, 1, v11\n"
    "v_mfma_f32_16x16x32_bf16 a[236:239], a[0:3], a[68:71], a[236:239]\n"
    "buffer_load_dwordx4 v2, s[40:43], s59 offen offset:3072 lds\n"
    "v_lshl_add_u32 v9, v11, 2, v9\n"
    "v_mfma_f32_16x16x32_bf16 a[240:243], a[0:3], a[140:143], a[240:243]\n"
    "v_bfe_u32 v11, v0, 2, 2\n"
    "v_lshl_add_u32 v8, v11, 8, v8\n"
    "v_mfma_f32_16x16x32_bf16 a[244:247], a[0:3], a[212:215], a[244:247]\n"
    "s_waitcnt lgkmcnt(6)\n"
    "v_lshl_add_u32 v9, v11, 3, v9\n"
    "v_and_b32 v11, 3, v0\n"
    "v_mfma_f32_16x16x32_bf16 a[88:91], a[4:7], a[72:75], a[88:91]\n"
    "v_lshl_add_u32 v8, v11, 4, v8\n"
    "v_mul_u32_u24 v11, 1152, v11\n"
    "v_mfma_f32_16x16x32_bf16 a[92:95], a[4:7], a[144:147], a[92:95]\n"
    "buffer_load_dwordx4 a[136:139], v41, s[48:51], s61 offen offset:1024 sc1\n"
    "s_add_u32 s61, s93, 40960\n"
    "v_mfma_f32_16x16x32_bf16 a[96:99], a[4:7], a[216:219], a[96:99]\n"
    "v_add_u32 v9, v9, v11\n"
    "v_bfe_u32 v11, v0, 3, 1\n"
    "v_mfma_f32_16x16x32_bf16 a[100:103], a[4:7], a[76:79], a[100:103]\n"
    "s_waitcnt lgkmcnt(4)\n"
    "v_and_b32 v10, 3, v0\n"
    "v_lshl_add_u32 v11, v11, 2, v10\n"
    "v_mfma_f32_16x16x32_bf16 a[104:107], a[8:11], a[72:75], a[104:107]\n"
    "buffer_load_dwordx4 a[208:211], v40, s[48:51], s61 offen offset:0 sc1\n"
    "v_lshrrev_b32 v14, 2, v0\n"
    "v_mfma_f32_16x16x32_bf16 a[108:111], a[8:11], a[144:147], a[108:111]\n"
    "v_lshrrev_b32 v15, 4, v0\n"
    "v_xor_b32 v14, v14, v15\n"
    "v_mfma_f32_16x16x32_bf16 a[160:163], a[8:11], a[216:219], a[160:163]\n"
    "v_and_b32 v14, 1, v14\n"
    "v_lshl_add_u32 v11, v14, 4, v11\n"
    "v_mfma_f32_16x16x32_bf16 a[164:167], a[8:11], a[76:79], a[164:167]\n"
    "s_waitcnt lgkmcnt(2)\n"
    "buffer_load_dwordx4 a[68:71], v40, s[48:51], s61 offen offset:1024 sc1\n"
    "v_and_b32 v12, 1, v11\n"
    "v_mfma_f32_16x16x32_bf16 v[184:187], a[12:15], a[72:75], v[184:187]\n"
    "v_lshlrev_b32 v12, 4, v12\n"
    "v_mov_b32 v13, 0x3f80\n"
    "v_mfma_f32_16x16x32_bf16 v[188:191], a[12:15], a[144:147], v[188:191]\n"
    "v_lshlrev_b32 v12, v12, v13\n"
    "v_lshrrev_b32 v11, 1, v11\n"
    "v_mfma_f32_16x16x32_bf16 v[192:195], a[12:15], a[216:219], v[192:195]\n"
    "s_waitcnt vmcnt(21)\n"
    "v_sub_u32 v37, v36, v26\n"
    "v_subrev_u32 v37, 3, v37\n"
    "v_cmp_gt_u32 vcc, 32, v37\n"
    "s_nop 1\n"
    "s_cmp_eq_u64 vcc, -1\n"
    "s_cbranch_scc1 L_ok4\n"
    "s_mov_b32 s79, 100000\n"
    "L_sp4:\n"
    "s_sleep 2\n"
    "buffer_load_dword v36, v25, s[48:51], 0 offen offset:256 sc1\n"
    "s_waitcnt vmcnt(0)\n"
    "v_sub_u32 v37, v36, v26\n"
    "v_subrev_u32 v37, 3, v37\n"
    "v_cmp_gt_u32 vcc, 32, v37\n"
    "s_nop 1\n"
    "s_cmp_eq_u64 vcc, -1\n"
    "s_cbranch_scc1 L_ok4\n"
    "s_sub_u32 s79, s79, 1\n"
    "s_cmp_lg_u32 s79, 0\n"
    "s_cbranch_scc1 L_sp4\n"
    "L_ok4:\n"
    "v_mfma_f32_16x16x32_bf16 v[196:199], a[12:15], a[76:79], v[196:199]\n"
    "s_waitcnt lgkmcnt(0)\n"
    "s_barrier\n"
    "ds_read_b64_tr_b16 a[0:1], v22 offset:49152\n"
    "ds_read_b64_tr_b16 a[2:3], v22 offset:57344\n"
    "v_mfma_f32_16x16x32_bf16 v[200:203], a[252:255], a[72:75], v[200:203]\n"
    "s_add_u32 m0, s56, 49152\n"
    "ds_read_b64_tr_b16 a[4:5], v23 offset:49152\n"
    "ds_read_b64_tr_b16 a[6:7], v23 offset:57344\n"
    "v_mfma_f32_16x16x32_bf16 v[204:207], a[252:255], a[144:147], v[204:207]\n"
    "s_add_u32 s58, s66, 36864\n"
    "ds_read_b64_tr_b16 a[8:9], v22 offset:50176\n"
    "buffer_load_dwordx2 a[32:33], v180, s[48:51], s94 offen offset:0 sc1\n"
    "v_mfma_f32_16x16x32_bf16 v[208:211], a[252:255], a[216:219], v[208:211]\n"
    "s_add_u32 s59, s66, 72704\n"
    "ds_read_b64_tr_b16 a[10:11], v22 offset:58368\n"
    "buffer_load_dwordx4 v2, s[40:43], s58 offen offset:0 lds\n"
    "v_mfma_f32_16x16x32_bf16 v[212:215], a[252:255], a[76:79], v[212:215]\n"
    "s_waitcnt lgkmcnt(4)\n"
    "ds_read_b64_tr_b16 a[12:13], v23 offset:50176\n"
    "v_cmp_eq_u32_e64 s[86:87], 0, v11\n"
    "v_mfma_f32_16x16x32_bf16 v[216:219], a[0:3], a[72:75], v[216:219]\n"
    "ds_read_b64_tr_b16 a[14:15], v23 offset:58368\n"
    "v_cndmask_b32_e64 v44, 0, v12, s[86:87]\n"
    "v_mfma_f32_16x16x32_bf16 v[220:223], a[0:3], a[144:147], v[220:223]\n"
    "ds_read_b64_tr_b16 a[252:253], v22 offset:51200\n"
    "buffer_load_dwordx2 a[34:35], v180, s[48:51], s94 offen offset:256 sc1\n"
    "v_mfma_f32_16x16x32_bf16 v[224:227], a[0:3], a[216:219], v[224:227]\n"
    "ds_read_b64_tr_b16 a[254:255], v22 offset:59392\n"
    "v_cmp_eq_u32_e64 s[86:87], 1, v11\n"
    "v_mfma_f32_16x16x32_bf16 v[228:231], a[0:3], a[76:79], v[228:231]\n"
    "s_waitcnt lgkmcnt(6)\n"
    "ds_read_b64_tr_b16 a[0:1], v23 offset:51200\n"
    "buffer_load_dwordx4 v2, s[40:43], s59 offen offset:1024 lds\n"
    "v_mfma_f32_16x16x32_bf16 v[232:235], a[4:7], a[72:75], v[232:235]\n"
    "s_add_u32 s58, s66, 108544\n"
    "ds_read_b64_tr_b16 a[2:3], v23 offset:59392\n"
    "v_cndmask_b32_e64 v45, 0, v12, s[86:87]\n"
    "v_mfma_f32_16x16x32_bf16 v[236:239], a[4:7], a[144:147], v[236:239]\n"
    "v_cmp_eq_u32_e64 s[86:87], 2, v11\n"
    "s_nop 1\n"
    "v_cndmask_b32_e64 v46, 0, v12, s[86:87]\n"
    "v_mfma_f32_16x16x32_bf16 v[240:243], a[4:7], a[216:219], v[240:243]\n"
    "v_cmp_eq_u32_e64 s[86:87], 3, v11\n"
    "s_nop 1\n"
    "v_cndmask_b32_e64 v47, 0, v12, s[86:87]\n"
    "v_mfma_f32_16x16x32_bf16 v[244:247], a[4:7], a[76:79], v[244:247]\n"
    "s_waitcnt lgkmcnt(6)\n"
    "ds_read_b64_tr_b16 a[4:5], v22 offset:52224\n"
    "v_bfe_u32 v11, v0, 2, 2\n"
    "v_mfma_f32_16x16x32_bf16 v[144:147], a[8:11], a[72:75], v[144:147]\n"
    "s_add_u32 s59, s66, 144384\n"
    "ds_read_b64_tr_b16 a[6:7], v22 offset:60416\n"
    "buffer_load_dwordx4 v2, s[40:43], s58 offen offset:2048 lds\n"
    "v_mfma_f32_16x16x32_bf16 v[148:151], a[8:11], a[144:147], v[148:151]\n"
    "v_and_b32 v10, 1, v0\n"
    "v_lshl_add_u32 v11, v11, 1, v10\n"
    "v_mfma_f32_16x16x32_bf16 v[152:155], a[8:11], a[216:219], v[152:155]\n"
    "v_bfe_u32 v10, v0, 1, 1\n"
    "v_lshl_add_u32 v11, v10, 4, v11\n"
    "v_mfma_f32_16x16x32_bf16 v[156:159], a[8:11], a[76:79], v[156:159]\n"
    "s_waitcnt lgkmcnt(6)\n"
    "ds_read_b64_tr_b16 a[8:9], v23 offset:52224\n"
    "buffer_load_dwordx2 a[36:37], v181, s[48:51], s94 offen offset:0 sc1\n"
    "v_mfma_f32_16x16x32_bf16 v[160:163], a[12:15], a[72:75], v[160:163]\n"
    "ds_read_b64_tr_b16 a[10:11], v23 offset:60416\n"
    "v_and_b32 v12, 1, v11\n"
    "v_mfma_f32_16x16x32_bf16 v[164:167], a[12:15], a[144:147], v[164:167]\n"
    "buffer_load_dwordx4 v2, s[40:43], s59 offen offset:3072 lds\n"
    "v_lshlrev_b32 v12, 4, v12\n"
    "v_mfma_f32_16x16x32_bf16 v[168:171], a[12:15], a[216:219], v[168:171]\n"
    "v_mov_b32 v13, 0x3f80\n"
    "v_lshlrev_b32 v12, v12, v13\n"
    "v_mfma_f32_16x16x32_bf16 v[172:175], a[12:15], a[76:79], v[172:175]\n"
    "s_waitcnt lgkmcnt(6)\n"
    "v_lshrrev_b32 v11, 1, v11\n"
    "v_cmp_eq_u32_e64 s[86:87], 0, v11\n"
    "v_mfma_f32_16x16x32_bf16 v[248:251], a[252:255], a[72:75], v[248:251]\n"
    "s_nop 0\n"
    "v_cndmask_b32_e64 v14, 0, v12, s[86:87]\n"
    "v_accvgpr_write_b32 a248, v14\n"
    "v_mfma_f32_16x16x32_bf16 v[252:255], a[252:255], a[144:147], v[252:255]\n"
    "v_cmp_eq_u32_e64 s[86:87], 1, v11\n"
    "s_nop 1\n"
    "v_cndmask_b32_e64 v14, 0, v12, s[86:87]\n"
    "v_mfma_f32_16x16x32_bf16 v[28:31], a[252:255], a[216:219], v[28:31]\n"
    "buffer_load_dwordx2 a[38:39], v181, s[48:51], s94 offen offset:256 sc1\n"
    "v_accvgpr_write_b32 a249, v14\n"
    "v_mfma_f32_16x16x32_bf16 v[32:35], a[252:255], a[76:79], v[32:35]\n"
    "s_waitcnt lgkmcnt(4)\n"
    "v_cmp_eq_u32_e64 s[86:87], 2, v11\n"
    "s_nop 1\n"
    "v_cndmask_b32_e64 v14, 0, v12, s[86:87]\n"
    "v_mfma_f32_16x16x32_bf16 a[232:235], a[0:3], a[72:75], a[232:235]\n"
    "v_accvgpr_write_b32 a250, v14\n"
    "v_cmp_eq_u32_e64 s[86:87], 3, v11\n"
    "v_mfma_f32_16x16x32_bf16 a[236:239], a[0:3], a[144:147], a[236:239]\n"
    "buffer_load_dwordx4 a[140:143], v41, s[48:51], s61 offen offset:0 sc1\n"
    "v_cndmask_b32_e64 v14, 0, v12, s[86:87]\n"
    "v_mfma_f32_16x16x32_bf16 a[240:243], a[0:3], a[216:219], a[240:243]\n"
    "v_accvgpr_write_b32 a251, v14\n"
    "v_mfma_f32_16x16x32_bf16 a[244:247], a[0:3], a[76:79], a[244:247]\n"
    "s_waitcnt lgkmcnt(2)\n"
    "v_mfma_f32_16x16x32_bf16 a[88:91], a[4:7], a[148:151], a[88:91]\n"
    "buffer_load_dwordx4 a[212:215], v41, s[48:51], s61 offen offset:1024 sc1\n"
    "v_mfma_f32_16x16x32_bf16 a[92:95], a[4:7], a[220:223], a[92:95]\n"
    "v_mfma_f32_16x16x32_bf16 a[96:99], a[4:7], a[80:83], a[96:99]\n"
    "v_mfma_f32_16x16x32_bf16 a[100:103], a[4:7], a[152:155], a[100:103]\n"
    "s_waitcnt lgkmcnt(0)\n"
    "s_barrier\n"
    "ds_read_b64_tr_b16 a[12:13], v20 offset:40960\n"
    "ds_read_b64_tr_b16 a[14:15], v20 offset:49152\n"
    "v_mfma_f32_16x16x32_bf16 a[104:107], a[8:11], a[148:151], a[104:107]\n"
    "s_add_u32 m0, s56, 65536\n"
    "ds_read_b64_tr_b16 a[252:253], v21 offset:40960\n"
    "ds_read_b64_tr_b16 a[254:255], v21 offset:49152\n"
    "v_mfma_f32_16x16x32_bf16 a[108:111], a[8:11], a[220:223], a[108:111]\n"
    "s_add_u32 s58, s66, 184320\n"
    "ds_read_b64_tr_b16 a[0:1], v20 offset:41984\n"
    "buffer_load_dwordx4 a[168:171], v40, s[48:51], s63 offen offset:0 sc1\n"
    "v_mfma_f32_16x16x32_bf16 a[160:163], a[8:11], a[80:83], a[160:163]\n"
    "s_sub_u32 s59, s90, 1024\n"
    "ds_read_b64_tr_b16 a[2:3], v20 offset:50176\n"
    "buffer_load_dwordx4 v2, s[40:43], s58 offen offset:0 lds\n"
    "v_mfma_f32_16x16x32_bf16 a[164:167], a[8:11], a[152:155], a[164:167]\n"
    "s_waitcnt lgkmcnt(4)\n"
    "ds_read_b64_tr_b16 a[4:5], v21 offset:41984\n"
    "v_mfma_f32_16x16x32_bf16 v[184:187], a[12:15], a[148:151], v[184:187]\n"
    "ds_read_b64_tr_b16 a[6:7], v21 offset:50176\n"
    "v_mfma_f32_16x16x32_bf16 v[188:191], a[12:15], a[220:223], v[188:191]\n"
    "ds_read_b64_tr_b16 a[8:9], v20 offset:43008\n"
    "v_mfma_f32_16x16x32_bf16 v[192:195], a[12:15], a[80:83], v[192:195]\n"
    "ds_read_b64_tr_b16 a[10:11], v20 offset:51200\n"
    "buffer_load_dwordx4 a[172:175], v40, s[48:51], s63 offen offset:1024 sc1\n"
    "v_mfma_f32_16x16x32_bf16 v[196:199], a[12:15], a[152:155], v[196:199]\n"
    "s_waitcnt lgkmcnt(6)\n"
    "ds_read_b64_tr_b16 a[12:13], v21 offset:43008\n"
    "buffer_load_dwordx4 v2, s[44:47], s59 offen offset:1024 lds\n"
    "v_mfma_f32_16x16x32_bf16 v[200:203], a[252:255], a[148:151], v[200:203]\n"
    "s_sub_u32 s58, s68, 2048\n"
    "ds_read_b64_tr_b16 a[14:15], v21 offset:51200\n"
    "v_mfma_f32_16x16x32_bf16 v[204:207], a[252:255], a[220:223], v[204:207]\n"
    "v_mfma_f32_16x16x32_bf16 v[208:211], a[252:255], a[80:83], v[208:211]\n"
    "v_mfma_f32_16x16x32_bf16 v[212:215], a[252:255], a[152:155], v[212:215]\n"
    "s_waitcnt lgkmcnt(6)\n"
    "ds_read_b64_tr_b16 a[252:253], v20 offset:44032\n"
    "v_mfma_f32_16x16x32_bf16 v[216:219], a[0:3], a[148:151], v[216:219]\n"
    "s_sub_u32 s59, s91, 3072\n"
    "ds_read_b64_tr_b16 a[254:255], v20 offset:52224\n"
    "buffer_load_dwordx4 v2, s[44:47], s58 offen offset:2048 lds\n"
    "v_mfma_f32_16x16x32_bf16 v[220:223], a[0:3], a[220:223], v[220:223]\n"
    "v_mfma_f32_16x16x32_bf16 v[224:227], a[0:3], a[80:83], v[224:227]\n"
    "v_mfma_f32_16x16x32_bf16 v[228:231], a[0:3], a[152:155], v[228:231]\n"
    "s_waitcnt lgkmcnt(6)\n"
    "ds_read_b64_tr_b16 a[0:1], v21 offset:44032\n"
    "v_mfma_f32_16x16x32_bf16 v[232:235], a[4:7], a[148:151], v[232:235]\n"
    "ds_read_b64_tr_b16 a[2:3], v21 offset:52224\n"
    "buffer_load_dwordx4 a[176:179], v41, s[48:51], s63 offen offset:0 sc1\n"
    "v_mfma_f32_16x16x32_bf16 v[236:239], a[4:7], a[220:223], v[236:239]\n"
    "buffer_load_dwordx4 v3, s[44:47], s59 offen offset:3072 lds\n"
    "v_mfma_f32_16x16x32_bf16 v[240:243], a[4:7], a[80:83], v[240:243]\n"
    "v_mfma_f32_16x16x32_bf16 v[244:247], a[4:7], a[152:155], v[244:247]\n"
    "s_waitcnt lgkmcnt(6)\n"
    "v_mfma_f32_16x16x32_bf16 v[144:147], a[8:11], a[148:151], v[144:147]\n"
    "v_mfma_f32_16x16x32_bf16 v[148:151], a[8:11], a[220:223], v[148:151]\n"
    "s_add_u32 s89, s27, 5\n"
    "v_mov_b32 v38, 0x80000000\n"
    "v_writelane_b32 v38, s26, 0\n"
    "v_mfma_f32_16x16x32_bf16 v[152:155], a[8:11], a[80:83], v[152:155]\n"
    "v_mov_b32 v39, s89\n"
    "buffer_load_dwordx4 a[180:183], v41, s[48:51], s63 offen offset:1024 sc1\n"
    "v_mfma_f32_16x16x32_bf16 v[156:159], a[8:11], a[152:155], v[156:159]\n"
    "s_waitcnt lgkmcnt(4)\n"
    "v_mfma_f32_16x16x32_bf16 v[160:163], a[12:15], a[148:151], v[160:163]\n"
    "v_mfma_f32_16x16x32_bf16 v[164:167], a[12:15], a[220:223], v[164:167]\n"
    "v_mfma_f32_16x16x32_bf16 v[168:171], a[12:15], a[80:83], v[168:171]\n"
    "v_mfma_f32_16x16x32_bf16 v[172:175], a[12:15], a[152:155], v[172:175]\n"
    "s_waitcnt lgkmcnt(2)\n"
    "v_mfma_f32_16x16x32_bf16 v[248:251], a[252:255], a[148:151], v[248:251]\n"
    "v_mfma_f32_16x16x32_bf16 v[252:255], a[252:255], a[220:223], v[252:255]\n"
    "s_waitcnt vmcnt(29)\n"
    "buffer_store_dword v39, v38, s[48:51], 0 offen offset:0 sc1\n"
    "v_mfma_f32_16x16x32_bf16 v[28:31], a[252:255], a[80:83], v[28:31]\n"
    "v_mfma_f32_16x16x32_bf16 v[32:35], a[252:255], a[152:155], v[32:35]\n"
    "s_waitcnt lgkmcnt(0)\n"
    "s_barrier\n"
    "ds_read_b64_tr_b16 a[4:5], v22 offset:0\n"
    "ds_read_b64_tr_b16 a[6:7], v22 offset:8192\n"
    "L_hop11:\n"
    "s_cbranch_execz L_back0\n"
    "v_mfma_f32_16x16x32_bf16 a[232:235], a[0:3], a[148:151], a[232:235]\n"
    "s_add_u32 m0, s56, 0\n"
    "ds_read_b64_tr_b16 a[8:9], v23 offset:0\n"
    "ds_read_b64_tr_b16 a[10:11], v23 offset:8192\n"
    "v_mfma_f32_16x16x32_bf16 a[236:239], a[0:3], a[220:223], a[236:239]\n"
    "s_add_u32 s58, s66, 128\n"
    "ds_read_b64_tr_b16 a[12:13], v22 offset:1024\n"
    "v_mfma_f32_16x16x32_bf16 a[240:243], a[0:3], a[80:83], a[240:243]\n"
    "s_add_u32 s59, s66, 35968\n"
    "ds_read_b64_tr_b16 a[14:15], v22 offset:9216\n"
    "buffer_load_dwordx4 v2, s[40:43], s58 offen offset:0 lds\n"
    "v_mfma_f32_16x16x32_bf16 a[244:247], a[0:3], a[152:155], a[244:247]\n"
    "s_waitcnt lgkmcnt(4)\n"
    "ds_read_b64_tr_b16 a[252:253], v23 offset:1024\n"
    "v_mfma_f32_16x16x32_bf16 a[88:91], a[4:7], a[224:227], a[88:91]\n"
    "ds_read_b64_tr_b16 a[254:255], v23 offset:9216\n"
    "v_mfma_f32_16x16x32_bf16 a[92:95], a[4:7], a[84:87], a[92:95]\n"
    "ds_read_b64_tr_b16 a[0:1], v22 offset:2048\n"
    "v_mfma_f32_16x16x32_bf16 a[96:99], a[4:7], a[156:159], a[96:99]\n"
    "ds_read_b64_tr_b16 a[2:3], v22 offset:10240\n"
    "v_mfma_f32_16x16x32_bf16 a[100:103], a[4:7], a[228:231], a[100:103]\n"
    "s_waitcnt lgkmcnt(6)\n"
    "ds_read_b64_tr_b16 a[4:5], v23 offset:2048\n"
    "buffer_load_dwordx4 v2, s[40:43], s59 offen offset:1024 lds\n"
    "v_mfma_f32_16x16x32_bf16 a[104:107], a[8:11], a[224:227], a[104:107]\n"
    "s_add_u32 s58, s66, 71808\n"
    "ds_read_b64_tr_b16 a[6:7], v23 offset:10240\n"
    "v_mfma_f32_16x16x32_bf16 a[108:111], a[8:11], a[84:87], a[108:111]\n"
    "v_mfma_f32_16x16x32_bf16 a[160:163], a[8:11], a[156:159], a[160:163]\n"
    "v_mfma_f32_16x16x32_bf16 a[164:167], a[8:11], a[228:231], a[164:167]\n"
    "s_waitcnt lgkmcnt(6)\n"
    "ds_read_b64_tr_b16 a[8:9], v22 offset:3072\n"
    "v_mfma_f32_16x16x32_bf16 v[184:187], a[12:15], a[224:227], v[184:187]\n"
    "s_add_u32 s59, s66, 107648\n"
    "ds_read_b64_tr_b16 a[10:11], v22 offset:11264\n"
    "buffer_load_dwordx4 v2, s[40:43], s58 offen offset:2048 lds\n"
    "v_mfma_f32_16x16x32_bf16 v[188:191], a[12:15], a[84:87], v[188:191]\n"
    "v_mfma_f32_16x16x32_bf16 v[192:195], a[12:15], a[156:159], v[192:195]\n"
    "v_mfma_f32_16x16x32_bf16 v[196:199], a[12:15], a[228:231], v[196:199]\n"
    "s_waitcnt lgkmcnt(6)\n"
    "ds_read_b64_tr_b16 a[12:13], v23 offset:3072\n"
    "v_mfma_f32_16x16x32_bf16 v[200:203], a[252:255], a[224:227], v[200:203]\n"
    "ds_read_b64_tr_b16 a[14:15], v23 offset:11264\n"
    "v_mfma_f32_16x16x32_bf16 v[204:207], a[252:255], a[84:87], v[204:207]\n"
    "buffer_load_dwordx4 v2, s[40:43], s59 offen offset:3072 lds\n"
    "v_mfma_f32_16x16x32_bf16 v[208:211], a[252:255], a[156:159], v[208:211]\n"
    "v_mfma_f32_16x16x32_bf16 v[212:215], a[252:255], a[228:231], v[212:215]\n"
    "s_waitcnt lgkmcnt(6)\n"
    "v_mfma_f32_16x16x32_bf16 v[216:219], a[0:3], a[224:227], v[216:219]\n"
    "v_mfma_f32_16x16x32_bf16 v[220:223], a[0:3], a[84:87], v[220:223]\n"
    "v_mfma_f32_16x16x32_bf16 v[224:227], a[0:3], a[156:159], v[224:227]\n"
    "v_mfma_f32_16x16x32_bf16 v[228:231], a[0:3], a[228:231], v[228:231]\n"
    "s_waitcnt lgkmcnt(4)\n"
    "v_mfma_f32_16x16x32_bf16 v[232:235], a[4:7], a[224:227], v[232:235]\n"
    "v_mfma_f32_16x16x32_bf16 v[236:239], a[4:7], a[84:87], v[236:239]\n"
    "v_mfma_f32_16x16x32_bf16 v[240:243], a[4:7], a[156:159], v[240:243]\n"
    "v_mfma_f32_16x16x32_bf16 v[244:247], a[4:7], a[228:231], v[244:247]\n"
    "s_waitcnt lgkmcnt(2)\n"
    "v_mfma_f32_16x16x32_bf16 v[144:147], a[8:11], a[224:227], v[144:147]\n"
    "v_mfma_f32_16x16x32_bf16 v[148:151], a[8:11], a[84:87], v[148:151]\n"
    "v_mfma_f32_16x16x32_bf16 v[152:155], a[8:11], a[156:159], v[152:155]\n"
    "v_mfma_f32_16x16x32_bf16 v[156:159], a[8:11], a[228:231], v[156:159]\n"
    "s_waitcnt vmcnt(26) lgkmcnt(0)\n"
    "s_barrier\n"
    "ds_read_b64_tr_b16 a[252:253], v22 offset:16384\n"
    "ds_read_b64_tr_b16 a[254:255], v22 offset:24576\n"
    "v_mfma_f32_16x16x32_bf16 v[160:163], a[12:15], a[224:227], v[160:163]\n"
    "s_add_u32 m0, s56, 16384\n"
    "ds_read_b64_tr_b16 a[0:1], v23 offset:16384\n"
    "ds_read_b64_tr_b16 a[2:3], v23 offset:24576\n"
    "v_mfma_f32_16x16x32_bf16 v[164:167], a[12:15], a[84:87], v[164:167]\n"
    "s_add_u32 s58, s66, 147584\n"
    "ds_read_b64_tr_b16 a[4:5], v22 offset:17408\n"
    "v_mfma_f32_16x16x32_bf16 v[168:171], a[12:15], a[156:159], v[168:171]\n"
    "s_add_u32 s59, s66, 183424\n"
    "ds_read_b64_tr_b16 a[6:7], v22 offset:25600\n"
    "buffer_load_dwordx4 v2, s[40:43], s58 offen offset:0 lds\n"
    "v_mfma_f32_16x16x32_bf16 v[172:175], a[12:15], a[228:231], v[172:175]\n"
    "s_waitcnt lgkmcnt(4)\n"
    "ds_read_b64_tr_b16 a[8:9], v23 offset:17408\n"
    "v_mfma_f32_16x16x32_bf16 v[248:251], a[252:255], a[224:227], v[248:251]\n"
    "ds_read_b64_tr_b16 a[10:11], v23 offset:25600\n"
    "v_mfma_f32_16x16x32_bf16 v[252:255], a[252:255], a[84:87], v[252:255]\n"
    "ds_read_b64_tr_b16 a[12:13], v22 offset:18432\n"
    "v_mfma_f32_16x16x32_bf16 v[28:31], a[252:255], a[156:159], v[28:31]\n"
    "v_mfma_f32_16x16x32_bf16 v[32:35], a[252:255], a[228:231], v[32:35]\n"
    "s_waitcnt lgkmcnt(5)\n"
    "ds_read_b64_tr_b16 a[252:253], v23 offset:18432\n"
    "buffer_load_dwordx4 v2, s[40:43], s59 offen offset:1024 lds\n"
    "v_mfma_f32_16x16x32_bf16 a[232:235], a[0:3], a[224:227], a[232:235]\n"
    "s_sub_u32 s58, s90, 1920\n"
    "v_mfma_f32_16x16x32_bf16 a[236:239], a[0:3], a[84:87], a[236:239]\n"
    "v_mfma_f32_16x16x32_bf16 a[240:243], a[0:3], a[156:159], a[240:243]\n"
    "v_mfma_f32_16x16x32_bf16 a[244:247], a[0:3], a[228:231], a[244:247]\n"
    "s_waitcnt lgkmcnt(4)\n"
    "ds_read_b64_tr_b16 a[0:1], v22 offset:19456\n"
    "v_mfma_f32_16x16x32_bf16 v[248:251], a[4:7], a[16:19], v[248:251]\n"
    "s_sub_u32 s59, s68, 2944\n"
    "ds_read_b64_tr_b16 a[2:3], v22 offset:27648\n"
    "buffer_load_dwordx4 v2, s[44:47], s58 offen offset:2048 lds\n"
    "v_mfma_f32_16x16x32_bf16 v[252:255], a[4:7], a[20:23], v[252:255]\n"
    "v_mfma_f32_16x16x32_bf16 v[28:31], a[4:7], a[24:27], v[28:31]\n"
    "v_mfma_f32_16x16x32_bf16 v[32:35], a[4:7], a[28:31], v[32:35]\n"
    "s_waitcnt lgkmcnt(4)\n"
    "ds_read_b64_tr_b16 a[4:5], v23 offset:19456\n"
    "v_mfma_f32_16x16x32_bf16 a[232:235], a[8:11], a[16:19], a[232:235]\n"
    "ds_read_b64_tr_b16 a[6:7], v23 offset:27648\n"
    "v_mfma_f32_16x16x32_bf16 a[236:239], a[8:11], a[20:23], a[236:239]\n"
    "buffer_load_dwordx4 v2, s[44:47], s59 offen offset:3072 lds\n"
    "v_mfma_f32_16x16x32_bf16 a[240:243], a[8:11], a[24:27], a[240:243]\n"
    "v_mfma_f32_16x16x32_bf16 a[244:247], a[8:11], a[28:31], a[244:247]\n"
    "s_waitcnt vmcnt(26) lgkmcnt(5)\n"
    "v_mfma_f32_16x16x16_bf16 v[248:251], a[12:13], a[32:33], v[248:251]\n"
    "s_waitcnt vmcnt(24)\n"
    "v_mfma_f32_16x16x16_bf16 v[252:255], a[12:13], a[34:35], v[252:255]\n"
    "s_waitcnt vmcnt(21)\n"
    "v_mfma_f32_16x16x16_bf16 v[28:31], a[12:13], a[36:37], v[28:31]\n"
    "s_waitcnt vmcnt(19)\n"
    "buffer_load_dword v36, v25, s[48:51], 0 offen offset:256 sc1\n"
    "v_mfma_f32_16x16x16_bf16 v[32:35], a[12:13], a[38:39], v[32:35]\n"
    "s_waitcnt lgkmcnt(4)\n"
    "v_mfma_f32_16x16x16_bf16 a[232:235], a[252:253], a[32:33], a[232:235]\n"
    "v_mfma_f32_16x16x16_bf16 a[236:239], a[252:253], a[34:35], a[236:239]\n"
    "v_mfma_f32_16x16x16_bf16 a[240:243], a[252:253], a[36:37], a[240:243]\n"
    "v_mfma_f32_16x16x16_bf16 a[244:247], a[252:253], a[38:39], a[244:247]\n"
    "s_waitcnt lgkmcnt(2)\n"
    "v_mfma_f32_16x16x32_bf16 a[88:91], a[0:3], a[40:43], a[88:91]\n"
    "v_mfma_f32_16x16x32_bf16 a[92:95], a[0:3], a[112:115], a[92:95]\n"
    "v_mfma_f32_16x16x32_bf16 a[96:99], a[0:3], a[184:187], a[96:99]\n"
    "s_nop 0\n"
    "v_accvgpr_read_b32 v116, a232\n"
    "v_accvgpr_read_b32 v117, a233\n"
    "v_mfma_f32_16x16x32_bf16 a[100:103], a[0:3], a[44:47], a[100:103]\n"
    "s_waitcnt lgkmcnt(0)\n"
    "s_barrier\n"
    "ds_read_b64_tr_b16 a[8:9], v22 offset:32768\n"
    "ds_read_b64_tr_b16 a[10:11], v22 offset:40960\n"
    "v_mfma_f32_16x16x32_bf16 a[104:107], a[4:7], a[40:43], a[104:107]\n"
    "s_add_u32 m0, s56, 32768\n"
    "ds_read_b64_tr_b16 a[12:13], v23 offset:32768\n"
    "ds_read_b64_tr_b16 a[14:15], v23 offset:40960\n"
    "v_mfma_f32_16x16x32_bf16 a[108:111], a[4:7], a[112:115], a[108:111]\n"
    "s_add_u32 s58, s91, 128\n"
    "ds_read_b64_tr_b16 a[252:253], v22 offset:33792\n"
    "v_accvgpr_read_b32 v118, a234\n"
    "v_mfma_f32_16x16x32_bf16 a[160:163], a[4:7], a[184:187], a[160:163]\n"
    "s_sub_u32 s59, s66, 768\n"
    "ds_read_b64_tr_b16 a[254:255], v22 offset:41984\n"
    "buffer_load_dwordx4 v3, s[44:47], s58 offen offset:0 lds\n"
    "v_accvgpr_read_b32 v119, a235\n"
    "v_mfma_f32_16x16x32_bf16 a[164:167], a[4:7], a[44:47], a[164:167]\n"
    "s_waitcnt lgkmcnt(4)\n"
    "ds_read_b64_tr_b16 a[0:1], v23 offset:33792\n"
    "v_cvt_pk_bf16_f32 v248, v248, v249\n"
    "v_mfma_f32_16x16x32_bf16 a[88:91], a[8:11], a[116:119], a[88:91]\n"
    "ds_read_b64_tr_b16 a[2:3], v23 offset:41984\n"
    "v_cvt_pk_bf16_f32 v249, v250, v251\n"
    "v_mfma_f32_16x16x32_bf16 a[92:95], a[8:11], a[188:191], a[92:95]\n"
    "s_add_u32 s70, s69, 16384\n"
    "ds_read_b64_tr_b16 a[4:5], v22 offset:34816\n"
    "v_cvt_pk_bf16_f32 v250, v116, v117\n"
    "v_cvt_pk_bf16_f32 v251, v118, v119\n"
    "v_mfma_f32_16x16x32_bf16 a[96:99], a[8:11], a[48:51], a[96:99]\n"
    "ds_read_b64_tr_b16 a[6:7], v22 offset:43008\n"
    "buffer_store_dwordx4 v[248:251], v27, s[48:51], s70 offen offset:0 sc1\n"
    "v_mfma_f32_16x16x32_bf16 a[100:103], a[8:11], a[120:123], a[100:103]\n"
    "s_waitcnt lgkmcnt(6)\n"
    "ds_read_b64_tr_b16 a[8:9], v23 offset:34816\n"
    "buffer_load_dwordx4 v2, s[40:43], s59 offen offset:1024 lds\n"
    "v_mfma_f32_16x16x32_bf16 a[104:107], a[12:15], a[116:119], a[104:107]\n"
    "s_add_u32 s58, s66, 35072\n"
    "ds_read_b64_tr_b16 a[10:11], v23 offset:43008\n"
    "v_accvgpr_read_b32 v124, a236\n"
    "v_mfma_f32_16x16x32_bf16 a[108:111], a[12:15], a[188:191], a[108:111]\n"
    "v_accvgpr_read_b32 v125, a237\n"
    "v_accvgpr_read_b32 v126, a238\n"
    "v_mfma_f32_16x16x32_bf16 a[160:163], a[12:15], a[48:51], a[160:163]\n"
    "v_accvgpr_read_b32 v127, a239\n"
    "v_cvt_pk_bf16_f32 v252, v252, v253\n"
    "v_mfma_f32_16x16x32_bf16 a[164:167], a[12:15], a[120:123], a[164:167]\n"
    "s_waitcnt lgkmcnt(6)\n"
    "ds_read_b64_tr_b16 a[12:13], v22 offset:35840\n"
    "v_cvt_pk_bf16_f32 v253, v254, v255\n"
    "v_mfma_f32_16x16x32_bf16 a[88:91], a[252:255], a[192:195], a[88:91]\n"
    "s_add_u32 s59, s66, 218112\n"
    "ds_read_b64_tr_b16 a[14:15], v22 offset:44032\n"
    "buffer_load_dwordx4 v2, s[40:43], s58 offen offset:2048 lds\n"
    "v_mfma_f32_16x16x32_bf16 a[92:95], a[252:255], a[52:55], a[92:95]\n"
    "v_cvt_pk_bf16_f32 v254, v124, v125\n"
    "v_cvt_pk_bf16_f32 v255, v126, v127\n"
    "v_mfma_f32_16x16x32_bf16 a[96:99], a[252:255], a[124:127], a[96:99]\n"
    "v_mfma_f32_16x16x32_bf16 a[100:103], a[252:255], a[196:199], a[100:103]\n"
    "s_waitcnt lgkmcnt(6)\n"
    "ds_read_b64_tr_b16 a[252:253], v23 offset:35840\n"
    "v_mfma_f32_16x16x32_bf16 a[104:107], a[0:3], a[192:195], a[104:107]\n"
    "ds_read_b64_tr_b16 a[254:255], v23 offset:44032\n"
    "buffer_store_dwordx4 v[252:255], v27, s[48:51], s70 offen offset:1024 sc1\n"
    "v_mfma_f32_16x16x32_bf16 a[108:111], a[0:3], a[52:55], a[108:111]\n"
    "buffer_load_dwordx4 v2, s[40:43], s59 offen offset:3072 lds\n"
    "v_accvgpr_read_b32 v132, a240\n"
    "v_mfma_f32_16x16x32_bf16 a[160:163], a[0:3], a[124:127], a[160:163]\n"
    "v_accvgpr_read_b32 v133, a241\n"
    "v_accvgpr_read_b32 v134, a242\n"
    "v_mfma_f32_16x16x32_bf16 a[164:167], a[0:3], a[196:199], a[164:167]\n"
    "s_waitcnt lgkmcnt(6)\n"
    "v_accvgpr_read_b32 v135, a243\n"
    "v_cvt_pk_bf16_f32 v28, v28, v29\n"
    "v_mfma_f32_16x16x32_bf16 a[88:91], a[4:7], a[56:59], a[88:91]\n"
    "v_cvt_pk_bf16_f32 v29, v30, v31\n"
    "v_cvt_pk_bf16_f32 v30, v132, v133\n"
    "v_mfma_f32_16x16x32_bf16 a[92:95], a[4:7], a[128:131], a[92:95]\n"
    "v_cvt_pk_bf16_f32 v31, v134, v135\n"
    "v_mfma_f32_16x16x32_bf16 a[96:99], a[4:7], a[200:203], a[96:99]\n"
    "v_mfma_f32_16x16x32_bf16 a[100:103], a[4:7], a[60:63], a[100:103]\n"
    "s_waitcnt lgkmcnt(4)\n"
    "v_mfma_f32_16x16x32_bf16 a[104:107], a[8:11], a[56:59], a[104:107]\n"
    "v_mfma_f32_16x16x32_bf16 a[108:111], a[8:11], a[128:131], a[108:111]\n"
    "v_mfma_f32_16x16x32_bf16 a[160:163], a[8:11], a[200:203], a[160:163]\n"
    "v_mfma_f32_16x16x32_bf16 a[164:167], a[8:11], a[60:63], a[164:167]\n"
    "s_waitcnt lgkmcnt(2)\n"
    "v_mfma_f32_16x16x32_bf16 a[88:91], a[12:15], a[132:135], a[88:91]\n"
    "v_mfma_f32_16x16x32_bf16 a[92:95], a[12:15], a[204:207], a[92:95]\n"
    "v_mfma_f32_16x16x32_bf16 a[96:99], a[12:15], a[64:67], a[96:99]\n"
    "v_mfma_f32_16x16x32_bf16 a[100:103], a[12:15], a[136:139], a[100:103]\n"
    "s_waitcnt vmcnt(17) lgkmcnt(0)\n"
    "s_barrier\n"
    "ds_read_b64_tr_b16 a[0:1], v22 offset:49152\n"
    "ds_read_b64_tr_b16 a[2:3], v22 offset:57344\n"
    "v_mfma_f32_16x16x32_bf16 a[104:107], a[252:255], a[132:135], a[104:107]\n"
    "s_add_u32 m0, s56, 49152\n"
    "ds_read_b64_tr_b16 a[4:5], v23 offset:49152\n"
    "ds_read_b64_tr_b16 a[6:7], v23 offset:57344\n"
    "v_mfma_f32_16x16x32_bf16 a[108:111], a[252:255], a[204:207], a[108:111]\n"
    "s_add_u32 s58, s66, 258048\n"
    "ds_read_b64_tr_b16 a[8:9], v22 offset:50176\n"
    "buffer_store_dwordx4 v[28:31], v27, s[48:51], s70 offen offset:2048 sc1\n"
    "v_mfma_f32_16x16x32_bf16 a[160:163], a[252:255], a[64:67], a[160:163]\n"
    "s_sub_u32 s59, s67, 1024\n"
    "ds_read_b64_tr_b16 a[10:11], v22 offset:58368\n"
    "buffer_load_dwordx4 v2, s[40:43], s58 offen offset:0 lds\n"
    "v_mfma_f32_16x16x32_bf16 a[164:167], a[252:255], a[136:139], a[164:167]\n"
    "s_waitcnt lgkmcnt(4)\n"
    "ds_read_b64_tr_b16 a[12:13], v23 offset:50176\n"
    "v_accvgpr_read_b32 v140, a244\n"
    "v_mfma_f32_16x16x32_bf16 a[88:91], a[0:3], a[208:211], a[88:91]\n"
    "ds_read_b64_tr_b16 a[14:15], v23 offset:58368\n"
    "v_accvgpr_read_b32 v141, a245\n"
    "v_mfma_f32_16x16x32_bf16 a[92:95], a[0:3], a[68:71], a[92:95]\n"
    "ds_read_b64_tr_b16 a[252:253], v22 offset:51200\n"
    "v_accvgpr_read_b32 v142, a246\n"
    "v_mfma_f32_16x16x32_bf16 a[96:99], a[0:3], a[140:143], a[96:99]\n"
    "ds_read_b64_tr_b16 a[254:255], v22 offset:59392\n"
    "v_accvgpr_read_b32 v143, a247\n"
    "v_mfma_f32_16x16x32_bf16 a[100:103], a[0:3], a[212:215], a[100:103]\n"
    "s_waitcnt lgkmcnt(6)\n"
    "ds_read_b64_tr_b16 a[0:1], v23 offset:51200\n"
    "buffer_load_dwordx4 v2, s[40:43], s59 offen offset:1024 lds\n"
    "v_mfma_f32_16x16x32_bf16 a[104:107], a[4:7], a[208:211], a[104:107]\n"
    "s_add_u32 s58, s66, 219264\n"
    "ds_read_b64_tr_b16 a[2:3], v23 offset:59392\n"
    "v_cvt_pk_bf16_f32 v32, v32, v33\n"
    "v_mfma_f32_16x16x32_bf16 a[108:111], a[4:7], a[68:71], a[108:111]\n"
    "v_cvt_pk_bf16_f32 v33, v34, v35\n"
    "v_cvt_pk_bf16_f32 v34, v140, v141\n"
    "v_mfma_f32_16x16x32_bf16 a[160:163], a[4:7], a[140:143], a[160:163]\n"
    "v_cvt_pk_bf16_f32 v35, v142, v143\n"
    "v_mfma_f32_16x16x32_bf16 a[164:167], a[4:7], a[212:215], a[164:167]\n"
    "s_waitcnt lgkmcnt(6)\n"
    "ds_read_b64_tr_b16 a[4:5], v22 offset:52224\n"
    "v_mfma_f32_16x16x32_bf16 a[88:91], a[8:11], a[16:19], a[88:91]\n"
    "s_add_u32 s59, s66, 255104\n"
    "buffer_load_dwordx4 v2, s[40:43], s58 offen offset:2048 lds\n"
    "v_mfma_f32_16x16x32_bf16 a[92:95], a[8:11], a[20:23], a[92:95]\n"
    "v_mfma_f32_16x16x32_bf16 a[96:99], a[8:11], a[24:27], a[96:99]\n"
    "buffer_store_dwordx4 v[32:35], v27, s[48:51], s70 offen offset:3072 sc1\n"
    "v_mfma_f32_16x16x32_bf16 a[100:103], a[8:11], a[28:31], a[100:103]\n"
    "s_waitcnt lgkmcnt(5)\n"
    "ds_read_b64_tr_b16 a[8:9], v23 offset:52224\n"
    "v_mfma_f32_16x16x32_bf16 a[104:107], a[12:15], a[16:19], a[104:107]\n"
    "v_mfma_f32_16x16x32_bf16 a[108:111], a[12:15], a[20:23], a[108:111]\n"
    "buffer_load_dwordx4 v2, s[40:43], s59 offen offset:3072 lds\n"
    "v_mfma_f32_16x16x32_bf16 a[160:163], a[12:15], a[24:27], a[160:163]\n"
    "v_mfma_f32_16x16x32_bf16 a[164:167], a[12:15], a[28:31], a[164:167]\n"
    "s_waitcnt lgkmcnt(4)\n"
    "v_mfma_f32_16x16x32_bf16 a[88:91], a[252:255], a[168:171], a[88:91]\n"
    "v_mfma_f32_16x16x32_bf16 a[92:95], a[252:255], a[172:175], a[92:95]\n"
    "v_mfma_f32_16x16x32_bf16 a[96:99], a[252:255], a[176:179], a[96:99]\n"
    "s_waitcnt vmcnt(22)\n"
    "v_mfma_f32_16x16x32_bf16 a[100:103], a[252:255], a[180:183], a[100:103]\n"
    "s_waitcnt lgkmcnt(2)\n"
    "v_mfma_f32_16x16x32_bf16 a[104:107], a[0:3], a[168:171], a[104:107]\n"
    "v_mfma_f32_16x16x32_bf16 a[108:111], a[0:3], a[172:175], a[108:111]\n"
    "v_mfma_f32_16x16x32_bf16 a[160:163], a[0:3], a[176:179], a[160:163]\n"
    "v_mfma_f32_16x16x32_bf16 a[164:167], a[0:3], a[180:183], a[164:167]\n"
    "s_waitcnt lgkmcnt(1)\n"
    "v_mfma_f32_16x16x16_bf16 a[88:91], a[4:5], a[32:33], a[88:91]\n"
    "s_waitcnt vmcnt(12)\n"
    "v_sub_u32 v37, v36, v26\n"
    "v_subrev_u32 v37, 5, v37\n"
    "v_cmp_gt_u32 vcc, 32, v37\n"
    "s_nop 1\n"
    "s_cmp_eq_u64 vcc, -1\n"
    "s_cbranch_scc1 L_ok3\n"
    "s_mov_b32 s79, 100000\n"
    "L_sp3:\n"
    "s_sleep 2\n"
    "buffer_load_dword v36, v25, s[48:51], 0 offen offset:256 sc1\n"
    "s_waitcnt vmcnt(0)\n"
    "v_sub_u32 v37, v36, v26\n"
    "v_subrev_u32 v37, 5, v37\n"
    "v_cmp_gt_u32 vcc, 32, v37\n"
    "s_nop 1\n"
    "s_cmp_eq_u64 vcc, -1\n"
    "s_cbranch_scc1 L_ok3\n"
    "s_sub_u32 s79, s79, 1\n"
    "s_cmp_lg_u32 s79, 0\n"
    "s_cbranch_scc1 L_sp3\n"
    "L_ok3:\n"
    "v_mfma_f32_16x16x16_bf16 a[92:95], a[4:5], a[34:35], a[92:95]\n"
    "s_add_u32 s61, s93, 49152\n"
    "buffer_load_dwordx4 a[72:75], v40, s[48:51], s61 offen offset:0 sc1\n"
    "v_mfma_f32_16x16x16_bf16 a[96:99], a[4:5], a[36:37], a[96:99]\n"
    "buffer_load_dwordx4 a[144:147], v40, s[48:51], s61 offen offset:1024 sc1\n"
    "v_mfma_f32_16x16x16_bf16 a[100:103], a[4:5], a[38:39], a[100:103]\n"
    "s_waitcnt lgkmcnt(0)\n"
    "s_barrier\n"
    "ds_read_b64_tr_b16 a[12:13], v20 offset:40960\n"
    "ds_read_b64_tr_b16 a[14:15], v20 offset:49152\n"
    "v_mfma_f32_16x16x16_bf16 a[104:107], a[8:9], a[32:33], a[104:107]\n"
    "s_add_u32 m0, s56, 65536\n"
    "ds_read_b64_tr_b16 a[252:253], v21 offset:40960\n"
    "ds_read_b64_tr_b16 a[254:255], v21 offset:49152\n"
    "v_mfma_f32_16x16x16_bf16 a[108:111], a[8:9], a[34:35], a[108:111]\n"
    "s_add_u32 s58, s67, 128\n"
    "ds_read_b64_tr_b16 a[0:1], v20 offset:41984\n"
    "buffer_load_dwordx4 a[216:219], v41, s[48:51], s61 offen offset:0 sc1\n"
    "v_mfma_f32_16x16x16_bf16 a[160:163], a[8:9], a[36:37], a[160:163]\n"
    "s_add_u32 s59, s66, 72960\n"
    "ds_read_b64_tr_b16 a[2:3], v20 offset:50176\n"
    "buffer_load_dwordx4 v2, s[40:43], s58 offen offset:0 lds\n"
    "v_mfma_f32_16x16x16_bf16 a[164:167], a[8:9], a[38:39], a[164:167]\n"
    "s_waitcnt lgkmcnt(4)\n"
    "ds_read_b64_tr_b16 a[4:5], v21 offset:41984\n"
    "v_mfma_f32_16x16x32_bf16 v[184:187], a[12:15], a[40:43], v[184:187]\n"
    "ds_read_b64_tr_b16 a[6:7], v21 offset:50176\n"
    "v_mfma_f32_16x16x32_bf16 v[188:191], a[12:15], a[112:115], v[188:191]\n"
    "ds_read_b64_tr_b16 a[8:9], v20 offset:43008\n"
    "v_mfma_f32_16x16x32_bf16 v[192:195], a[12:15], a[184:187], v[192:195]\n"
    "ds_read_b64_tr_b16 a[10:11], v20 offset:51200\n"
    "v_mfma_f32_16x16x32_bf16 v[196:199], a[12:15], a[44:47], v[196:199]\n"
    "s_waitcnt lgkmcnt(6)\n"
    "ds_read_b64_tr_b16 a[12:13], v21 offset:43008\n"
    "buffer_load_dwordx4 v2, s[40:43], s59 offen offset:1024 lds\n"
    "v_mfma_f32_16x16x32_bf16 v[200:203], a[252:255], a[40:43], v[200:203]\n"
    "s_add_u32 s58, s66, 108800\n"
    "ds_read_b64_tr_b16 a[14:15], v21 offset:51200\n"
    "v_mfma_f32_16x16x32_bf16 v[204:207], a[252:255], a[112:115], v[204:207]\n"
    "v_mfma_f32_16x16x32_bf16 v[208:211], a[252:255], a[184:187], v[208:211]\n"
    "v_mfma_f32_16x16x32_bf16 v[212:215], a[252:255], a[44:47], v[212:215]\n"
    "s_waitcnt lgkmcnt(6)\n"
    "ds_read_b64_tr_b16 a[252:253], v20 offset:44032\n"
    "buffer_load_dwordx4 a[76:79], v41, s[48:51], s61 offen offset:1024 sc1\n"
    "v_mfma_f32_16x16x32_bf16 v[184:187], a[0:3], a[116:119], v[184:187]\n"
    "s_add_u32 s59, s66, 144640\n"
    "ds_read_b64_tr_b16 a[254:255], v20 offset:52224\n"
    "buffer_load_dwordx4 v2, s[40:43], s58 offen offset:2048 lds\n"
    "v_mfma_f32_16x16x32_bf16 v[188:191], a[0:3], a[188:191], v[188:191]\n"
    "s_add_u32 s61, s93, 57344\n"
    "v_mfma_f32_16x16x32_bf16 v[192:195], a[0:3], a[48:51], v[192:195]\n"
    "v_mfma_f32_16x16x32_bf16 v[196:199], a[0:3], a[120:123], v[196:199]\n"
    "s_waitcnt lgkmcnt(6)\n"
    "ds_read_b64_tr_b16 a[0:1], v21 offset:44032\n"
    "v_mfma_f32_16x16x32_bf16 v[200:203], a[4:7], a[116:119], v[200:203]\n"
    "ds_read_b64_tr_b16 a[2:3], v21 offset:52224\n"
    "v_mfma_f32_16x16x32_bf16 v[204:207], a[4:7], a[188:191], v[204:207]\n"
    "buffer_load_dwordx4 v2, s[40:43], s59 offen offset:3072 lds\n"
    "v_mfma_f32_16x16x32_bf16 v[208:211], a[4:7], a[48:51], v[208:211]\n"
    "v_mfma_f32_16x16x32_bf16 v[212:215], a[4:7], a[120:123], v[212:215]\n"
    "s_waitcnt lgkmcnt(6)\n"
    "v_mfma_f32_16x16x32_bf16 v[184:187], a[8:11], a[192:195], v[184:187]\n"
    "v_mfma_f32_16x16x32_bf16 v[188:191], a[8:11], a[52:55], v[188:191]\n"
    "buffer_load_dwordx4 a[148:151], v40, s[48:51], s61 offen offset:0 sc1\n"
    "v_mfma_f32_16x16x32_bf16 v[192:195], a[8:11], a[124:127], v[192:195]\n"
    "v_mfma_f32_16x16x32_bf16 v[196:199], a[8:11], a[196:199], v[196:199]\n"
    "s_waitcnt lgkmcnt(4)\n"
    "v_mfma_f32_16x16x32_bf16 v[200:203], a[12:15], a[192:195], v[200:203]\n"
    "buffer_load_dwordx4 a[220:223], v40, s[48:51], s61 offen offset:1024 sc1\n"
    "v_mfma_f32_16x16x32_bf16 v[204:207], a[12:15], a[52:55], v[204:207]\n"
    "v_mfma_f32_16x16x32_bf16 v[208:211], a[12:15], a[124:127], v[208:211]\n"
    "v_mfma_f32_16x16x32_bf16 v[212:215], a[12:15], a[196:199], v[212:215]\n"
    "s_waitcnt lgkmcnt(2)\n"
    "buffer_load_dwordx4 a[80:83], v41, s[48:51], s61 offen offset:0 sc1\n"
    "v_mfma_f32_16x16x32_bf16 v[184:187], a[252:255], a[56:59], v[184:187]\n"
    "v_mfma_f32_16x16x32_bf16 v[188:191], a[252:255], a[128:131], v[188:191]\n"
    "buffer_load_dwordx4 a[152:155], v41, s[48:51], s61 offen offset:1024 sc1\n"
    "v_mfma_f32_16x16x32_bf16 v[192:195], a[252:255], a[200:203], v[192:195]\n"
    "s_add_u32 s61, s93, 65536\n"
    "v_mfma_f32_16x16x32_bf16 v[196:199], a[252:255], a[60:63], v[196:199]\n"
    "s_waitcnt lgkmcnt(0)\n"
    "s_barrier\n"
    "ds_read_b64_tr_b16 a[4:5], v22 offset:0\n"
    "ds_read_b64_tr_b16 a[6:7], v22 offset:8192\n"
    "v_mfma_f32_16x16x32_bf16 v[200:203], a[0:3], a[56:59], v[200:203]\n"
    "s_add_u32 m0, s56, 0\n"
    "ds_read_b64_tr_b16 a[8:9], v23 offset:0\n"
    "ds_read_b64_tr_b16 a[10:11], v23 offset:8192\n"
    "v_mfma_f32_16x16x32_bf16 v[204:207], a[0:3], a[128:131], v[204:207]\n"
    "s_add_u32 s58, s66, 184576\n"
    "ds_read_b64_tr_b16 a[12:13], v22 offset:1024\n"
    "buffer_load_dwordx4 a[224:227], v40, s[48:51], s61 offen offset:0 sc1\n"
    "v_mfma_f32_16x16x32_bf16 v[208:211], a[0:3], a[200:203], v[208:211]\n"
    "s_sub_u32 s59, s90, 768\n"
    "ds_read_b64_tr_b16 a[14:15], v22 offset:9216\n"
    "buffer_load_dwordx4 v2, s[40:43], s58 offen offset:0 lds\n"
    "v_mfma_f32_16x16x32_bf16 v[212:215], a[0:3], a[60:63], v[212:215]\n"
    "s_waitcnt lgkmcnt(4)\n"
    "ds_read_b64_tr_b16 a[252:253], v23 offset:1024\n"
    "v_mfma_f32_16x16x32_bf16 v[184:187], a[4:7], a[132:135], v[184:187]\n"
    "ds_read_b64_tr_b16 a[254:255], v23 offset:9216\n"
    "v_mfma_f32_16x16x32_bf16 v[188:191], a[4:7], a[204:207], v[188:191]\n"
    "ds_read_b64_tr_b16 a[0:1], v22 offset:2048\n"
    "buffer_load_dwordx4 a[84:87], v40, s[48:51], s61 offen offset:1024 sc1\n"
    "buffer_load_dwordx4 a[156:159], v41, s[48:51], s61 offen offset:0 sc1\n"
    "buffer_load_dwordx4 a[228:231], v41, s[48:51], s61 offen offset:1024 sc1\n"
    "v_mfma_f32_16x16x32_bf16 v[192:195], a[4:7], a[64:67], v[192:195]\n"
    "ds_read_b64_tr_b16 a[2:3], v22 offset:10240\n"
    "v_mfma_f32_16x16x32_bf16 v[196:199], a[4:7], a[136:139], v[196:199]\n"
    "s_waitcnt lgkmcnt(6)\n"
    "ds_read_b64_tr_b16 a[4:5], v23 offset:2048\n"
    "buffer_load_dwordx4 v2, s[44:47], s59 offen offset:1024 lds\n"
    "v_mfma_f32_16x16x32_bf16 v[200:203], a[8:11], a[132:135], v[200:203]\n"
    "s_sub_u32 s58, s68, 1792\n"
    "ds_read_b64_tr_b16 a[6:7], v23 offset:10240\n"
    "v_mfma_f32_16x16x32_bf16 v[204:207], a[8:11], a[204:207], v[204:207]\n"
    "v_mfma_f32_16x16x32_bf16 v[208:211], a[8:11], a[64:67], v[208:211]\n"
    "v_mfma_f32_16x16x32_bf16 v[212:215], a[8:11], a[136:139], v[212:215]\n"
    "s_waitcnt lgkmcnt(6)\n"
    "ds_read_b64_tr_b16 a[8:9], v22 offset:3072\n"
    "v_mfma_f32_16x16x32_bf16 v[184:187], a[12:15], a[208:211], v[184:187]\n"
    "s_sub_u32 s59, s91, 2816\n"
    "ds_read_b64_tr_b16 a[10:11], v22 offset:11264\n"
    "buffer_load_dwordx4 v2, s[44:47], s58 offen offset:2048 lds\n"
    "v_mfma_f32_16x16x32_bf16 v[188:191], a[12:15], a[68:71], v[188:191]\n"
    "v_mfma_f32_16x16x32_bf16 v[192:195], a[12:15], a[140:143], v[192:195]\n"
    "v_mfma_f32_16x16x32_bf16 v[196:199], a[12:15], a[212:215], v[196:199]\n"
    "s_waitcnt lgkmcnt(6)\n"
    "ds_read_b64_tr_b16 a[12:13], v23 offset:3072\n"
    "v_mfma_f32_16x16x32_bf16 v[200:203], a[252:255], a[208:211], v[200:203]\n"
    "ds_read_b64_tr_b16 a[14:15], v23 offset:11264\n"
    "v_mfma_f32_16x16x32_bf16 v[204:207], a[252:255], a[68:71], v[204:207]\n"
    "buffer_load_dwordx4 v3, s[44:47], s59 offen offset:3072 lds\n"
    "v_mfma_f32_16x16x32_bf16 v[208:211], a[252:255], a[140:143], v[208:211]\n"
    "v_mfma_f32_16x16x32_bf16 v[212:215], a[252:255], a[212:215], v[212:215]\n"
    "s_waitcnt lgkmcnt(6)\n"
    "v_mfma_f32_16x16x32_bf16 v[184:187], a[0:3], a[16:19], v[184:187]\n"
    "v_mfma_f32_16x16x32_bf16 v[188:191], a[0:3], a[20:23], v[188:191]\n"
    "v_mfma_f32_16x16x32_bf16 v[192:195], a[0:3], a[24:27], v[192:195]\n"
    "v_mfma_f32_16x16x32_bf16 v[196:199], a[0:3], a[28:31], v[196:199]\n"
    "s_waitcnt lgkmcnt(4)\n"
    "v_mfma_f32_16x16x32_bf16 v[200:203], a[4:7], a[16:19], v[200:203]\n"
    "v_mfma_f32_16x16x32_bf16 v[204:207], a[4:7], a[20:23], v[204:207]\n"
    "v_mfma_f32_16x16x32_bf16 v[208:211], a[4:7], a[24:27], v[208:211]\n"
    "v_mfma_f32_16x16x32_bf16 v[212:215], a[4:7], a[28:31], v[212:215]\n"
    "s_waitcnt lgkmcnt(2)\n"
    "v_mfma_f32_16x16x32_bf16 v[184:187], a[8:11], a[168:171], v[184:187]\n"
    "v_mfma_f32_16x16x32_bf16 v[188:191], a[8:11], a[172:175], v[188:191]\n"
    "v_mfma_f32_16x16x32_bf16 v[192:195], a[8:11], a[176:179], v[192:195]\n"
    "v_mfma_f32_16x16x32_bf16 v[196:199], a[8:11], a[180:183], v[196:199]\n"
    "s_waitcnt vmcnt(26) lgkmcnt(0)\n"
    "s_barrier\n"
    "ds_read_b64_tr_b16 a[252:253], v22 offset:16384\n"
    "ds_read_b64_tr_b16 a[0:1], v23 offset:16384\n"
    "v_mfma_f32_16x16x32_bf16 v[200:203], a[12:15], a[168:171], v[200:203]\n"
    "s_add_u32 m0, s56, 16384\n"
    "ds_read_b64_tr_b16 a[4:5], v22 offset:17408\n"
    "s_add_u32 s58, s66, 221440\n"
    "v_mfma_f32_16x16x32_bf16 v[204:207], a[12:15], a[172:175], v[204:207]\n"
    "ds_read_b64_tr_b16 a[6:7], v22 offset:25600\n"
    "v_mfma_f32_16x16x32_bf16 v[208:211], a[12:15], a[176:179], v[208:211]\n"
    "s_add_u32 s59, s66, 257280\n"
    "ds_read_b64_tr_b16 a[8:9], v23 offset:17408\n"
    "buffer_load_dwordx4 v2, s[40:43], s58 offen offset:0 lds\n"
    "v_mfma_f32_16x16x32_bf16 v[212:215], a[12:15], a[180:183], v[212:215]\n"
    "s_waitcnt lgkmcnt(4)\n"
    "ds_read_b64_tr_b16 a[10:11], v23 offset:25600\n"
    "v_mfma_f32_16x16x16_bf16 v[184:187], a[252:253], a[32:33], v[184:187]\n"
    "ds_read_b64_tr_b16 a[12:13], v22 offset:18432\n"
    "v_mfma_f32_16x16x16_bf16 v[188:191], a[252:253], a[34:35], v[188:191]\n"
    "ds_read_b64_tr_b16 a[14:15], v22 offset:26624\n"
    "v_mfma_f32_16x16x16_bf16 v[192:195], a[252:253], a[36:37], v[192:195]\n"
    "v_mfma_f32_16x16x16_bf16 v[196:199], a[252:253], a[38:39], v[196:199]\n"
    "s_waitcnt lgkmcnt(6)\n"
    "ds_read_b64_tr_b16 a[252:253], v23 offset:18432\n"
    "buffer_load_dwordx4 v2, s[40:43], s59 offen offset:1024 lds\n"
    "v_mfma_f32_16x16x16_bf16 v[200:203], a[0:1], a[32:33], v[200:203]\n"
    "s_sub_u32 s58, s67, 1792\n"
    "ds_read_b64_tr_b16 a[254:255], v23 offset:26624\n"
    "v_mfma_f32_16x16x16_bf16 v[204:207], a[0:1], a[34:35], v[204:207]\n"
    "v_mfma_f32_16x16x16_bf16 v[208:211], a[0:1], a[36:37], v[208:211]\n"
    "v_mfma_f32_16x16x16_bf16 v[212:215], a[0:1], a[38:39], v[212:215]\n"
    "s_waitcnt lgkmcnt(6)\n"
    "ds_read_b64_tr_b16 a[0:1], v22 offset:19456\n"
    "v_mfma_f32_16x16x32_bf16 v[216:219], a[4:7], a[40:43], v[216:219]\n"
    "s_sub_u32 s59, s66, 2688\n"
    "ds_read_b64_tr_b16 a[2:3], v22 offset:27648\n"
    "buffer_load_dwordx4 v2, s[40:43], s58 offen offset:2048 lds\n"
    "v_mfma_f32_16x16x32_bf16 v[220:223], a[4:7], a[112:115], v[220:223]\n"
    "v_mfma_f32_16x16x32_bf16 v[224:227], a[4:7], a[184:187], v[224:227]\n"
    "v_mfma_f32_16x16x32_bf16 v[228:231], a[4:7], a[44:47], v[228:231]\n"
    "s_waitcnt lgkmcnt(6)\n"
    "ds_read_b64_tr_b16 a[4:5], v23 offset:19456\n"
    "v_mfma_f32_16x16x32_bf16 v[232:235], a[8:11], a[40:43], v[232:235]\n"
    "ds_read_b64_tr_b16 a[6:7], v23 offset:27648\n"
    "v_mfma_f32_16x16x32_bf16 v[236:239], a[8:11], a[112:115], v[236:239]\n"
    "buffer_load_dwordx4 v2, s[40:43], s59 offen offset:3072 lds\n"
    "v_mfma_f32_16x16x32_bf16 v[240:243], a[8:11], a[184:187], v[240:243]\n"
    "v_mfma_f32_16x16x32_bf16 v[244:247], a[8:11], a[44:47], v[244:247]\n"
    "s_waitcnt lgkmcnt(6)\n"
    "v_mfma_f32_16x16x32_bf16 v[216:219], a[12:15], a[116:119], v[216:219]\n"
    "v_mfma_f32_16x16x32_bf16 v[220:223], a[12:15], a[188:191], v[220:223]\n"
    "v_mfma_f32_16x16x32_bf16 v[224:227], a[12:15], a[48:51], v[224:227]\n"
    "s_add_u32 s89, s27, 6\n"
    "v_mov_b32 v38, 0x80000000\n"
    "v_writelane_b32 v38, s26, 0\n"
    "v_mfma_f32_16x16x32_bf16 v[228:231], a[12:15], a[120:123], v[228:231]\n"
    "s_waitcnt lgkmcnt(4)\n"
    "v_mov_b32 v39, s89\n"
    "v_mfma_f32_16x16x32_bf16 v[232:235], a[252:255], a[116:119], v[232:235]\n"
    "v_mfma_f32_16x16x32_bf16 v[236:239], a[252:255], a[188:191], v[236:239]\n"
    "v_mfma_f32_16x16x32_bf16 v[240:243], a[252:255], a[48:51], v[240:243]\n"
    "v_mfma_f32_16x16x32_bf16 v[244:247], a[252:255], a[120:123], v[244:247]\n"
    "s_waitcnt vmcnt(23) lgkmcnt(2)\n"
    "v_mfma_f32_16x16x32_bf16 a[88:91], a[0:3], a[72:75], a[88:91]\n"
    "s_waitcnt vmcnt(22)\n"
    "v_mfma_f32_16x16x32_bf16 a[92:95], a[0:3], a[144:147], a[92:95]\n"
    "s_waitcnt vmcnt(21)\n"
    "v_mfma_f32_16x16x32_bf16 a[96:99], a[0:3], a[216:219], a[96:99]\n"
    "s_waitcnt vmcnt(18)\n"
    "buffer_store_dword v39, v38, s[48:51], 0 offen offset:0 sc1\n"
    "v_mfma_f32_16x16x32_bf16 a[100:103], a[0:3], a[76:79], a[100:103]\n"
    "s_waitcnt lgkmcnt(0)\n"
    "s_barrier\n"
    "ds_read_b64_tr_b16 a[8:9], v22 offset:32768\n"
    "ds_read_b64_tr_b16 a[10:11], v22 offset:40960\n"
    "v_mfma_f32_16x16x32_bf16 a[104:107], a[4:7], a[72:75], a[104:107]\n"
    "s_add_u32 m0, s56, 32768\n"
    "ds_read_b64_tr_b16 a[12:13], v23 offset:32768\n"
    "ds_read_b64_tr_b16 a[14:15], v23 offset:40960\n"
    "v_mfma_f32_16x16x32_bf16 a[108:111], a[4:7], a[144:147], a[108:111]\n"
    "s_add_u32 s58, s66, 37248\n"
    "ds_read_b64_tr_b16 a[252:253], v22 offset:33792\n"
    "v_mfma_f32_16x16x32_bf16 a[160:163], a[4:7], a[216:219], a[160:163]\n"
    "s_add_u32 s59, s66, 73088\n"
    "ds_read_b64_tr_b16 a[254:255], v22 offset:41984\n"
    "buffer_load_dwordx4 v2, s[40:43], s58 offen offset:0 lds\n"
    "v_mfma_f32_16x16x32_bf16 a[164:167], a[4:7], a[76:79], a[164:167]\n"
    "s_waitcnt vmcnt(17) lgkmcnt(4)\n"
    "ds_read_b64_tr_b16 a[0:1], v23 offset:33792\n"
    "v_mfma_f32_16x16x32_bf16 a[88:91], a[8:11], a[148:151], a[88:91]\n"
    "s_waitcnt vmcnt(16)\n"
    "ds_read_b64_tr_b16 a[2:3], v23 offset:41984\n"
    "L_hop12:\n"
    "s_cbranch_execz L_back0\n"
    "v_mfma_f32_16x16x32_bf16 a[92:95], a[8:11], a[220:223], a[92:95]\n"
    "s_waitcnt vmcnt(15)\n"
    "ds_read_b64_tr_b16 a[4:5], v22 offset:34816\n"
    "v_mfma_f32_16x16x32_bf16 a[96:99], a[8:11], a[80:83], a[96:99]\n"
    "s_waitcnt vmcnt(14)\n"
    "ds_read_b64_tr_b16 a[6:7], v22 offset:43008\n"
    "v_mfma_f32_16x16x32_bf16 a[100:103], a[8:11], a[152:155], a[100:103]\n"
    "s_waitcnt lgkmcnt(6)\n"
    "ds_read_b64_tr_b16 a[8:9], v23 offset:34816\n"
    "buffer_load_dwordx4 v2, s[40:43], s59 offen offset:1024 lds\n"
    "v_mfma_f32_16x16x32_bf16 a[104:107], a[12:15], a[148:151], a[104:107]\n"
    "s_add_u32 s58, s66, 108928\n"
    "ds_read_b64_tr_b16 a[10:11], v23 offset:43008\n"
    "v_mfma_f32_16x16x32_bf16 a[108:111], a[12:15], a[220:223], a[108:111]\n"
    "v_mfma_f32_16x16x32_bf16 a[160:163], a[12:15], a[80:83], a[160:163]\n"
    "v_mfma_f32_16x16x32_bf16 a[164:167], a[12:15], a[152:155], a[164:167]\n"
    "s_waitcnt vmcnt(14) lgkmcnt(6)\n"
    "ds_read_b64_tr_b16 a[12:13], v22 offset:35840\n"
    "v_mfma_f32_16x16x32_bf16 a[88:91], a[252:255], a[224:227], a[88:91]\n"
    "s_waitcnt vmcnt(12)\n"
    "s_add_u32 s59, s66, 144768\n"
    "ds_read_b64_tr_b16 a[14:15], v22 offset:44032\n"
    "buffer_load_dwordx4 v2, s[40:43], s58 offen offset:2048 lds\n"
    "v_mfma_f32_16x16x32_bf16 a[92:95], a[252:255], a[84:87], a[92:95]\n"
    "s_waitcnt vmcnt(12)\n"
    "v_mfma_f32_16x16x32_bf16 a[96:99], a[252:255], a[156:159], a[96:99]\n"
    "s_waitcnt vmcnt(11)\n"
    "v_mfma_f32_16x16x32_bf16 a[100:103], a[252:255], a[228:231], a[100:103]\n"
    "s_waitcnt lgkmcnt(6)\n"
    "ds_read_b64_tr_b16 a[252:253], v23 offset:35840\n"
    "v_mfma_f32_16x16x32_bf16 a[104:107], a[0:3], a[224:227], a[104:107]\n"
    "ds_read_b64_tr_b16 a[254:255], v23 offset:44032\n"
    "v_mfma_f32_16x16x32_bf16 a[108:111], a[0:3], a[84:87], a[108:111]\n"
    "buffer_load_dwordx4 v2, s[40:43], s59 offen offset:3072 lds\n"
    "v_mfma_f32_16x16x32_bf16 a[160:163], a[0:3], a[156:159], a[160:163]\n"
    "v_mfma_f32_16x16x32_bf16 a[164:167], a[0:3], a[228:231], a[164:167]\n"
    "s_waitcnt lgkmcnt(6)\n"
    "v_mfma_f32_16x16x32_bf16 v[184:187], a[4:7], a[72:75], v[184:187]\n"
    "v_mfma_f32_16x16x32_bf16 v[188:191], a[4:7], a[144:147], v[188:191]\n"
    "v_mfma_f32_16x16x32_bf16 v[192:195], a[4:7], a[216:219], v[192:195]\n"
    "v_accvgpr_read_b32 v112, a88\n"
    "v_accvgpr_read_b32 v113, a89\n"
    "v_mfma_f32_16x16x32_bf16 v[196:199], a[4:7], a[76:79], v[196:199]\n"
    "s_waitcnt lgkmcnt(4)\n"
    "v_accvgpr_read_b32 v114, a90\n"
    "v_accvgpr_read_b32 v115, a91\n"
    "v_mfma_f32_16x16x32_bf16 v[200:203], a[8:11], a[72:75], v[200:203]\n"
    "v_accvgpr_read_b32 v116, a104\n"
    "v_accvgpr_read_b32 v117, a105\n"
    "v_mfma_f32_16x16x32_bf16 v[204:207], a[8:11], a[144:147], v[204:207]\n"
    "v_accvgpr_read_b32 v118, a106\n"
    "v_accvgpr_read_b32 v119, a107\n"
    "v_mfma_f32_16x16x32_bf16 v[208:211], a[8:11], a[216:219], v[208:211]\n"
    "v_cvt_pk_bf16_f32 v112, v112, v113\n"
    "v_cvt_pk_bf16_f32 v113, v114, v115\n"
    "v_mfma_f32_16x16x32_bf16 v[212:215], a[8:11], a[76:79], v[212:215]\n"
    "s_waitcnt lgkmcnt(2)\n"
    "v_cvt_pk_bf16_f32 v114, v116, v117\n"
    "v_cvt_pk_bf16_f32 v115, v118, v119\n"
    "v_mfma_f32_16x16x32_bf16 v[184:187], a[12:15], a[148:151], v[184:187]\n"
    "s_add_u32 s70, s69, 0\n"
    "buffer_store_dwordx4 v[112:115], v27, s[48:51], s70 offen offset:0 sc1\n"
    "v_accvgpr_read_b32 v120, a92\n"
    "v_mfma_f32_16x16x32_bf16 v[188:191], a[12:15], a[220:223], v[188:191]\n"
    "v_accvgpr_read_b32 v121, a93\n"
    "v_accvgpr_read_b32 v122, a94\n"
    "v_mfma_f32_16x16x32_bf16 v[192:195], a[12:15], a[80:83], v[192:195]\n"
    "v_accvgpr_read_b32 v123, a95\n"
    "v_accvgpr_read_b32 v124, a108\n"
    "v_mfma_f32_16x16x32_bf16 v[196:199], a[12:15], a[152:155], v[196:199]\n"
    "s_waitcnt lgkmcnt(0)\n"
    "s_barrier\n"
    "ds_read_b64_tr_b16 a[0:1], v22 offset:49152\n"
    "ds_read_b64_tr_b16 a[2:3], v22 offset:57344\n"
    "v_mfma_f32_16x16x32_bf16 v[200:203], a[252:255], a[148:151], v[200:203]\n"
    "s_add_u32 m0, s56, 49152\n"
    "ds_read_b64_tr_b16 a[4:5], v23 offset:49152\n"
    "ds_read_b64_tr_b16 a[6:7], v23 offset:57344\n"
    "v_mfma_f32_16x16x32_bf16 v[204:207], a[252:255], a[220:223], v[204:207]\n"
    "s_add_u32 s58, s66, 184704\n"
    "ds_read_b64_tr_b16 a[8:9], v22 offset:50176\n"
    "v_accvgpr_read_b32 v125, a109\n"
    "v_mfma_f32_16x16x32_bf16 v[208:211], a[252:255], a[80:83], v[208:211]\n"
    "s_sub_u32 s59, s90, 640\n"
    "ds_read_b64_tr_b16 a[10:11], v22 offset:58368\n"
    "buffer_load_dwordx4 v2, s[40:43], s58 offen offset:0 lds\n"
    "v_mfma_f32_16x16x32_bf16 v[212:215], a[252:255], a[152:155], v[212:215]\n"
    "s_waitcnt lgkmcnt(4)\n"
    "ds_read_b64_tr_b16 a[12:13], v23 offset:50176\n"
    "v_accvgpr_read_b32 v126, a110\n"
    "v_mfma_f32_16x16x32_bf16 v[184:187], a[0:3], a[224:227], v[184:187]\n"
    "ds_read_b64_tr_b16 a[14:15], v23 offset:58368\n"
    "v_accvgpr_read_b32 v127, a111\n"
    "v_mfma_f32_16x16x32_bf16 v[188:191], a[0:3], a[84:87], v[188:191]\n"
    "ds_read_b64_tr_b16 a[252:253], v22 offset:51200\n"
    "v_cvt_pk_bf16_f32 v120, v120, v121\n"
    "v_mfma_f32_16x16x32_bf16 v[192:195], a[0:3], a[156:159], v[192:195]\n"
    "ds_read_b64_tr_b16 a[254:255], v22 offset:59392\n"
    "v_cvt_pk_bf16_f32 v121, v122, v123\n"
    "v_mfma_f32_16x16x32_bf16 v[196:199], a[0:3], a[228:231], v[196:199]\n"
    "s_waitcnt lgkmcnt(6)\n"
    "ds_read_b64_tr_b16 a[0:1], v23 offset:51200\n"
    "buffer_load_dwordx4 v2, s[44:47], s59 offen offset:1024 lds\n"
    "v_mfma_f32_16x16x32_bf16 v[200:203], a[4:7], a[224:227], v[200:203]\n"
    "s_sub_u32 s58, s68, 1664\n"
    "ds_read_b64_tr_b16 a[2:3], v23 offset:59392\n"
    "v_cvt_pk_bf16_f32 v122, v124, v125\n"
    "v_mfma_f32_16x16x32_bf16 v[204:207], a[4:7], a[84:87], v[204:207]\n"
    "v_cvt_pk_bf16_f32 v123, v126, v127\n"
    "buffer_store_dwordx4 v[120:123], v27, s[48:51], s70 offen offset:1024 sc1\n"
    "v_mfma_f32_16x16x32_bf16 v[208:211], a[4:7], a[156:159], v[208:211]\n"
    "v_accvgpr_read_b32 v128, a96\n"
    "v_accvgpr_read_b32 v129, a97\n"
    "v_mfma_f32_16x16x32_bf16 v[212:215], a[4:7], a[228:231], v[212:215]\n"
    "s_waitcnt lgkmcnt(6)\n"
    "ds_read_b64_tr_b16 a[4:5], v22 offset:52224\n"
    "v_accvgpr_read_b32 v130, a98\n"
    "v_mfma_f32_16x16x32_bf16 v[216:219], a[8:11], a[192:195], v[216:219]\n"
    "s_sub_u32 s59, s91, 2688\n"
    "ds_read_b64_tr_b16 a[6:7], v22 offset:60416\n"
    "buffer_load_dwordx4 v2, s[44:47], s58 offen offset:2048 lds\n"
    "v_mfma_f32_16x16x32_bf16 v[220:223], a[8:11], a[52:55], v[220:223]\n"
    "v_accvgpr_read_b32 v131, a99\n"
    "v_accvgpr_read_b32 v132, a160\n"
    "v_mfma_f32_16x16x32_bf16 v[224:227], a[8:11], a[124:127], v[224:227]\n"
    "v_accvgpr_read_b32 v133, a161\n"
    "v_accvgpr_read_b32 v134, a162\n"
    "v_mfma_f32_16x16x32_bf16 v[228:231], a[8:11], a[196:199], v[228:231]\n"
    "s_waitcnt lgkmcnt(6)\n"
    "ds_read_b64_tr_b16 a[8:9], v23 offset:52224\n"
    "v_accvgpr_read_b32 v135, a163\n"
    "v_mfma_f32_16x16x32_bf16 v[232:235], a[12:15], a[192:195], v[232:235]\n"
    "ds_read_b64_tr_b16 a[10:11], v23 offset:60416\n"
    "v_cvt_pk_bf16_f32 v128, v128, v129\n"
    "v_mfma_f32_16x16x32_bf16 v[236:239], a[12:15], a[52:55], v[236:239]\n"
    "buffer_load_dwordx4 v3, s[44:47], s59 offen offset:3072 lds\n"
    "v_cvt_pk_bf16_f32 v129, v130, v131\n"
    "v_mfma_f32_16x16x32_bf16 v[240:243], a[12:15], a[124:127], v[240:243]\n"
    "v_cvt_pk_bf16_f32 v130, v132, v133\n"
    "v_cvt_pk_bf16_f32 v131, v134, v135\n"
    "v_mfma_f32_16x16x32_bf16 v[244:247], a[12:15], a[196:199], v[244:247]\n"
    "s_waitcnt lgkmcnt(6)\n"
    "v_mfma_f32_16x16x32_bf16 v[216:219], a[252:255], a[56:59], v[216:219]\n"
    "v_mfma_f32_16x16x32_bf16 v[220:223], a[252:255], a[128:131], v[220:223]\n"
    "v_mfma_f32_16x16x32_bf16 v[224:227], a[252:255], a[200:203], v[224:227]\n"
    "v_mfma_f32_16x16x32_bf16 v[228:231], a[252:255], a[60:63], v[228:231]\n"
    "s_waitcnt lgkmcnt(4)\n"
    "buffer_store_dwordx4 v[128:131], v27, s[48:51], s70 offen offset:2048 sc1\n"
    "v_accvgpr_read_b32 v136, a100\n"
    "v_mfma_f32_16x16x32_bf16 v[232:235], a[0:3], a[56:59], v[232:235]\n"
    "v_accvgpr_read_b32 v137, a101\n"
    "v_accvgpr_read_b32 v138, a102\n"
    "v_mfma_f32_16x16x32_bf16 v[236:239], a[0:3], a[128:131], v[236:239]\n"
    "v_accvgpr_read_b32 v139, a103\n"
    "v_accvgpr_read_b32 v140, a164\n"
    "v_mfma_f32_16x16x32_bf16 v[240:243], a[0:3], a[200:203], v[240:243]\n"
    "v_accvgpr_read_b32 v141, a165\n"
    "v_accvgpr_read_b32 v142, a166\n"
    "v_mfma_f32_16x16x32_bf16 v[244:247], a[0:3], a[60:63], v[244:247]\n"
    "s_waitcnt lgkmcnt(2)\n"
    "v_accvgpr_read_b32 v143, a167\n"
    "v_cvt_pk_bf16_f32 v136, v136, v137\n"
    "v_mfma_f32_16x16x32_bf16 v[216:219], a[4:7], a[132:135], v[216:219]\n"
    "v_cvt_pk_bf16_f32 v137, v138, v139\n"
    "v_cvt_pk_bf16_f32 v138, v140, v141\n"
    "v_mfma_f32_16x16x32_bf16 v[220:223], a[4:7], a[204:207], v[220:223]\n"
    "v_cvt_pk_bf16_f32 v139, v142, v143\n"
    "v_mfma_f32_16x16x32_bf16 v[224:227], a[4:7], a[64:67], v[224:227]\n"
    "v_mfma_f32_16x16x32_bf16 v[228:231], a[4:7], a[136:139], v[228:231]\n"
    "s_waitcnt vmcnt(16) lgkmcnt(0)\n"
    "s_barrier\n"
    "ds_read_b64_tr_b16 a[12:13], v20 offset:40960\n"
    "ds_read_b64_tr_b16 a[14:15], v20 offset:49152\n"
    "v_mfma_f32_16x16x32_bf16 v[232:235], a[8:11], a[132:135], v[232:235]\n"
    "s_add_u32 m0, s56, 65536\n"
    "ds_read_b64_tr_b16 a[252:253], v21 offset:40960\n"
    "ds_read_b64_tr_b16 a[254:255], v21 offset:49152\n"
    "v_mfma_f32_16x16x32_bf16 v[236:239], a[8:11], a[204:207], v[236:239]\n"
    "s_add_u32 s58, s66, 221568\n"
    "ds_read_b64_tr_b16 a[0:1], v20 offset:41984\n"
    "v_mfma_f32_16x16x32_bf16 v[240:243], a[8:11], a[64:67], v[240:243]\n"
    "s_add_u32 s59, s66, 257408\n"
    "ds_read_b64_tr_b16 a[2:3], v20 offset:50176\n"
    "buffer_load_dwordx4 v2, s[40:43], s58 offen offset:0 lds\n"
    "v_mfma_f32_16x16x32_bf16 v[244:247], a[8:11], a[136:139], v[244:247]\n"
    "s_waitcnt lgkmcnt(4)\n"
    "ds_read_b64_tr_b16 a[4:5], v21 offset:41984\n"
    "v_mfma_f32_16x16x32_bf16 v[216:219], a[12:15], a[208:211], v[216:219]\n"
    "ds_read_b64_tr_b16 a[6:7], v21 offset:50176\n"
    "v_mfma_f32_16x16x32_bf16 v[220:223], a[12:15], a[68:71], v[220:223]\n"
    "ds_read_b64_tr_b16 a[8:9], v20 offset:43008\n"
    "v_mfma_f32_16x16x32_bf16 v[224:227], a[12:15], a[140:143], v[224:227]\n"
    "ds_read_b64_tr_b16 a[10:11], v20 offset:51200\n"
    "buffer_store_dwordx4 v[136:139], v27, s[48:51], s70 offen offset:3072 sc1\n"
    "v_mfma_f32_16x16x32_bf16 v[228:231], a[12:15], a[212:215], v[228:231]\n"
    "s_waitcnt lgkmcnt(6)\n"
    "ds_read_b64_tr_b16 a[12:13], v21 offset:43008\n"
    "buffer_load_dwordx4 v2, s[40:43], s59 offen offset:1024 lds\n"
    "v_mfma_f32_16x16x32_bf16 v[232:235], a[252:255], a[208:211], v[232:235]\n"
    "s_sub_u32 s58, s67, 1664\n"
    "ds_read_b64_tr_b16 a[14:15], v21 offset:51200\n"
    "v_cvt_pk_bf16_f32 v184, v184, v185\n"
    "v_mfma_f32_16x16x32_bf16 v[236:239], a[252:255], a[68:71], v[236:239]\n"
    "v_cvt_pk_bf16_f32 v185, v186, v187\n"
    "v_cvt_pk_bf16_f32 v186, v200, v201\n"
    "v_mfma_f32_16x16x32_bf16 v[240:243], a[252:255], a[140:143], v[240:243]\n"
    "s_add_u32 s70, s69, 4096\n"
    "v_cvt_pk_bf16_f32 v187, v202, v203\n"
    "buffer_load_dwordx2 v[28:29], v7, s[28:31], s34 offen offset:0\n"
    "v_mfma_f32_16x16x32_bf16 v[244:247], a[252:255], a[212:215], v[244:247]\n"
    "s_waitcnt lgkmcnt(6)\n"
    "ds_read_b64_tr_b16 a[252:253], v20 offset:44032\n"
    "v_mfma_f32_16x16x32_bf16 v[216:219], a[0:3], a[16:19], v[216:219]\n"
    "buffer_load_dwordx4 v2, s[40:43], s58 offen offset:2048 lds\n"
    "v_mfma_f32_16x16x32_bf16 v[220:223], a[0:3], a[20:23], v[220:223]\n"
    "v_mfma_f32_16x16x32_bf16 v[224:227], a[0:3], a[24:27], v[224:227]\n"
    "v_mfma_f32_16x16x32_bf16 v[228:231], a[0:3], a[28:31], v[228:231]\n"
    "s_waitcnt lgkmcnt(5)\n"
    "ds_read_b64_tr_b16 a[0:1], v21 offset:44032\n"
    "v_mfma_f32_16x16x32_bf16 v[232:235], a[4:7], a[16:19], v[232:235]\n"
    "v_mfma_f32_16x16x32_bf16 v[236:239], a[4:7], a[20:23], v[236:239]\n"
    "buffer_load_dwordx2 v[30:31], v7, s[28:31], s34 offen offset:128\n"
    "v_mfma_f32_16x16x32_bf16 v[240:243], a[4:7], a[24:27], v[240:243]\n"
    "v_mfma_f32_16x16x32_bf16 v[244:247], a[4:7], a[28:31], v[244:247]\n"
    "s_waitcnt lgkmcnt(4)\n"
    "v_mfma_f32_16x16x32_bf16 v[216:219], a[8:11], a[168:171], v[216:219]\n"
    "buffer_load_dwordx2 v[32:33], v7, s[28:31], s34 offen offset:256\n"
    "v_mfma_f32_16x16x32_bf16 v[220:223], a[8:11], a[172:175], v[220:223]\n"
    "v_mfma_f32_16x16x32_bf16 v[224:227], a[8:11], a[176:179], v[224:227]\n"
    "v_mfma_f32_16x16x32_bf16 v[228:231], a[8:11], a[180:183], v[228:231]\n"
    "s_waitcnt lgkmcnt(2)\n"
    "buffer_store_dwordx4 v[184:187], v27, s[48:51], s70 offen offset:0 sc1\n"
    "v_cvt_pk_bf16_f32 v188, v188, v189\n"
    "v_mfma_f32_16x16x32_bf16 v[232:235], a[12:15], a[168:171], v[232:235]\n"
    "v_cvt_pk_bf16_f32 v189, v190, v191\n"
    "v_cvt_pk_bf16_f32 v190, v204, v205\n"
    "v_mfma_f32_16x16x32_bf16 v[236:239], a[12:15], a[172:175], v[236:239]\n"
    "v_cvt_pk_bf16_f32 v191, v206, v207\n"
    "v_mfma_f32_16x16x32_bf16 v[240:243], a[12:15], a[176:179], v[240:243]\n"
    "v_mfma_f32_16x16x32_bf16 v[244:247], a[12:15], a[180:183], v[244:247]\n"
    "s_waitcnt lgkmcnt(1)\n"
    "buffer_load_dwordx2 v[34:35], v7, s[28:31], s34 offen offset:384\n"
    "buffer_load_dword v10, v17, s[48:51], 0 offen offset:256 sc1\n"
    "v_mfma_f32_16x16x16_bf16 v[216:219], a[252:253], a[32:33], v[216:219]\n"
    "v_mfma_f32_16x16x16_bf16 v[220:223], a[252:253], a[34:35], v[220:223]\n"
    "v_mfma_f32_16x16x16_bf16 v[224:227], a[252:253], a[36:37], v[224:227]\n"
    "v_mfma_f32_16x16x16_bf16 v[228:231], a[252:253], a[38:39], v[228:231]\n"
    "s_waitcnt vmcnt(22) lgkmcnt(0)\n"
    "s_barrier\n"
    "ds_read_b64_tr_b16 a[4:5], v22 offset:0\n"
    "ds_read_b64_tr_b16 a[6:7], v22 offset:8192\n"
    "v_mfma_f32_16x16x16_bf16 v[232:235], a[0:1], a[32:33], v[232:235]\n"
    "ds_read_b64_tr_b16 a[8:9], v23 offset:0\n"
    "ds_read_b64_tr_b16 a[10:11], v23 offset:8192\n"
    "v_mfma_f32_16x16x16_bf16 v[236:239], a[0:1], a[34:35], v[236:239]\n"
    "ds_read_b64_tr_b16 a[12:13], v22 offset:1024\n"
    "v_mfma_f32_16x16x16_bf16 v[240:243], a[0:1], a[36:37], v[240:243]\n"
    "ds_read_b64_tr_b16 a[14:15], v22 offset:9216\n"
    "v_mfma_f32_16x16x16_bf16 v[244:247], a[0:1], a[38:39], v[244:247]\n"
    "s_waitcnt lgkmcnt(4)\n"
    "ds_read_b64_tr_b16 a[252:253], v23 offset:1024\n"
    "v_mfma_f32_16x16x32_bf16 v[216:219], a[4:7], a[72:75], v[216:219]\n"
    "ds_read_b64_tr_b16 a[254:255], v23 offset:9216\n"
    "buffer_store_dwordx4 v[188:191], v27, s[48:51], s70 offen offset:1024 sc1\n"
    "v_mfma_f32_16x16x32_bf16 v[220:223], a[4:7], a[144:147], v[220:223]\n"
    "ds_read_b64_tr_b16 a[0:1], v22 offset:2048\n"
    "v_cvt_pk_bf16_f32 v192, v192, v193\n"
    "v_mfma_f32_16x16x32_bf16 v[224:227], a[4:7], a[216:219], v[224:227]\n"
    "ds_read_b64_tr_b16 a[2:3], v22 offset:10240\n"
    "v_cvt_pk_bf16_f32 v193, v194, v195\n"
    "v_mfma_f32_16x16x32_bf16 v[228:231], a[4:7], a[76:79], v[228:231]\n"
    "s_waitcnt lgkmcnt(6)\n"
    "ds_read_b64_tr_b16 a[4:5], v23 offset:2048\n"
    "v_cvt_pk_bf16_f32 v194, v208, v209\n"
    "v_mfma_f32_16x16x32_bf16 v[232:235], a[8:11], a[72:75], v[232:235]\n"
    "ds_read_b64_tr_b16 a[6:7], v23 offset:10240\n"
    "v_cvt_pk_bf16_f32 v195, v210, v211\n"
    "v_mfma_f32_16x16x32_bf16 v[236:239], a[8:11], a[144:147], v[236:239]\n"
    "v_mfma_f32_16x16x32_bf16 v[240:243], a[8:11], a[216:219], v[240:243]\n"
    "v_mfma_f32_16x16x32_bf16 v[244:247], a[8:11], a[76:79], v[244:247]\n"
    "s_waitcnt lgkmcnt(6)\n"
    "ds_read_b64_tr_b16 a[8:9], v22 offset:3072\n"
    "v_mfma_f32_16x16x32_bf16 v[216:219], a[12:15], a[148:151], v[216:219]\n"
    "ds_read_b64_tr_b16 a[10:11], v22 offset:11264\n"
    "v_mfma_f32_16x16x32_bf16 v[220:223], a[12:15], a[220:223], v[220:223]\n"
    "v_mfma_f32_16x16x32_bf16 v[224:227], a[12:15], a[80:83], v[224:227]\n"
    "v_mfma_f32_16x16x32_bf16 v[228:231], a[12:15], a[152:155], v[228:231]\n"
    "s_waitcnt lgkmcnt(6)\n"
    "ds_read_b64_tr_b16 a[12:13], v23 offset:3072\n"
    "v_mfma_f32_16x16x32_bf16 v[232:235], a[252:255], a[148:151], v[232:235]\n"
    "ds_read_b64_tr_b16 a[14:15], v23 offset:11264\n"
    "v_mfma_f32_16x16x32_bf16 v[236:239], a[252:255], a[220:223], v[236:239]\n"
    "v_mfma_f32_16x16x32_bf16 v[240:243], a[252:255], a[80:83], v[240:243]\n"
    "v_mfma_f32_16x16x32_bf16 v[244:247], a[252:255], a[152:155], v[244:247]\n"
    "s_waitcnt lgkmcnt(6)\n"
    "buffer_store_dwordx4 v[192:195], v27, s[48:51], s70 offen offset:2048 sc1\n"
    "v_cvt_pk_bf16_f32 v196, v196, v197\n"
    "v_mfma_f32_16x16x32_bf16 v[216:219], a[0:3], a[224:227], v[216:219]\n"
    "v_cvt_pk_bf16_f32 v197, v198, v199\n"
    "v_cvt_pk_bf16_f32 v198, v212, v213\n"
    "v_mfma_f32_16x16x32_bf16 v[220:223], a[0:3], a[84:87], v[220:223]\n"
    "v_cvt_pk_bf16_f32 v199, v214, v215\n"
    "v_mfma_f32_16x16x32_bf16 v[224:227], a[0:3], a[156:159], v[224:227]\n"
    "v_mfma_f32_16x16x32_bf16 v[228:231], a[0:3], a[228:231], v[228:231]\n"
    "s_waitcnt lgkmcnt(4)\n"
    "v_mfma_f32_16x16x32_bf16 v[232:235], a[4:7], a[224:227], v[232:235]\n"
    "v_mfma_f32_16x16x32_bf16 v[236:239], a[4:7], a[84:87], v[236:239]\n"
    "v_mfma_f32_16x16x32_bf16 v[240:243], a[4:7], a[156:159], v[240:243]\n"
    "v_mfma_f32_16x16x32_bf16 v[244:247], a[4:7], a[228:231], v[244:247]\n"
    "s_waitcnt lgkmcnt(2)\n"
    "v_mfma_f32_16x16x32_bf16 v[144:147], a[8:11], a[40:43], v[144:147]\n"
    "v_mfma_f32_16x16x32_bf16 v[148:151], a[8:11], a[112:115], v[148:151]\n"
    "v_mfma_f32_16x16x32_bf16 v[152:155], a[8:11], a[184:187], v[152:155]\n"
    "v_mfma_f32_16x16x32_bf16 v[156:159], a[8:11], a[44:47], v[156:159]\n"
    "s_waitcnt vmcnt(19) lgkmcnt(0)\n"
    "s_barrier\n"
    "ds_read_b64_tr_b16 a[252:253], v22 offset:16384\n"
    "ds_read_b64_tr_b16 a[254:255], v22 offset:24576\n"
    "v_mfma_f32_16x16x32_bf16 v[160:163], a[12:15], a[40:43], v[160:163]\n"
    "ds_read_b64_tr_b16 a[0:1], v23 offset:16384\n"
    "ds_read_b64_tr_b16 a[2:3], v23 offset:24576\n"
    "v_mfma_f32_16x16x32_bf16 v[164:167], a[12:15], a[112:115], v[164:167]\n"
    "ds_read_b64_tr_b16 a[4:5], v22 offset:17408\n"
    "v_mfma_f32_16x16x32_bf16 v[168:171], a[12:15], a[184:187], v[168:171]\n"
    "ds_read_b64_tr_b16 a[6:7], v22 offset:25600\n"
    "buffer_store_dwordx4 v[196:199], v27, s[48:51], s70 offen offset:3072 sc1\n"
    "v_mfma_f32_16x16x32_bf16 v[172:175], a[12:15], a[44:47], v[172:175]\n"
    "s_waitcnt lgkmcnt(4)\n"
    "ds_read_b64_tr_b16 a[8:9], v23 offset:17408\n"
    "v_cvt_pk_bf16_f32 v216, v216, v217\n"
    "v_mfma_f32_16x16x32_bf16 v[144:147], a[252:255], a[116:119], v[144:147]\n"
    "ds_read_b64_tr_b16 a[10:11], v23 offset:25600\n"
    "v_cvt_pk_bf16_f32 v217, v218, v219\n"
    "v_mfma_f32_16x16x32_bf16 v[148:151], a[252:255], a[188:191], v[148:151]\n"
    "ds_read_b64_tr_b16 a[12:13], v22 offset:18432\n"
    "v_cvt_pk_bf16_f32 v218, v232, v233\n"
    "v_mfma_f32_16x16x32_bf16 v[152:155], a[252:255], a[48:51], v[152:155]\n"
    "s_add_u32 s70, s69, 8192\n"
    "ds_read_b64_tr_b16 a[14:15], v22 offset:26624\n"
    "v_cvt_pk_bf16_f32 v219, v234, v235\n"
    "v_mfma_f32_16x16x32_bf16 v[156:159], a[252:255], a[120:123], v[156:159]\n"
    "s_waitcnt lgkmcnt(6)\n"
    "ds_read_b64_tr_b16 a[252:253], v23 offset:18432\n"
    "v_mfma_f32_16x16x32_bf16 v[160:163], a[0:3], a[116:119], v[160:163]\n"
    "ds_read_b64_tr_b16 a[254:255], v23 offset:26624\n"
    "v_mfma_f32_16x16x32_bf16 v[164:167], a[0:3], a[188:191], v[164:167]\n"
    "v_mfma_f32_16x16x32_bf16 v[168:171], a[0:3], a[48:51], v[168:171]\n"
    "v_mfma_f32_16x16x32_bf16 v[172:175], a[0:3], a[120:123], v[172:175]\n"
    "s_waitcnt lgkmcnt(6)\n"
    "ds_read_b64_tr_b16 a[0:1], v22 offset:19456\n"
    "v_mfma_f32_16x16x32_bf16 v[144:147], a[4:7], a[192:195], v[144:147]\n"
    "ds_read_b64_tr_b16 a[2:3], v22 offset:27648\n"
    "v_mfma_f32_16x16x32_bf16 v[148:151], a[4:7], a[52:55], v[148:151]\n"
    "v_mfma_f32_16x16x32_bf16 v[152:155], a[4:7], a[124:127], v[152:155]\n"
    "v_mfma_f32_16x16x32_bf16 v[156:159], a[4:7], a[196:199], v[156:159]\n"
    "s_waitcnt lgkmcnt(6)\n"
    "ds_read_b64_tr_b16 a[4:5], v23 offset:19456\n"
    "v_mfma_f32_16x16x32_bf16 v[160:163], a[8:11], a[192:195], v[160:163]\n"
    "ds_read_b64_tr_b16 a[6:7], v23 offset:27648\n"
    "v_mfma_f32_16x16x32_bf16 v[164:167], a[8:11], a[52:55], v[164:167]\n"
    "buffer_store_dwordx4 v[216:219], v27, s[48:51], s70 offen offset:0 sc1\n"
    "v_cvt_pk_bf16_f32 v220, v220, v221\n"
    "v_mfma_f32_16x16x32_bf16 v[168:171], a[8:11], a[124:127], v[168:171]\n"
    "v_cvt_pk_bf16_f32 v221, v222, v223\n"
    "v_cvt_pk_bf16_f32 v222, v236, v237\n"
    "v_mfma_f32_16x16x32_bf16 v[172:175], a[8:11], a[196:199], v[172:175]\n"
    "s_waitcnt lgkmcnt(6)\n"
    "v_cvt_pk_bf16_f32 v223, v238, v239\n"
    "v_mfma_f32_16x16x32_bf16 v[144:147], a[12:15], a[56:59], v[144:147]\n"
    "v_mfma_f32_16x16x32_bf16 v[148:151], a[12:15], a[128:131], v[148:151]\n"
    "v_mfma_f32_16x16x32_bf16 v[152:155], a[12:15], a[200:203], v[152:155]\n"
    "s_waitcnt vmcnt(4)\n"
    "v_sub_u32 v11, v10, v16\n"
    "v_subrev_u32 v11, 6, v11\n"
    "v_cmp_gt_u32 vcc, 32, v11\n"
    "s_nop 1\n"
    "s_or_b64 vcc, vcc, s[84:85]\n"
    "s_cmp_eq_u64 vcc, -1\n"
    "s_cbranch_scc1 L_ok5\n"
    "s_mov_b32 s79, 100000\n"
    "L_sp5:\n"
    "s_sleep 2\n"
    "buffer_load_dword v10, v17, s[48:51], 0 offen offset:256 sc1\n"
    "s_waitcnt vmcnt(0)\n"
    "v_sub_u32 v11, v10, v16\n"
    "v_subrev_u32 v11, 6, v11\n"
    "v_cmp_gt_u32 vcc, 32, v11\n"
    "s_nop 1\n"
    "s_or_b64 vcc, vcc, s[84:85]\n"
    "s_cmp_eq_u64 vcc, -1\n"
    "s_cbranch_scc1 L_ok5\n"
    "s_sub_u32 s79, s79, 1\n"
    "s_cmp_lg_u32 s79, 0\n"
    "s_cbranch_scc1 L_sp5\n"
    "L_ok5:\n"
    "v_mfma_f32_16x16x32_bf16 v[156:159], a[12:15], a[60:63], v[156:159]\n"
    "s_waitcnt lgkmcnt(4)\n"
    "buffer_load_dword v248, v9, s[28:31], s35 offen offset:0\n"
    "s_add_u32 s71, s33, 0\n"
    "v_mfma_f32_16x16x32_bf16 v[160:163], a[252:255], a[56:59], v[160:163]\n"
    "buffer_load_dwordx4 a[232:235], v8, s[48:51], s71 offen offset:0 sc1\n"
    "s_add_u32 s71, s33, 327680\n"
    "v_mfma_f32_16x16x32_bf16 v[164:167], a[252:255], a[128:131], v[164:167]\n"
    "buffer_load_dwordx4 a[236:239], v8, s[48:51], s71 offen offset:0 sc1\n"
    "s_add_u32 s71, s33, 655360\n"
    "v_mfma_f32_16x16x32_bf16 v[168:171], a[252:255], a[200:203], v[168:171]\n"
    "v_mfma_f32_16x16x32_bf16 v[172:175], a[252:255], a[60:63], v[172:175]\n"
    "s_waitcnt lgkmcnt(2)\n"
    "v_mfma_f32_16x16x32_bf16 v[144:147], a[0:3], a[132:135], v[144:147]\n"
    "buffer_load_dwordx4 a[240:243], v8, s[48:51], s71 offen offset:0 sc1\n"
    "s_add_u32 s71, s33, 983040\n"
    "v_mfma_f32_16x16x32_bf16 v[148:151], a[0:3], a[204:207], v[148:151]\n"
    "v_mfma_f32_16x16x32_bf16 v[152:155], a[0:3], a[64:67], v[152:155]\n"
    "v_mfma_f32_16x16x32_bf16 v[156:159], a[0:3], a[136:139], v[156:159]\n"
    "s_waitcnt lgkmcnt(0)\n"
    "s_barrier\n"
    "ds_read_b64_tr_b16 a[8:9], v22 offset:32768\n"
    "ds_read_b64_tr_b16 a[10:11], v22 offset:40960\n"
    "v_mfma_f32_16x16x32_bf16 v[160:163], a[4:7], a[132:135], v[160:163]\n"
    "ds_read_b64_tr_b16 a[12:13], v23 offset:32768\n"
    "ds_read_b64_tr_b16 a[14:15], v23 offset:40960\n"
    "v_mfma_f32_16x16x32_bf16 v[164:167], a[4:7], a[204:207], v[164:167]\n"
    "ds_read_b64_tr_b16 a[252:253], v22 offset:33792\n"
    "buffer_store_dwordx4 v[220:223], v27, s[48:51], s70 offen offset:1024 sc1\n"
    "v_mfma_f32_16x16x32_bf16 v[168:171], a[4:7], a[64:67], v[168:171]\n"
    "ds_read_b64_tr_b16 a[254:255], v22 offset:41984\n"
    "v_cvt_pk_bf16_f32 v224, v224, v225\n"
    "v_mfma_f32_16x16x32_bf16 v[172:175], a[4:7], a[136:139], v[172:175]\n"
    "s_waitcnt lgkmcnt(4)\n"
    "ds_read_b64_tr_b16 a[0:1], v23 offset:33792\n"
    "v_cvt_pk_bf16_f32 v225, v226, v227\n"
    "v_mfma_f32_16x16x32_bf16 v[144:147], a[8:11], a[208:211], v[144:147]\n"
    "ds_read_b64_tr_b16 a[2:3], v23 offset:41984\n"
    "v_cvt_pk_bf16_f32 v226, v240, v241\n"
    "v_mfma_f32_16x16x32_bf16 v[148:151], a[8:11], a[68:71], v[148:151]\n"
    "ds_read_b64_tr_b16 a[4:5], v22 offset:34816\n"
    "v_cvt_pk_bf16_f32 v227, v242, v243\n"
    "v_mfma_f32_16x16x32_bf16 v[152:155], a[8:11], a[140:143], v[152:155]\n"
    "ds_read_b64_tr_b16 a[6:7], v22 offset:43008\n"
    "buffer_load_dwordx4 a[244:247], v8, s[48:51], s71 offen offset:0 sc1\n"
    "v_mfma_f32_16x16x32_bf16 v[156:159], a[8:11], a[212:215], v[156:159]\n"
    "s_waitcnt lgkmcnt(6)\n"
    "ds_read_b64_tr_b16 a[8:9], v23 offset:34816\n"
    "v_mov_b32 v11, 0x80000000\n"
    "v_mfma_f32_16x16x32_bf16 v[160:163], a[12:15], a[208:211], v[160:163]\n"
    "ds_read_b64_tr_b16 a[10:11], v23 offset:43008\n"
    "v_cndmask_b32_e64 v17, v17, v11, s[82:83]\n"
    "v_mfma_f32_16x16x32_bf16 v[164:167], a[12:15], a[68:71], v[164:167]\n"
    "v_mfma_f32_16x16x32_bf16 v[168:171], a[12:15], a[140:143], v[168:171]\n"
    "v_mfma_f32_16x16x32_bf16 v[172:175], a[12:15], a[212:215], v[172:175]\n"
    "s_waitcnt lgkmcnt(6)\n"
    "ds_read_b64_tr_b16 a[12:13], v22 offset:35840\n"
    "v_mfma_f32_16x16x32_bf16 v[144:147], a[252:255], a[16:19], v[144:147]\n"
    "v_mfma_f32_16x16x32_bf16 v[148:151], a[252:255], a[20:23], v[148:151]\n"
    "v_mfma_f32_16x16x32_bf16 v[152:155], a[252:255], a[24:27], v[152:155]\n"
    "v_mfma_f32_16x16x32_bf16 v[156:159], a[252:255], a[28:31], v[156:159]\n"
    "s_waitcnt lgkmcnt(5)\n"
    "ds_read_b64_tr_b16 a[252:253], v23 offset:35840\n"
    "buffer_store_dwordx4 v[224:227], v27, s[48:51], s70 offen offset:2048 sc1\n"
    "v_mfma_f32_16x16x32_bf16 v[160:163], a[0:3], a[16:19], v[160:163]\n"
    "v_cvt_pk_bf16_f32 v228, v228, v229\n"
    "v_cvt_pk_bf16_f32 v229, v230, v231\n"
    "v_mfma_f32_16x16x32_bf16 v[164:167], a[0:3], a[20:23], v[164:167]\n"
    "v_cvt_pk_bf16_f32 v230, v244, v245\n"
    "v_cvt_pk_bf16_f32 v231, v246, v247\n"
    "v_mfma_f32_16x16x32_bf16 v[168:171], a[0:3], a[24:27], v[168:171]\n"
    "v_mfma_f32_16x16x32_bf16 v[172:175], a[0:3], a[28:31], v[172:175]\n"
    "s_waitcnt lgkmcnt(4)\n"
    "v_mfma_f32_16x16x32_bf16 v[144:147], a[4:7], a[168:171], v[144:147]\n"
    "v_mfma_f32_16x16x32_bf16 v[148:151], a[4:7], a[172:175], v[148:151]\n"
    "v_mfma_f32_16x16x32_bf16 v[152:155], a[4:7], a[176:179], v[152:155]\n"
    "v_mfma_f32_16x16x32_bf16 v[156:159], a[4:7], a[180:183], v[156:159]\n"
    "s_waitcnt lgkmcnt(2)\n"
    "v_mfma_f32_16x16x32_bf16 v[160:163], a[8:11], a[168:171], v[160:163]\n"
    "v_mfma_f32_16x16x32_bf16 v[164:167], a[8:11], a[172:175], v[164:167]\n"
    "v_mfma_f32_16x16x32_bf16 v[168:171], a[8:11], a[176:179], v[168:171]\n"
    "v_mfma_f32_16x16x32_bf16 v[172:175], a[8:11], a[180:183], v[172:175]\n"
    "s_waitcnt lgkmcnt(1)\n"
    "v_mfma_f32_16x16x16_bf16 v[144:147], a[12:13], a[32:33], v[144:147]\n"
    "v_mfma_f32_16x16x16_bf16 v[148:151], a[12:13], a[34:35], v[148:151]\n"
    "v_mfma_f32_16x16x16_bf16 v[152:155], a[12:13], a[36:37], v[152:155]\n"
    "buffer_store_dwordx4 v[228:231], v27, s[48:51], s70 offen offset:3072 sc1\n"
    "v_mfma_f32_16x16x16_bf16 v[156:159], a[12:13], a[38:39], v[156:159]\n"
    "s_waitcnt lgkmcnt(0)\n"
    "s_barrier\n"
    "ds_read_b64_tr_b16 a[0:1], v22 offset:49152\n"
    "ds_read_b64_tr_b16 a[2:3], v22 offset:57344\n"
    "v_mfma_f32_16x16x16_bf16 v[160:163], a[252:253], a[32:33], v[160:163]\n"
    "ds_read_b64_tr_b16 a[4:5], v23 offset:49152\n"
    "ds_read_b64_tr_b16 a[6:7], v23 offset:57344\n"
    "v_mfma_f32_16x16x16_bf16 v[164:167], a[252:253], a[34:35], v[164:167]\n"
    "ds_read_b64_tr_b16 a[8:9], v22 offset:50176\n"
    "v_mfma_f32_16x16x16_bf16 v[168:171], a[252:253], a[36:37], v[168:171]\n"
    "ds_read_b64_tr_b16 a[10:11], v22 offset:58368\n"
    "v_mfma_f32_16x16x16_bf16 v[172:175], a[252:253], a[38:39], v[172:175]\n"
    "s_waitcnt lgkmcnt(4)\n"
    "ds_read_b64_tr_b16 a[12:13], v23 offset:50176\n"
    "v_mfma_f32_16x16x32_bf16 v[144:147], a[0:3], a[72:75], v[144:147]\n"
    "ds_read_b64_tr_b16 a[14:15], v23 offset:58368\n"
    "v_mfma_f32_16x16x32_bf16 v[148:151], a[0:3], a[144:147], v[148:151]\n"
    "ds_read_b64_tr_b16 a[252:253], v22 offset:51200\n"
    "v_mfma_f32_16x16x32_bf16 v[152:155], a[0:3], a[216:219], v[152:155]\n"
    "ds_read_b64_tr_b16 a[254:255], v22 offset:59392\n"
    "v_mfma_f32_16x16x32_bf16 v[156:159], a[0:3], a[76:79], v[156:159]\n"
    "s_waitcnt lgkmcnt(6)\n"
    "ds_read_b64_tr_b16 a[0:1], v23 offset:51200\n"
    "v_mfma_f32_16x16x32_bf16 v[160:163], a[4:7], a[72:75], v[160:163]\n"
    "s_add_u32 s89, s27, 7\n"
    "ds_read_b64_tr_b16 a[2:3], v23 offset:59392\n"
    "v_mov_b32 v38, 0x80000000\n"
    "v_mfma_f32_16x16x32_bf16 v[164:167], a[4:7], a[144:147], v[164:167]\n"
    "v_writelane_b32 v38, s26, 0\n"
    "v_mov_b32 v39, s89\n"
    "v_mfma_f32_16x16x32_bf16 v[168:171], a[4:7], a[216:219], v[168:171]\n"
    "v_mfma_f32_16x16x32_bf16 v[172:175], a[4:7], a[76:79], v[172:175]\n"
    "s_waitcnt lgkmcnt(6)\n"
    "v_mfma_f32_16x16x32_bf16 v[144:147], a[8:11], a[148:151], v[144:147]\n"
    "v_mfma_f32_16x16x32_bf16 v[148:151], a[8:11], a[220:223], v[148:151]\n"
    "v_mfma_f32_16x16x32_bf16 v[152:155], a[8:11], a[80:83], v[152:155]\n"
    "v_mfma_f32_16x16x32_bf16 v[156:159], a[8:11], a[152:155], v[156:159]\n"
    "s_waitcnt lgkmcnt(4)\n"
    "v_mfma_f32_16x16x32_bf16 v[160:163], a[12:15], a[148:151], v[160:163]\n"
    "s_waitcnt vmcnt(9)\n"
    "buffer_store_dword v39, v38, s[48:51], 0 offen offset:0 sc1\n"
    "v_mfma_f32_16x16x32_bf16 v[164:167], a[12:15], a[220:223], v[164:167]\n"
    "v_mfma_f32_16x16x32_bf16 v[168:171], a[12:15], a[80:83], v[168:171]\n"
    "v_mfma_f32_16x16x32_bf16 v[172:175], a[12:15], a[152:155], v[172:175]\n"
    "s_waitcnt lgkmcnt(2)\n"
    "v_mfma_f32_16x16x32_bf16 v[144:147], a[252:255], a[224:227], v[144:147]\n"
    "v_mfma_f32_16x16x32_bf16 v[148:151], a[252:255], a[84:87], v[148:151]\n"
    "v_mfma_f32_16x16x32_bf16 v[152:155], a[252:255], a[156:159], v[152:155]\n"
    "v_mfma_f32_16x16x32_bf16 v[156:159], a[252:255], a[228:231], v[156:159]\n"
    "s_waitcnt lgkmcnt(0)\n"
    "v_mfma_f32_16x16x32_bf16 v[160:163], a[0:3], a[224:227], v[160:163]\n"
    "v_mfma_f32_16x16x32_bf16 v[164:167], a[0:3], a[84:87], v[164:167]\n"
    "v_mfma_f32_16x16x32_bf16 v[168:171], a[0:3], a[156:159], v[168:171]\n"
    "v_mfma_f32_16x16x32_bf16 v[172:175], a[0:3], a[228:231], v[172:175]\n"
    "v_cvt_pk_bf16_f32 v144, v144, v145\n"
    "v_cvt_pk_bf16_f32 v145, v146, v147\n"
    "s_nop 2\n"
    "v_cvt_pk_bf16_f32 v146, v160, v161\n"
    "v_cvt_pk_bf16_f32 v147, v162, v163\n"
    "s_add_u32 s70, s69, 12288\n"
    "buffer_store_dwordx4 v[144:147], v27, s[48:51], s70 offen offset:0 sc1\n"
    "v_cvt_pk_bf16_f32 v148, v148, v149\n"
    "v_cvt_pk_bf16_f32 v149, v150, v151\n"
    "v_cvt_pk_bf16_f32 v150, v164, v165\n"
    "v_cvt_pk_bf16_f32 v151, v166, v167\n"
    "buffer_store_dwordx4 v[148:151], v27, s[48:51], s70 offen offset:1024 sc1\n"
    "v_cvt_pk_bf16_f32 v152, v152, v153\n"
    "v_cvt_pk_bf16_f32 v153, v154, v155\n"
    "v_cvt_pk_bf16_f32 v154, v168, v169\n"
    "v_cvt_pk_bf16_f32 v155, v170, v171\n"
    "buffer_store_dwordx4 v[152:155], v27, s[48:51], s70 offen offset:2048 sc1\n"
    "v_cvt_pk_bf16_f32 v156, v156, v157\n"
    "v_cvt_pk_bf16_f32 v157, v158, v159\n"
    "v_cvt_pk_bf16_f32 v158, v172, v173\n"
    "v_cvt_pk_bf16_f32 v159, v174, v175\n"
    "buffer_store_dwordx4 v[156:159], v27, s[48:51], s70 offen offset:3072 sc1\n"
    "L_hop13:\n"
    "s_cbranch_execz L_back0\n"
    "buffer_load_dword v10, v17, s[48:51], 0 offen offset:256 sc1\n"
    "s_waitcnt vmcnt(12)\n"
    "v_mfma_f32_16x16x32_bf16 v[200:203], a[248:251], a[232:235], 0\n"
    "s_waitcnt vmcnt(11)\n"
    "v_mfma_f32_16x16x32_bf16 v[200:203], a[248:251], a[236:239], v[200:203]\n"
    "s_waitcnt vmcnt(10)\n"
    "v_mfma_f32_16x16x32_bf16 v[200:203], a[248:251], a[240:243], v[200:203]\n"
    "s_waitcnt vmcnt(8)\n"
    "v_mfma_f32_16x16x32_bf16 v[200:203], a[248:251], a[244:247], v[200:203]\n"
    "v_lshlrev_b32 v220, 16, v248\n"
    "v_and_b32 v221, s77, v248\n"
    "s_add_u32 s89, s27, 9\n"
    "v_mov_b32 v38, 0x80000000\n"
    "v_writelane_b32 v38, s26, 0\n"
    "v_mov_b32 v39, s89\n"
    "s_waitcnt vmcnt(1)\n"
    "buffer_store_dword v39, v38, s[48:51], 0 offen offset:0 sc1\n"
    "s_waitcnt vmcnt(1)\n"
    "v_sub_u32 v11, v10, v16\n"
    "v_subrev_u32 v11, 7, v11\n"
    "v_cmp_gt_u32 vcc, 32, v11\n"
    "s_nop 1\n"
    "s_or_b64 vcc, vcc, s[82:83]\n"
    "s_cmp_eq_u64 vcc, -1\n"
    "s_cbranch_scc1 L_ok6\n"
    "s_mov_b32 s79, 100000\n"
    "L_sp6:\n"
    "s_sleep 1\n"
    "buffer_load_dword v10, v17, s[48:51], 0 offen offset:256 sc1\n"
    "s_waitcnt vmcnt(0)\n"
    "v_sub_u32 v11, v10, v16\n"
    "v_subrev_u32 v11, 7, v11\n"
    "v_cmp_gt_u32 vcc, 32, v11\n"
    "s_nop 1\n"
    "s_or_b64 vcc, vcc, s[82:83]\n"
    "s_cmp_eq_u64 vcc, -1\n"
    "s_cbranch_scc1 L_ok6\n"
    "s_sub_u32 s79, s79, 1\n"
    "s_cmp_lg_u32 s79, 0\n"
    "s_cbranch_scc1 L_sp6\n"
    "L_ok6:\n"
    "s_add_u32 s71, s32, 0\n"
    "buffer_load_dwordx4 v[48:51], v6, s[48:51], s71 offen offset:0 sc1\n"
    "s_add_u32 s71, s32, 327680\n"
    "buffer_load_dwordx4 v[52:55], v6, s[48:51], s71 offen offset:0 sc1\n"
    "s_add_u32 s71, s32, 655360\n"
    "buffer_load_dwordx4 v[56:59], v6, s[48:51], s71 offen offset:0 sc1\n"
    "s_add_u32 s71, s32, 983040\n"
    "buffer_load_dwordx4 v[60:63], v6, s[48:51], s71 offen offset:0 sc1\n"
    "s_add_u32 s71, s32, 4096\n"
    "buffer_load_dwordx4 v[64:67], v6, s[48:51], s71 offen offset:0 sc1\n"
    "s_add_u32 s71, s32, 331776\n"
    "buffer_load_dwordx4 v[68:71], v6, s[48:51], s71 offen offset:0 sc1\n"
    "s_add_u32 s71, s32, 659456\n"
    "buffer_load_dwordx4 v[72:75], v6, s[48:51], s71 offen offset:0 sc1\n"
    "s_add_u32 s71, s32, 987136\n"
    "buffer_load_dwordx4 v[76:79], v6, s[48:51], s71 offen offset:0 sc1\n"
    "buffer_load_dword v10, v17, s[48:51], 0 offen offset:256 sc1\n"
    "s_waitcnt vmcnt(0)\n"
    "v_sub_u32 v11, v10, v16\n"
    "v_subrev_u32 v11, 9, v11\n"
    "v_cmp_gt_u32 vcc, 32, v11\n"
    "s_nop 1\n"
    "s_or_b64 vcc, vcc, s[82:83]\n"
    "s_cmp_eq_u64 vcc, -1\n"
    "s_cbranch_scc1 L_ok7\n"
    "s_mov_b32 s79, 100000\n"
    "L_sp7:\n"
    "s_sleep 1\n"
    "buffer_load_dword v10, v17, s[48:51], 0 offen offset:256 sc1\n"
    "s_waitcnt vmcnt(0)\n"
    "v_sub_u32 v11, v10, v16\n"
    "v_subrev_u32 v11, 9, v11\n"
    "v_cmp_gt_u32 vcc, 32, v11\n"
    "s_nop 1\n"
    "s_or_b64 vcc, vcc, s[82:83]\n"
    "s_cmp_eq_u64 vcc, -1\n"
    "s_cbranch_scc1 L_ok7\n"
    "s_sub_u32 s79, s79, 1\n"
    "s_cmp_lg_u32 s79, 0\n"
    "s_cbranch_scc1 L_sp7\n"
    "L_ok7:\n"
    "s_add_u32 s71, s32, 8192\n"
    "buffer_load_dwordx4 v[80:83], v6, s[48:51], s71 offen offset:0 sc1\n"
    "s_add_u32 s71, s32, 335872\n"
    "buffer_load_dwordx4 v[84:87], v6, s[48:51], s71 offen offset:0 sc1\n"
    "s_add_u32 s71, s32, 663552\n"
    "buffer_load_dwordx4 v[88:91], v6, s[48:51], s71 offen offset:0 sc1\n"
    "s_add_u32 s71, s32, 991232\n"
    "buffer_load_dwordx4 v[92:95], v6, s[48:51], s71 offen offset:0 sc1\n"
    "s_add_u32 s71, s32, 12288\n"
    "buffer_load_dwordx4 v[96:99], v6, s[48:51], s71 offen offset:0 sc1\n"
    "s_add_u32 s71, s32, 339968\n"
    "buffer_load_dwordx4 v[100:103], v6, s[48:51], s71 offen offset:0 sc1\n"
    "s_add_u32 s71, s32, 667648\n"
    "buffer_load_dwordx4 v[104:107], v6, s[48:51], s71 offen offset:0 sc1\n"
    "s_add_u32 s71, s32, 995328\n"
    "buffer_load_dwordx4 v[108:111], v6, s[48:51], s71 offen offset:0 sc1\n"
    "v_fmac_f32 v220, s72, v200\n"
    "v_fmac_f32 v221, s72, v201\n"
    "v_cvt_pk_bf16_f32 v248, v220, v221\n"
    "buffer_store_dword v248, v9, s[52:55], s35 offen offset:0 sc1\n"
    "v_mfma_f32_16x16x32_bf16 v[184:187], v[44:47], v[48:51], 0\n"
    "v_mfma_f32_16x16x32_bf16 v[184:187], v[44:47], v[52:55], v[184:187]\n"
    "v_mfma_f32_16x16x32_bf16 v[184:187], v[44:47], v[56:59], v[184:187]\n"
    "v_mfma_f32_16x16x32_bf16 v[184:187], v[44:47], v[60:63], v[184:187]\n"
    "v_lshlrev_b32 v204, 16, v28\n"
    "v_and_b32 v205, s77, v28\n"
    "v_lshlrev_b32 v206, 16, v29\n"
    "v_and_b32 v207, s77, v29\n"
    "v_mfma_f32_16x16x32_bf16 v[188:191], v[44:47], v[64:67], 0\n"
    "v_mfma_f32_16x16x32_bf16 v[188:191], v[44:47], v[68:71], v[188:191]\n"
    "v_mfma_f32_16x16x32_bf16 v[188:191], v[44:47], v[72:75], v[188:191]\n"
    "v_mfma_f32_16x16x32_bf16 v[188:191], v[44:47], v[76:79], v[188:191]\n"
    "v_lshlrev_b32 v208, 16, v30\n"
    "v_and_b32 v209, s77, v30\n"
    "v_lshlrev_b32 v210, 16, v31\n"
    "v_and_b32 v211, s77, v31\n"
    "v_fmac_f32 v204, s72, v184\n"
    "v_fmac_f32 v205, s72, v185\n"
    "v_fmac_f32 v206, s72, v186\n"
    "v_fmac_f32 v207, s72, v187\n"
    "v_cvt_pk_bf16_f32 v28, v204, v205\n"
    "v_cvt_pk_bf16_f32 v29, v206, v207\n"
    "buffer_store_dwordx2 v[28:29], v7, s[52:55], s34 offen offset:0 sc1\n"
    "v_fmac_f32 v208, s72, v188\n"
    "v_fmac_f32 v209, s72, v189\n"
    "v_fmac_f32 v210, s72, v190\n"
    "v_fmac_f32 v211, s72, v191\n"
    "v_cvt_pk_bf16_f32 v30, v208, v209\n"
    "v_cvt_pk_bf16_f32 v31, v210, v211\n"
    "buffer_store_dwordx2 v[30:31], v7, s[52:55], s34 offen offset:128 sc1\n"
    "s_waitcnt vmcnt(10)\n"
    "v_mfma_f32_16x16x32_bf16 v[192:195], v[44:47], v[80:83], 0\n"
    "s_waitcnt vmcnt(9)\n"
    "v_mfma_f32_16x16x32_bf16 v[192:195], v[44:47], v[84:87], v[192:195]\n"
    "s_waitcnt vmcnt(8)\n"
    "v_mfma_f32_16x16x32_bf16 v[192:195], v[44:47], v[88:91], v[192:195]\n"
    "s_waitcnt vmcnt(7)\n"
    "v_mfma_f32_16x16x32_bf16 v[192:195], v[44:47], v[92:95], v[192:195]\n"
    "s_waitcnt vmcnt(6)\n"
    "v_lshlrev_b32 v212, 16, v32\n"
    "v_and_b32 v213, s77, v32\n"
    "v_lshlrev_b32 v214, 16, v33\n"
    "v_and_b32 v215, s77, v33\n"
    "v_mfma_f32_16x16x32_bf16 v[196:199], v[44:47], v[96:99], 0\n"
    "s_waitcnt vmcnt(5)\n"
    "v_mfma_f32_16x16x32_bf16 v[196:199], v[44:47], v[100:103], v[196:199]\n"
    "s_waitcnt vmcnt(4)\n"
    "v_mfma_f32_16x16x32_bf16 v[196:199], v[44:47], v[104:107], v[196:199]\n"
    "s_waitcnt vmcnt(3)\n"
    "v_mfma_f32_16x16x32_bf16 v[196:199], v[44:47], v[108:111], v[196:199]\n"
    "v_lshlrev_b32 v216, 16, v34\n"
    "v_and_b32 v217, s77, v34\n"
    "v_lshlrev_b32 v218, 16, v35\n"
    "v_and_b32 v219, s77, v35\n"
    "v_fmac_f32 v212, s72, v192\n"
    "v_fmac_f32 v213, s72, v193\n"
    "v_fmac_f32 v214, s72, v194\n"
    "v_fmac_f32 v215, s72, v195\n"
    "v_cvt_pk_bf16_f32 v32, v212, v213\n"
    "v_cvt_pk_bf16_f32 v33, v214, v215\n"
    "buffer_store_dwordx2 v[32:33], v7, s[52:55], s34 offen offset:256 sc1\n"
    "v_fmac_f32 v216, s72, v196\n"
    "v_fmac_f32 v217, s72, v197\n"
    "v_fmac_f32 v218, s72, v198\n"
    "v_fmac_f32 v219, s72, v199\n"
    "v_cvt_pk_bf16_f32 v34, v216, v217\n"
    "v_cvt_pk_bf16_f32 v35, v218, v219\n"
    "buffer_store_dwordx2 v[34:35], v7, s[52:55], s34 offen offset:384 sc1\n"
    "L_end:\n"
    "s_endpgm\n"
    ".p2align 12\n"
    :: "s"(__builtin_amdgcn_kernarg_segment_ptr()), "s"(blockIdx.x), "v"(threadIdx.x), "s"((unsigned)(size_t)lds)
    : "v1", "v2", "v3", "v4", "v5", "v6", "v7", "v8", "v9", "v10", "v11", "v12", "v13", "v14", "v15", "v16", "v17", "v18", "v19", "v20", "v21", "v22", "v23", "v24", "v25", "v26", "v27", "v28", "v29", "v30", "v31", "v32", "v33", "v34", "v35", "v36", "v37", "v38", "v39", "v40", "v41", "v42", "v43", "v44", "v45", "v46", "v47", "v48", "v49", "v50", "v51", "v52", "v53", "v54", "v55", "v56", "v57", "v58", "v59", "v60", "v61", "v62", "v63", "v64", "v65", "v66", "v67", "v68", "v69", "v70", "v71", "v72", "v73", "v74", "v75", "v76", "v77", "v78", "v79", "v80", "v81", "v82", "v83", "v84", "v85", "v86", "v87", "v88", "v89", "v90", "v91", "v92", "v93", "v94", "v95", "v96", "v97", "v98", "v99", "v100", "v101", "v102", "v103", "v104", "v105", "v106", "v107", "v108", "v109", "v110", "v111", "v112", "v113", "v114", "v115", "v116", "v117", "v118", "v119", "v120", "v121", "v122", "v123", "v124", "v125", "v126", "v127", "v128", "v129", "v130", "v131", "v132", "v133", "v134", "v135", "v136", "v137", "v138", "v139", "v140", "v141", "v142", "v143", "v144", "v145", "v146", "v147", "v148", "v149", "v150", "v151", "v152", "v153", "v154", "v155", "v156", "v157", "v158", "v159", "v160", "v161", "v162", "v163", "v164", "v165", "v166", "v167", "v168", "v169", "v170", "v171", "v172", "v173", "v174", "v175", "v176", "v177", "v178", "v179", "v180", "v181", "v182", "v183", "v184", "v185", "v186", "v187", "v188", "v189", "v190", "v191", "v192", "v193", "v194", "v195", "v196", "v197", "v198", "v199", "v200", "v201", "v202", "v203", "v204", "v205", "v206", "v207", "v208", "v209", "v210", "v211", "v212", "v213", "v214", "v215", "v216", "v217", "v218", "v219", "v220", "v221", "v222", "v223", "v224", "v225", "v226", "v227", "v228", "v229", "v230", "v231", "v232", "v233", "v234", "v235", "v236", "v237", "v238", "v239", "v240", "v241", "v242", "v243", "v244", "v245", "v246", "v247", "v248", "v249", "v250", "v251", "v252", "v253", "v254", "v255", "a0", "a1", "a2", "a3", "a4", "a5", "a6", "a7", "a8", "a9", "a10", "a11", "a12", "a13", "a14", "a15", "a16", "a17", "a18", "a19", "a20", "a21", "a22", "a23", "a24", "a25", "a26", "a27", "a28", "a29", "a30", "a31", "a32", "a33", "a34", "a35", "a36", "a37", "a38", "a39", "a40", "a41", "a42", "a43", "a44", "a45", "a46", "a47", "a48", "a49", "a50", "a51", "a52", "a53", "a54", "a55", "a56", "a57", "a58", "a59", "a60", "a61", "a62", "a63", "a64", "a65", "a66", "a67", "a68", "a69", "a70", "a71", "a72", "a73", "a74", "a75", "a76", "a77", "a78", "a79", "a80", "a81", "a82", "a83", "a84", "a85", "a86", "a87", "a88", "a89", "a90", "a91", "a92", "a93", "a94", "a95", "a96", "a97", "a98", "a99", "a100", "a101", "a102", "a103", "a104", "a105", "a106", "a107", "a108", "a109", "a110", "a111", "a112", "a113", "a114", "a115", "a116", "a117", "a118", "a119", "a120", "a121", "a122", "a123", "a124", "a125", "a126", "a127", "a128", "a129", "a130", "a131", "a132", "a133", "a134", "a135", "a136", "a137", "a138", "a139", "a140", "a141", "a142", "a143", "a144", "a145", "a146", "a147", "a148", "a149", "a150", "a151", "a152", "a153", "a154", "a155", "a156", "a157", "a158", "a159", "a160", "a161", "a162", "a163", "a164", "a165", "a166", "a167", "a168", "a169", "a170", "a171", "a172", "a173", "a174", "a175", "a176", "a177", "a178", "a179", "a180", "a181", "a182", "a183", "a184", "a185", "a186", "a187", "a188", "a189", "a190", "a191", "a192", "a193", "a194", "a195", "a196", "a197", "a198", "a199", "a200", "a201", "a202", "a203", "a204", "a205", "a206", "a207", "a208", "a209", "a210", "a211", "a212", "a213", "a214", "a215", "a216", "a217", "a218", "a219", "a220", "a221", "a222", "a223", "a224", "a225", "a226", "a227", "a228", "a229", "a230", "a231", "a232", "a233", "a234", "a235", "a236", "a237", "a238", "a239", "a240", "a241", "a242", "a243", "a244", "a245", "a246", "a247", "a248", "a249", "a250", "a251", "a252", "a253", "a254", "a255", "s4", "s5", "s6", "s7", "s8", "s9", "s10", "s11", "s12", "s13", "s14", "s15", "s16", "s17", "s18", "s19", "s20", "s21", "s22", "s23", "s24", "s25", "s26", "s27", "s28", "s29", "s30", "s31", "s32", "s33", "s34", "s35", "s36", "s37", "s38", "s39", "s40", "s41", "s42", "s43", "s44", "s45", "s46", "s47", "s48", "s49", "s50", "s51", "s52", "s53", "s54", "s55", "s56", "s57", "s58", "s59", "s60", "s61", "s62", "s63", "s64", "s65", "s66", "s67", "s68", "s69", "s70", "s71", "s72", "s73", "s74", "s75", "s76", "s77", "s78", "s79", "s80", "s81", "s82", "s83", "s84", "s85", "s86", "s87", "s88", "s89", "s90", "s91", "s92", "s93", "s94", "s95", "s96", "s97", "s98", "s99", "s100", "s101", "vcc", "scc", "memory");
}

extern "C" void kernel_launch(void* const* d_in, const int* in_sizes, int n_in, void* d_out, int out_size, void* d_ws, size_t ws_size,
                              hipStream_t stream) {
  fused_main<<<256, 256, 0, stream>>>(d_in[0], d_in[1], d_in[2], d_in[4], d_in[5], d_in[6], d_ws, d_out);
}
```
